```python
import torch

def _softmax(x, axis=-1):
    return torch.softmax(x, dim=axis)

def _gelu(x):
    return torch.nn.functional.gelu(x, approximate='tanh')

def _silu(x):
    return torch.nn.functional.silu(x)

def _sigmoid(x):
    return torch.sigmoid(x)

def _layernorm(x, axis=-1, eps=1e-05):
    m = x.mean(dim=axis, keepdim=True)
    v = x.var(dim=axis, keepdim=True, unbiased=False)
    return (x - m) / torch.sqrt(v + eps)

def _scatter_add(tgt, idx, src, axis=0):
    return tgt.clone().index_add_(axis, idx, src)

def _segment_sum(src, seg, n):
    out = torch.zeros((n,) + src.shape[1:], dtype=src.dtype)
    return out.index_add_(0, seg, src)

def _pad(x, pw):
    flat = tuple((v for pair in reversed(pw) for v in pair))
    return torch.nn.functional.pad(x, flat)
kernel_name = 'standalone__dit_block__diffusion'

def setup_inputs():

    _w = lambda a: (lambda t: t.to(torch.bfloat16) if t.is_floating_point() else t)(torch.as_tensor(a))
    B, N, D = (2, 1024, 768)
    F = 4 * D
    return {'x': _w((lambda *s: torch.randn(*s, dtype=torch.bfloat16) * 0.05)(B, N, D)), 'c': _w((lambda *s: torch.randn(*s, dtype=torch.bfloat16) * 0.2)(B, D)), 'wqkv': _w((lambda *s: torch.randn(*s, dtype=torch.bfloat16) * 0.05)(D, 3 * D)), 'wo': _w((lambda *s: torch.randn(*s, dtype=torch.bfloat16) * 0.05)(D, D)), 'w_mod': _w((lambda *s: torch.randn(*s, dtype=torch.bfloat16) * 0.2)(D, 6 * D)), 'w_fc1': _w((lambda *s: torch.randn(*s, dtype=torch.bfloat16) * 0.05)(D, F)), 'w_fc2': _w((lambda *s: torch.randn(*s, dtype=torch.bfloat16) * 0.05)(F, D))}

def reference(x, c, wqkv, wo, w_mod, w_fc1, w_fc2):
    B, N, D = x.shape
    H = 2
    Dh = D // H
    mod = (_silu(c) @ w_mod).reshape(B, 6, 1, D)
    shift_a, scale_a, gate_a, shift_m, scale_m, gate_m = [mod[:, i] for i in range(6)]
    h = _layernorm(x, axis=-1) * (1 + scale_a) + shift_a
    qkv = (h @ wqkv).reshape(B, N, 3, H, Dh)
    q, k, v = (qkv[:, :, 0].clone(), qkv[:, :, 1].clone(), qkv[:, :, 2].clone())
    attn = _softmax(torch.einsum('bqhd,bkhd->bhqk', q, k) * Dh ** (-0.5), axis=-1)
    a = torch.einsum('bhqk,bkhd->bqhd', attn, v).reshape(B, N, D) @ wo
    x = x + gate_a * a
    h2 = _layernorm(x, axis=-1) * (1 + scale_m) + shift_m
    m = _gelu(h2 @ w_fc1) @ w_fc2
    return x + gate_m * m
```

```cpp
#include <hip/hip_runtime.h>
#include <hip/hip_bf16.h>
#include <stdint.h>
#include <type_traits>

typedef __attribute__((ext_vector_type(8))) __bf16 bf16x8;
typedef __attribute__((ext_vector_type(4))) __bf16 bf16x4;
typedef __attribute__((ext_vector_type(2))) __bf16 bf16x2;
typedef __attribute__((ext_vector_type(4))) float f32x4;
typedef __attribute__((ext_vector_type(2))) float f32x2;
typedef __attribute__((ext_vector_type(4))) unsigned u32x4;
typedef __attribute__((ext_vector_type(2))) unsigned u32x2;
typedef unsigned short u16;
#define LDS_AS __attribute__((address_space(3)))

#define DEVI __device__ __forceinline__

static constexpr int Bsz = 2, Nseq = 1024, Dm = 768, Ff = 3072, Dh = 384;
static constexpr int Mrows = Bsz * Nseq;

template <int I, int N, class F>
DEVI void static_for(F&& f) {
  if constexpr (I < N) {
    f(std::integral_constant<int, I>{});
    static_for<I + 1, N>(f);
  }
}

DEVI float lo_f(unsigned u) { return __uint_as_float(u << 16); }
DEVI float hi_f(unsigned u) { return __uint_as_float(u & 0xffff0000u); }
DEVI float bf2f(u16 h) { return __uint_as_float(((unsigned)h) << 16); }
DEVI unsigned pack2(float a, float b) {
  bf16x2 v;
  v[0] = (__bf16)a;
  v[1] = (__bf16)b;
  return __builtin_bit_cast(unsigned, v);
}
DEVI float rbf(float a) { return lo_f(pack2(a, 0.f) ); }

template <int N>
DEVI void wait_vm() { asm volatile("s_waitcnt vmcnt(%0)" ::"i"(N) : "memory"); }
DEVI void wait_lgkm0() { asm volatile("s_waitcnt lgkmcnt(0)" ::: "memory"); }
DEVI void bar() { __builtin_amdgcn_s_barrier(); }

DEVI void glds16(const void* sbase, unsigned voff, unsigned lds_dst) {
  unsigned keep;
  asm volatile(
      "s_mov_b32 %0, m0\n\t"
      "s_mov_b32 m0, %3\n\t"
      "s_nop 0\n\t"
      "global_load_lds_dwordx4 %1, %2\n\t"
      "s_mov_b32 m0, %0"
      : "=&s"(keep)
      : "v"(voff), "s"(sbase), "s"(lds_dst)
      : "memory");
}

DEVI bf16x8 lds_read16(const LDS_AS char* p) { return *(const LDS_AS bf16x8*)p; }
DEVI u32x4 lds_read16u(const LDS_AS char* p) { return *(const LDS_AS u32x4*)p; }
DEVI f32x4 lds_read16f(const LDS_AS char* p) { return *(const LDS_AS f32x4*)p; }
DEVI bf16x4 lds_tr(const LDS_AS char* p) {
  return __builtin_amdgcn_ds_read_tr16_b64_v4bf16((LDS_AS bf16x4*)p);
}
DEVI bf16x8 cat4(bf16x4 a, bf16x4 b) {
  return __builtin_shufflevector(a, b, 0, 1, 2, 3, 4, 5, 6, 7);
}
DEVI f32x4 mfma16(bf16x8 a, bf16x8 b, f32x4 c) {
  return __builtin_amdgcn_mfma_f32_16x16x32_bf16(a, b, c, 0, 0, 0);
}
DEVI int uni(int v) { return __builtin_amdgcn_readfirstlane(v); }

DEVI u32x4 pair_pack(unsigned ax, unsigned ay, unsigned bx, unsigned by) {
  auto r0 = __builtin_amdgcn_permlane16_swap(ax, bx, false, false);
  auto r1 = __builtin_amdgcn_permlane16_swap(ay, by, false, false);
  u32x4 o;
  o[0] = r0[0];
  o[1] = r1[0];
  o[2] = r0[1];
  o[3] = r1[1];
  return o;
}

static constexpr int K0_MOD_BLOCKS = 144;
static constexpr int K0_LN_BLOCKS = 128;

__global__ __launch_bounds__(256) void k0_mod_ln(const u16* __restrict__ x, const u16* __restrict__ c,
                                                 const u16* __restrict__ wmod, float* __restrict__ modp,
                                                 u16* __restrict__ xn) {
  __shared__ float sc[2][384];
  __shared__ float red[32][2][64];
  const int tid = threadIdx.x;
  if (blockIdx.x < K0_MOD_BLOCKS) {
    const int cb = blockIdx.x % 72, kp = blockIdx.x / 72;
    const int tc = tid & 7, tr = tid >> 3;
    u32x4 w[12];
    const u16* wp = wmod + (size_t)(kp * 384 + tr) * 4608 + cb * 64 + tc * 8;
#pragma unroll
    for (int i = 0; i < 12; ++i) w[i] = *(const u32x4*)(wp + (size_t)i * 32 * 4608);
    for (int i = tid; i < 768; i += 256) {
      int b = i / 384, k = i % 384;
      float v = bf2f(c[b * 768 + kp * 384 + k]);
      float s = v / (1.f + __expf(-v));
      sc[b][k] = rbf(s);
    }
    __syncthreads();
    float acc[2][8];
#pragma unroll
    for (int b = 0; b < 2; ++b)
#pragma unroll
      for (int j = 0; j < 8; ++j) acc[b][j] = 0.f;
#pragma unroll
    for (int i = 0; i < 12; ++i) {
      float s0 = sc[0][tr + 32 * i], s1 = sc[1][tr + 32 * i];
#pragma unroll
      for (int j = 0; j < 4; ++j) {
        float a = lo_f(w[i][j]), bb = hi_f(w[i][j]);
        acc[0][2 * j] += s0 * a;
        acc[0][2 * j + 1] += s0 * bb;
        acc[1][2 * j] += s1 * a;
        acc[1][2 * j + 1] += s1 * bb;
      }
    }
#pragma unroll
    for (int b = 0; b < 2; ++b)
#pragma unroll
      for (int j = 0; j < 8; ++j) red[tr][b][tc * 8 + j] = acc[b][j];
    __syncthreads();
    if (tid < 128) {
      int b = tid >> 6, col = tid & 63;
      float s = 0.f;
#pragma unroll
      for (int r = 0; r < 32; ++r) s += red[r][b][col];
      modp[(kp * 2 + b) * 4608 + cb * 64 + col] = s;
    }
  } else {
    const int blk = blockIdx.x - K0_MOD_BLOCKS;
    const int w = tid >> 6, l = tid & 63;
    u32x2 v[4][3];
#pragma unroll
    for (int r = 0; r < 4; ++r) {
      const u16* xp = x + (size_t)(blk * 16 + w * 4 + r) * 768 + l * 4;
#pragma unroll
      for (int i = 0; i < 3; ++i) v[r][i] = *(const u32x2*)(xp + i * 256);
    }
#pragma unroll
    for (int r = 0; r < 4; ++r) {
      float f[12];
#pragma unroll
      for (int i = 0; i < 3; ++i) {
        f[4 * i] = lo_f(v[r][i][0]);
        f[4 * i + 1] = hi_f(v[r][i][0]);
        f[4 * i + 2] = lo_f(v[r][i][1]);
        f[4 * i + 3] = hi_f(v[r][i][1]);
      }
      float s = 0.f;
#pragma unroll
      for (int i = 0; i < 12; ++i) s += f[i];
#pragma unroll
      for (int o = 32; o > 0; o >>= 1) s += __shfl_xor(s, o);
      float mean = s * (1.f / 768.f);
      float q = 0.f;
#pragma unroll
      for (int i = 0; i < 12; ++i) {
        f[i] -= mean;
        q += f[i] * f[i];
      }
#pragma unroll
      for (int o = 32; o > 0; o >>= 1) q += __shfl_xor(q, o);
      float rs = rsqrtf(q * (1.f / 768.f) + 1e-5f);
      u16* op = xn + (size_t)(blk * 16 + w * 4 + r) * 768 + l * 4;
#pragma unroll
      for (int i = 0; i < 3; ++i) {
        u32x2 o;
        o[0] = pack2(f[4 * i] * rs, f[4 * i + 1] * rs);
        o[1] = pack2(f[4 * i + 2] * rs, f[4 * i + 3] * rs);
        *(u32x2*)(op + i * 256) = o;
      }
    }
  }
}

template <int BN, int NS, int MODE>
__global__ __launch_bounds__(256, 1) void k_gemm_ln(const u16* __restrict__ A, const u16* __restrict__ W,
                                                    const float* __restrict__ modp,
                                                    const f32x2* __restrict__ stats, u16* __restrict__ out) {
  constexpr int NC = BN / 16;
  constexpr int LDW = (MODE == 0) ? 2304 : 3072;
  constexpr int LDA = 768;
  constexpr int ASZ = 128 * 128;
  constexpr int BSZ = 8 * NC * 256;
  constexpr int STG = ASZ + BSZ;
  constexpr int SUBW = 2 * NC;
  constexpr int NBI = (SUBW + 3) / 4;
  constexpr int NLD = 4 + NBI;
  constexpr int NK = 768 / 64;
  __shared__ __attribute__((aligned(16))) char smem_raw[NS * STG + 6144];
  LDS_AS char* smem = (LDS_AS char*)smem_raw;
  const unsigned lds0 = (unsigned)(uintptr_t)smem;

  const int tid = threadIdx.x;
  const int l = tid & 63;
  const int w = uni(tid >> 6);
  const int bid = blockIdx.x;
  const int xcd = bid & 7, idx = bid >> 3;
  const int tm = (xcd >> 1) * 4 + (idx & 3)  + 0;
  const int tn = (xcd & 1) * 8 + (idx >> 2);
  const int m0 = tm * 128, n0 = tn * BN;
  const int b = m0 >> 10;

  const unsigned voffA = (unsigned)((l >> 3) * LDA * 2 + (((l & 7) ^ (l >> 3)) << 4));
  unsigned voffB[NBI];
#pragma unroll
  for (int i = 0; i < NBI; ++i) {
    int sub = SUBW * w + 4 * i + (l >> 4);
    if (sub >= SUBW * (w + 1)) sub = SUBW * (w + 1) - 1;
    int kb = sub / NC, cc = sub % NC;
    int within = l & 15;
    int r = (within >> 1) ^ ((kb & 1) << 2);
    voffB[i] = (unsigned)(((kb * 8 + r) * LDW + cc * 16 + (within & 1) * 8) * 2);
  }
  const char* abase = (const char*)(A + (size_t)(m0 + 32 * w) * LDA);
  const char* bbase = (const char*)(W + n0);

  auto issue = [&](int kt, int st) {
    const unsigned sa = lds0 + st * STG;
#pragma unroll
    for (int i = 0; i < 4; ++i)
      glds16(abase + (size_t)i * 8 * LDA * 2 + kt * 128, voffA, sa + (32 * w + 8 * i) * 128);
#pragma unroll
    for (int i = 0; i < NBI; ++i) {
      const char* sb = bbase + (size_t)kt * 64 * LDW * 2;
      unsigned dst = sa + ASZ + (SUBW * w + 4 * i) * 256;
      if (4 * i + 4 <= SUBW) {
        glds16(sb, voffB[i], dst);
      } else {
        if (l < (SUBW - 4 * i) * 16) glds16(sb, voffB[i], dst);
      }
    }
  };

  static_for<0, NS - 1>([&](auto I) { issue(I.value, I.value); });

  {
    LDS_AS float* gt = (LDS_AS float*)(smem + NS * STG);
    const int offS = (MODE == 0) ? 0 : 3 * 768;
    for (int k = tid; k < 768; k += 256) {
      float sh = modp[(0 * 2 + b) * 4608 + offS + k] + modp[(1 * 2 + b) * 4608 + offS + k];
      float scv = modp[(0 * 2 + b) * 4608 + offS + 768 + k] + modp[(1 * 2 + b) * 4608 + offS + 768 + k];
      gt[k] = rbf(1.f + rbf(scv));
      gt[768 + k] = rbf(sh);
    }
  }
  float rr[2] = {1.f, 1.f}, nm[2] = {0.f, 0.f};
  if constexpr (MODE == 1) {
#pragma unroll
    for (int mf = 0; mf < 2; ++mf) {
      const f32x2* sp = stats + (size_t)(m0 + 32 * w + 16 * mf + (l & 15)) * 8;
      f32x2 p[8];
#pragma unroll
      for (int j = 0; j < 8; ++j) p[j] = sp[j];
      float s = 0.f;
#pragma unroll
      for (int j = 0; j < 8; ++j) s += p[j][0];
      float mean = s * (1.f / 768.f);
      float m2 = 0.f;
#pragma unroll
      for (int j = 0; j < 8; ++j) {
        float d = p[j][0] * (1.f / 96.f) - mean;
        m2 += p[j][1] + 96.f * d * d;
      }
      float rs = rsqrtf(m2 * (1.f / 768.f) + 1e-5f);
      rr[mf] = rs;
      nm[mf] = -mean * rs;
    }
  }

  const int g = l >> 4;
  const unsigned aoff0 = (unsigned)((32 * w + (l & 15)) * 128 + ((g ^ (l & 7)) << 4));
  const int q = (l & 15) >> 2, p = l & 3;
  const unsigned boff_t0 = (unsigned)(ASZ + g * NC * 256 + ((4 * (g & 1) + q) * 32) + p * 8);
  const unsigned boff_t1 = (unsigned)(ASZ + g * NC * 256 + ((4 * (1 - (g & 1)) + q) * 32) + p * 8);
  const LDS_AS char* gtp = smem + NS * STG + g * 32;

  f32x4 acc[2][NC];
#pragma unroll
  for (int i = 0; i < 2; ++i)
#pragma unroll
    for (int j = 0; j < NC; ++j) acc[i][j] = (f32x4){0.f, 0.f, 0.f, 0.f};

  static_for<0, NK>([&](auto KT) {
    constexpr int kt = KT.value;
    constexpr int st = kt % NS;
    constexpr int ahead = (NK - 1 - kt) < (NS - 2) ? (NK - 1 - kt) : (NS - 2);
    wait_vm<ahead * NLD>();
    wait_lgkm0();
    bar();
    if constexpr (kt + NS - 1 < NK) issue(kt + NS - 1, (kt + NS - 1) % NS);
    const LDS_AS char* sp = smem + st * STG;
#pragma unroll
    for (int ks = 0; ks < 2; ++ks) {
      bf16x8 xb[2];
      f32x4 g0 = lds_read16f(gtp + (kt * 64 + ks * 32) * 4);
      f32x4 g1 = lds_read16f(gtp + (kt * 64 + ks * 32) * 4 + 16);
      f32x4 t0 = lds_read16f(gtp + 3072 + (kt * 64 + ks * 32) * 4);
      f32x4 t1 = lds_read16f(gtp + 3072 + (kt * 64 + ks * 32) * 4 + 16);
#pragma unroll
      for (int mf = 0; mf < 2; ++mf) {
        u32x4 raw = lds_read16u(sp + ((aoff0 ^ (ks * 64)) + mf * 2048));
        float v[8];
#pragma unroll
        for (int j = 0; j < 4; ++j) {
          v[2 * j] = lo_f(raw[j]);
          v[2 * j + 1] = hi_f(raw[j]);
        }
        if constexpr (MODE == 1) {
#pragma unroll
          for (int j = 0; j < 8; ++j) v[j] = rbf(v[j] * rr[mf] + nm[mf]);
        }
        u32x4 o;
        o[0] = pack2(v[0] * g0[0] + t0[0], v[1] * g0[1] + t0[1]);
        o[1] = pack2(v[2] * g0[2] + t0[2], v[3] * g0[3] + t0[3]);
        o[2] = pack2(v[4] * g1[0] + t1[0], v[5] * g1[1] + t1[1]);
        o[3] = pack2(v[6] * g1[2] + t1[2], v[7] * g1[3] + t1[3]);
        xb[mf] = __builtin_bit_cast(bf16x8, o);
      }
#pragma unroll
      for (int cc = 0; cc < NC; ++cc) {
        bf16x4 w0 = lds_tr(sp + boff_t0 + ks * 4 * NC * 256 + cc * 256);
        bf16x4 w1 = lds_tr(sp + boff_t1 + ks * 4 * NC * 256 + cc * 256);
        bf16x8 wf = cat4(w0, w1);
        acc[0][cc] = mfma16(wf, xb[0], acc[0][cc]);
        acc[1][cc] = mfma16(wf, xb[1], acc[1][cc]);
      }
    }
  });

  constexpr int LDO = (MODE == 0) ? 2304 : 3072;
#pragma unroll
  for (int mf = 0; mf < 2; ++mf) {
    u16* op = out + (size_t)(m0 + 32 * w + 16 * mf + (l & 15)) * LDO + n0;
    unsigned pk[NC][2];
#pragma unroll
    for (int cc = 0; cc < NC; ++cc) {
      f32x4 v = acc[mf][cc];
      if constexpr (MODE == 1) {
#pragma unroll
        for (int j = 0; j < 4; ++j) {
          float y = v[j];
          float z = 1.5957691216057308f * (y + 0.044715f * y * y * y);
          v[j] = y / (1.f + __expf(-z));
        }
      }
      pk[cc][0] = pack2(v[0], v[1]);
      pk[cc][1] = pack2(v[2], v[3]);
    }
#pragma unroll
    for (int a = 0; a < NC / 2; ++a) {
      u32x4 o = pair_pack(pk[2 * a][0], pk[2 * a][1], pk[2 * a + 1][0], pk[2 * a + 1][1]);
      *(u32x4*)(op + 32 * a + (g & 1) * 16 + (g & 2) * 4) = o;
    }
    if constexpr (NC & 1) {
      u32x2 o;
      o[0] = pk[NC - 1][0];
      o[1] = pk[NC - 1][1];
      *(u32x2*)(op + 16 * (NC - 1) + 4 * g) = o;
    }
  }
}

__global__ __launch_bounds__(256, 1) void k_scores(const u16* __restrict__ qkv, u16* __restrict__ P,
                                                   f32x2* __restrict__ ml) {
  constexpr int NS = 4;
  constexpr int ASZ = 128 * 128;
  constexpr int STG = 2 * ASZ;
  constexpr int NK = 384 / 64;
  constexpr int NLD = 8;
  constexpr int LDQ = 2304;
  __shared__ __attribute__((aligned(16))) char smem_raw[NS * STG];
  LDS_AS char* smem = (LDS_AS char*)smem_raw;
  const unsigned lds0 = (unsigned)(uintptr_t)smem;
  const int tid = threadIdx.x, l = tid & 63;
  const int w = uni(tid >> 6);
  const int bid = blockIdx.x;
  const int xcd = bid & 7, idx = bid >> 3;
  const int bh = xcd >> 1;
  const int tq = (xcd & 1) * 4 + (idx & 3);
  const int tk = idx >> 2;
  const int b = bh >> 1, h = bh & 1;

  const unsigned voff = (unsigned)((l >> 3) * LDQ * 2 + (((l & 7) ^ (l >> 3)) << 4));
  const char* qbase = (const char*)(qkv + (size_t)(b * 1024 + tq * 128 + 32 * w) * LDQ + h * 384);
  const char* kbase = (const char*)(qkv + (size_t)(b * 1024 + tk * 128 + 32 * w) * LDQ + 768 + h * 384);

  auto issue = [&](int kt, int st) {
    const unsigned sa = lds0 + st * STG;
#pragma unroll
    for (int i = 0; i < 4; ++i)
      glds16(qbase + (size_t)i * 8 * LDQ * 2 + kt * 128, voff, sa + (32 * w + 8 * i) * 128);
#pragma unroll
    for (int i = 0; i < 4; ++i)
      glds16(kbase + (size_t)i * 8 * LDQ * 2 + kt * 128, voff, sa + ASZ + (32 * w + 8 * i) * 128);
  };
  static_for<0, NS - 1>([&](auto I) { issue(I.value, I.value); });

  const int g = l >> 4;
  const unsigned qoff0 = (unsigned)((32 * w + (l & 15)) * 128 + ((g ^ (l & 7)) << 4));
  const unsigned koff0 = (unsigned)(ASZ + (l & 15) * 128 + ((g ^ (l & 7)) << 4));
  f32x4 acc[2][8];
#pragma unroll
  for (int i = 0; i < 2; ++i)
#pragma unroll
    for (int j = 0; j < 8; ++j) acc[i][j] = (f32x4){0.f, 0.f, 0.f, 0.f};

  static_for<0, NK>([&](auto KT) {
    constexpr int kt = KT.value;
    constexpr int st = kt % NS;
    constexpr int ahead = (NK - 1 - kt) < (NS - 2) ? (NK - 1 - kt) : (NS - 2);
    wait_vm<ahead * NLD>();
    wait_lgkm0();
    bar();
    if constexpr (kt + NS - 1 < NK) issue(kt + NS - 1, (kt + NS - 1) % NS);
    const LDS_AS char* sp = smem + st * STG;
#pragma unroll
    for (int ks = 0; ks < 2; ++ks) {
      bf16x8 qf[2];
#pragma unroll
      for (int mf = 0; mf < 2; ++mf) qf[mf] = lds_read16(sp + ((qoff0 ^ (ks * 64)) + mf * 2048));
#pragma unroll
      for (int kf = 0; kf < 8; ++kf) {
        bf16x8 kfr = lds_read16(sp + ((koff0 ^ (ks * 64)) + kf * 2048));
        acc[0][kf] = mfma16(kfr, qf[0], acc[0][kf]);
        acc[1][kf] = mfma16(kfr, qf[1], acc[1][kf]);
      }
    }
  });

  const float sl2e = 0.051031036307982884f * 1.4426950408889634f;
#pragma unroll
  for (int mf = 0; mf < 2; ++mf) {
    float mx = -INFINITY;
#pragma unroll
    for (int kf = 0; kf < 8; ++kf)
#pragma unroll
      for (int j = 0; j < 4; ++j) mx = fmaxf(mx, acc[mf][kf][j]);
    mx = fmaxf(mx, __shfl_xor(mx, 16));
    mx = fmaxf(mx, __shfl_xor(mx, 32));
    const float mb = mx * sl2e;
    float sum = 0.f;
    unsigned pk[8][2];
#pragma unroll
    for (int kf = 0; kf < 8; ++kf) {
      float e[4];
#pragma unroll
      for (int j = 0; j < 4; ++j) {
        e[j] = __builtin_amdgcn_exp2f(acc[mf][kf][j] * sl2e - mb);
        sum += e[j];
      }
      pk[kf][0] = pack2(e[0], e[1]);
      pk[kf][1] = pack2(e[2], e[3]);
    }
    sum += __shfl_xor(sum, 16);
    sum += __shfl_xor(sum, 32);
    const int qrow = tq * 128 + 32 * w + 16 * mf + (l & 15);
    u16* op = P + ((size_t)bh * 1024 + qrow) * 1024 + tk * 128;
#pragma unroll
    for (int a = 0; a < 4; ++a) {
      u32x4 o = pair_pack(pk[2 * a][0], pk[2 * a][1], pk[2 * a + 1][0], pk[2 * a + 1][1]);
      *(u32x4*)(op + 32 * a + (g & 1) * 16 + (g & 2) * 4) = o;
    }
    if (g == 0) {
      f32x2 s2;
      s2[0] = mb;
      s2[1] = sum;
      ml[((size_t)bh * 1024 + qrow) * 8 + tk] = s2;
    }
  }
}

template <int MODE>
__global__ __launch_bounds__(256, 1) void k_gemm_small(const u16* __restrict__ Ain, const u16* __restrict__ Bin,
                                                       const float* __restrict__ modp,
                                                       const u16* __restrict__ resid,
                                                       const f32x2* __restrict__ ml, f32x2* __restrict__ stats,
                                                       u16* __restrict__ out) {
  constexpr int KTOT = (MODE == 0) ? 1024 : (MODE == 1 ? 768 : 3072);
  constexpr int KW = KTOT / 4;
  constexpr int S = KW / 32;
  constexpr int LDA = KTOT;
  constexpr int LDB = (MODE == 0) ? 2304 : 768;
  constexpr int WSZ = 40960;
  __shared__ __attribute__((aligned(16))) char smem_raw[4 * WSZ];
  LDS_AS char* smem = (LDS_AS char*)smem_raw;
  const int tid = threadIdx.x, l = tid & 63;
  const int w = uni(tid >> 6);
  const unsigned ldsw = (unsigned)(uintptr_t)smem + w * WSZ;
  const int bid = blockIdx.x;
  const int xcd = bid & 7, idx = bid >> 3;

  int m0, n0, bh = 0;
  const u16* Ap;
  const u16* Bp;
  if constexpr (MODE == 0) {
    bh = xcd >> 1;
    const int tq = (xcd & 1) * 8 + (idx & 7);
    const int td = idx >> 3;
    m0 = tq * 64;
    n0 = td * 96;
    Ap = Ain + ((size_t)bh * 1024 + m0) * 1024 + w * KW;
    Bp = Bin + (size_t)((bh >> 1) * 1024 + w * KW) * LDB + 1536 + (bh & 1) * 384 + n0;
  } else {
    const int tm = xcd * 4 + (idx & 3);
    const int tn = idx >> 2;
    m0 = tm * 64;
    n0 = tn * 96;
    Ap = Ain + (size_t)m0 * LDA + w * KW;
    Bp = Bin + (size_t)(w * KW) * LDB + n0;
  }

  const unsigned voffA = (unsigned)((l >> 3) * LDA * 2 + (((l & 7) ^ (l >> 3)) << 4));
  unsigned voffB[6];
#pragma unroll
  for (int i = 0; i < 6; ++i) {
    int sub = 4 * i + (l >> 4);
    int kb = sub / 6, cc = sub % 6;
    int within = l & 15;
    int r = (within >> 1) ^ ((kb & 1) << 2);
    voffB[i] = (unsigned)(((kb * 8 + r) * LDB + cc * 16 + (within & 1) * 8) * 2);
  }
  auto issueA = [&](int a) {
    const char* sb = (const char*)Ap + a * 128;
    const unsigned dst = ldsw + (a & 1) * 8192;
#pragma unroll
    for (int i = 0; i < 8; ++i) glds16(sb + (size_t)i * 8 * LDA * 2, voffA, dst + i * 1024);
  };
  auto issueB = [&](int s) {
    const char* sb = (const char*)Bp + (size_t)s * 32 * LDB * 2;
    const unsigned dst = ldsw + 16384 + (s & 3) * 6144;
#pragma unroll
    for (int i = 0; i < 6; ++i) glds16(sb, voffB[i], dst + i * 1024);
  };
  issueA(0);
  issueB(0);
  issueB(1);
  issueB(2);

  const int g = l >> 4;
  float cf[4][2];
  if constexpr (MODE == 0) {
#pragma unroll
    for (int mf = 0; mf < 4; ++mf) {
      const f32x2* sp = ml + ((size_t)bh * 1024 + m0 + 16 * mf + (l & 15)) * 8;
      f32x2 p[8];
#pragma unroll
      for (int j = 0; j < 8; ++j) p[j] = sp[j];
      float mx = p[0][0];
#pragma unroll
      for (int j = 1; j < 8; ++j) mx = fmaxf(mx, p[j][0]);
      float lsum = 0.f;
      float c0 = 0.f, c1 = 0.f;
#pragma unroll
      for (int j = 0; j < 8; ++j) {
        float e = __builtin_amdgcn_exp2f(p[j][0] - mx);
        lsum += e * p[j][1];
        if (j == 2 * w) c0 = e;
        if (j == 2 * w + 1) c1 = e;
      }
      float inv = 1.f / lsum;
      cf[mf][0] = c0 * inv;
      cf[mf][1] = c1 * inv;
    }
  }

  const LDS_AS char* wp = smem + w * WSZ;
  const unsigned aoff0 = (unsigned)((l & 15) * 128 + ((g ^ (l & 7)) << 4));
  const int q = (l & 15) >> 2, p = l & 3;
  const unsigned boff_t0 = (unsigned)(16384 + g * 6 * 256 + ((4 * (g & 1) + q) * 32) + p * 8);
  const unsigned boff_t1 = (unsigned)(16384 + g * 6 * 256 + ((4 * (1 - (g & 1)) + q) * 32) + p * 8);

  f32x4 acc[4][6];
  f32x4 tot[4][6];
#pragma unroll
  for (int i = 0; i < 4; ++i)
#pragma unroll
    for (int j = 0; j < 6; ++j) acc[i][j] = (f32x4){0.f, 0.f, 0.f, 0.f};

  static_for<0, S>([&](auto SS) {
    constexpr int s = SS.value;
    wait_lgkm0();
    if constexpr ((s & 1) == 0 && (s / 2 + 1) < S / 2) issueA(s / 2 + 1);
    if constexpr (s + 3 < S) issueB(s + 3);
    if constexpr (s + 3 < S) wait_vm<26>();
    else if constexpr (s == S - 3) wait_vm<20>();
    else if constexpr (s == S - 2) wait_vm<6>();
    else wait_vm<0>();
    bf16x8 af[4];
#pragma unroll
    for (int mf = 0; mf < 4; ++mf)
      af[mf] = lds_read16(wp + ((s >> 1) & 1) * 8192 + ((aoff0 ^ ((s & 1) * 64)) + mf * 2048));
#pragma unroll
    for (int cc = 0; cc < 6; ++cc) {
      bf16x4 w0 = lds_tr(wp + boff_t0 + (s & 3) * 6144 + cc * 256);
      bf16x4 w1 = lds_tr(wp + boff_t1 + (s & 3) * 6144 + cc * 256);
      bf16x8 wf = cat4(w0, w1);
#pragma unroll
      for (int mf = 0; mf < 4; ++mf) acc[mf][cc] = mfma16(wf, af[mf], acc[mf][cc]);
    }
    if constexpr (MODE == 0 && s == 3) {
#pragma unroll
      for (int mf = 0; mf < 4; ++mf)
#pragma unroll
        for (int cc = 0; cc < 6; ++cc) {
          tot[mf][cc] = acc[mf][cc] * cf[mf][0];
          acc[mf][cc] = (f32x4){0.f, 0.f, 0.f, 0.f};
        }
    }
  });
  if constexpr (MODE == 0) {
#pragma unroll
    for (int mf = 0; mf < 4; ++mf)
#pragma unroll
      for (int cc = 0; cc < 6; ++cc) acc[mf][cc] = tot[mf][cc] + acc[mf][cc] * cf[mf][1];
  }

  wait_lgkm0();
  {
    LDS_AS char* rp = smem + w * WSZ + l * 16;
#pragma unroll
    for (int mf = 0; mf < 4; ++mf)
#pragma unroll
      for (int cc = 0; cc < 6; ++cc) *(LDS_AS f32x4*)(rp + (mf * 6 + cc) * 1024) = acc[mf][cc];
  }
  __syncthreads();
  f32x4 r[6];
#pragma unroll
  for (int cc = 0; cc < 6; ++cc) {
    f32x4 s4 = lds_read16f(smem + 0 * WSZ + (w * 6 + cc) * 1024 + l * 16);
#pragma unroll
    for (int ww = 1; ww < 4; ++ww) s4 += lds_read16f(smem + ww * WSZ + (w * 6 + cc) * 1024 + l * 16);
    r[cc] = s4;
  }
  const int mrow = m0 + 16 * w + (l & 15);
  if constexpr (MODE == 0) {
    u16* op = out + (size_t)((bh >> 1) * 1024 + mrow) * 768 + (bh & 1) * 384 + n0;
#pragma unroll
    for (int a = 0; a < 3; ++a) {
      u32x4 o = pair_pack(pack2(r[2 * a][0], r[2 * a][1]), pack2(r[2 * a][2], r[2 * a][3]),
                          pack2(r[2 * a + 1][0], r[2 * a + 1][1]), pack2(r[2 * a + 1][2], r[2 * a + 1][3]));
      *(u32x4*)(op + 32 * a + (g & 1) * 16 + (g & 2) * 4) = o;
    }
  } else {
    const int b = mrow >> 10;
    const int goff = (MODE == 1) ? 2 * 768 : 5 * 768;
    const float* mp0 = modp + (0 * 2 + b) * 4608 + goff + n0 + 4 * g;
    const float* mp1 = modp + (1 * 2 + b) * 4608 + goff + n0 + 4 * g;
    const u16* rp = resid + (size_t)mrow * 768 + n0 + 4 * g;
    float v[6][4];
#pragma unroll
    for (int cc = 0; cc < 6; ++cc) {
      f32x4 ga = *(const f32x4*)(mp0 + 16 * cc);
      f32x4 gb = *(const f32x4*)(mp1 + 16 * cc);
      u32x2 xr = *(const u32x2*)(rp + 16 * cc);
      float xv[4] = {lo_f(xr[0]), hi_f(xr[0]), lo_f(xr[1]), hi_f(xr[1])};
#pragma unroll
      for (int j = 0; j < 4; ++j) {
        float gate = rbf(ga[j] + gb[j]);
        v[cc][j] = rbf(xv[j] + rbf(gate * rbf(r[cc][j])));
      }
    }
    u16* op = out + (size_t)mrow * 768 + n0;
#pragma unroll
    for (int a = 0; a < 3; ++a) {
      u32x4 o = pair_pack(pack2(v[2 * a][0], v[2 * a][1]), pack2(v[2 * a][2], v[2 * a][3]),
                          pack2(v[2 * a + 1][0], v[2 * a + 1][1]), pack2(v[2 * a + 1][2], v[2 * a + 1][3]));
      *(u32x4*)(op + 32 * a + (g & 1) * 16 + (g & 2) * 4) = o;
    }
    if constexpr (MODE == 1) {
      float s = 0.f;
#pragma unroll
      for (int cc = 0; cc < 6; ++cc)
#pragma unroll
        for (int j = 0; j < 4; ++j) s += v[cc][j];
      s += __shfl_xor(s, 16);
      s += __shfl_xor(s, 32);
      float mean = s * (1.f / 96.f);
      float m2 = 0.f;
#pragma unroll
      for (int cc = 0; cc < 6; ++cc)
#pragma unroll
        for (int j = 0; j < 4; ++j) {
          float d = v[cc][j] - mean;
          m2 += d * d;
        }
      m2 += __shfl_xor(m2, 16);
      m2 += __shfl_xor(m2, 32);
      if (g == 0) {
        f32x2 s2;
        s2[0] = s;
        s2[1] = m2;
        stats[(size_t)mrow * 8 + (n0 / 96)] = s2;
      }
    }
  }
}

extern "C" void kernel_launch(void* const* d_in, const int* in_sizes, int n_in, void* d_out, int out_size,
                              void* d_ws, size_t ws_size, hipStream_t stream) {
  const u16* x = (const u16*)d_in[0];
  const u16* c = (const u16*)d_in[1];
  const u16* wqkv = (const u16*)d_in[2];
  const u16* wo = (const u16*)d_in[3];
  const u16* wmod = (const u16*)d_in[4];
  const u16* wfc1 = (const u16*)d_in[5];
  const u16* wfc2 = (const u16*)d_in[6];
  u16* out = (u16*)d_out;

  char* ws = (char*)d_ws;
  size_t off = 0;
  auto alloc = [&](size_t bytes) {
    char* p = ws + off;
    off += (bytes + 255) & ~(size_t)255;
    return p;
  };
  float* modp = (float*)alloc(2 * 2 * 4608 * 4);
  u16* xn = (u16*)alloc((size_t)Mrows * 768 * 2);
  u16* qkv = (u16*)alloc((size_t)Mrows * 2304 * 2);
  u16* P = (u16*)alloc((size_t)4 * 1024 * 1024 * 2);
  f32x2* ml = (f32x2*)alloc((size_t)4 * 1024 * 8 * 8);
  u16* O = (u16*)alloc((size_t)Mrows * 768 * 2);
  u16* x1 = (u16*)alloc((size_t)Mrows * 768 * 2);
  f32x2* st2 = (f32x2*)alloc((size_t)Mrows * 8 * 8);
  u16* u = (u16*)alloc((size_t)Mrows * 3072 * 2);

  k0_mod_ln<<<K0_MOD_BLOCKS + K0_LN_BLOCKS, 256, 0, stream>>>(x, c, wmod, modp, xn);
  k_gemm_ln<144, 4, 0><<<256, 256, 0, stream>>>(xn, wqkv, modp, nullptr, qkv);
  k_scores<<<256, 256, 0, stream>>>(qkv, P, ml);
  k_gemm_small<0><<<256, 256, 0, stream>>>(P, qkv, modp, nullptr, ml, nullptr, O);
  k_gemm_small<1><<<256, 256, 0, stream>>>(O, wo, modp, x, nullptr, st2, x1);
  k_gemm_ln<192, 3, 1><<<256, 256, 0, stream>>>(x1, wfc1, modp, st2, u);
  k_gemm_small<2><<<256, 256, 0, stream>>>(u, wfc2, modp, x1, nullptr, nullptr, out);
}
```

```cpp
#include <hip/hip_runtime.h>
#include <hip/hip_bf16.h>
#include <stdint.h>
#include <type_traits>

typedef __attribute__((ext_vector_type(8))) __bf16 bf16x8;
typedef __attribute__((ext_vector_type(4))) __bf16 bf16x4;
typedef __attribute__((ext_vector_type(2))) __bf16 bf16x2;
typedef __attribute__((ext_vector_type(4))) float f32x4;
typedef __attribute__((ext_vector_type(2))) float f32x2;
typedef __attribute__((ext_vector_type(4))) unsigned u32x4;
typedef __attribute__((ext_vector_type(2))) unsigned u32x2;
typedef unsigned short u16;
#define LDS_AS __attribute__((address_space(3)))

#define DEVI __device__ __forceinline__
#define STAMP(K, I)

static constexpr int Bsz = 2, Nseq = 1024, Dm = 768, Ff = 3072, Dh = 384;
static constexpr int Mrows = Bsz * Nseq;

template <int I, int N, class F>
DEVI void static_for(F&& f) {
  if constexpr (I < N) {
    f(std::integral_constant<int, I>{});
    static_for<I + 1, N>(f);
  }
}

DEVI float lo_f(unsigned u) { return __uint_as_float(u << 16); }
DEVI float hi_f(unsigned u) { return __uint_as_float(u & 0xffff0000u); }
DEVI float bf2f(u16 h) { return __uint_as_float(((unsigned)h) << 16); }
DEVI unsigned pack2(float a, float b) {
  bf16x2 v;
  v[0] = (__bf16)a;
  v[1] = (__bf16)b;
  return __builtin_bit_cast(unsigned, v);
}
DEVI float rbf(float a) { return lo_f(pack2(a, 0.f) ); }

template <int N>
DEVI void wait_vm() { asm volatile("s_waitcnt vmcnt(%0)" ::"i"(N) : "memory"); }
DEVI void wait_lgkm0() { asm volatile("s_waitcnt lgkmcnt(0)" ::: "memory"); }
DEVI void bar() { __builtin_amdgcn_s_barrier(); }

DEVI void glds16(const void* sbase, unsigned voff, unsigned lds_dst) {
  unsigned keep;
  asm volatile(
      "s_mov_b32 %0, m0\n\t"
      "s_mov_b32 m0, %3\n\t"
      "s_nop 0\n\t"
      "global_load_lds_dwordx4 %1, %2\n\t"
      "s_mov_b32 m0, %0"
      : "=&s"(keep)
      : "v"(voff), "s"(sbase), "s"(lds_dst)
      : "memory");
}

DEVI void gld16(u32x4& d, const void* p) { asm volatile("global_load_dwordx4 %0, %1, off" : "=v"(d) : "v"(p) : "memory"); }
DEVI void gld8(u32x2& d, const void* p) { asm volatile("global_load_dwordx2 %0, %1, off" : "=v"(d) : "v"(p) : "memory"); }
template <class T>
DEVI void pin(T& v) { asm volatile("" : "+v"(v)); }
DEVI f32x4 asf4(u32x4 v) { return __builtin_bit_cast(f32x4, v); }

DEVI bf16x8 lds_read16(const LDS_AS char* p) { return *(const LDS_AS bf16x8*)p; }
DEVI u32x4 lds_read16u(const LDS_AS char* p) { return *(const LDS_AS u32x4*)p; }
DEVI f32x4 lds_read16f(const LDS_AS char* p) { return *(const LDS_AS f32x4*)p; }
DEVI bf16x4 lds_tr(const LDS_AS char* p) {
  return __builtin_amdgcn_ds_read_tr16_b64_v4bf16((LDS_AS bf16x4*)p);
}
DEVI bf16x8 cat4(bf16x4 a, bf16x4 b) {
  return __builtin_shufflevector(a, b, 0, 1, 2, 3, 4, 5, 6, 7);
}
DEVI f32x4 mfma16(bf16x8 a, bf16x8 b, f32x4 c) {
  return __builtin_amdgcn_mfma_f32_16x16x32_bf16(a, b, c, 0, 0, 0);
}
DEVI int uni(int v) { return __builtin_amdgcn_readfirstlane(v); }

DEVI u32x4 pair_pack(unsigned ax, unsigned ay, unsigned bx, unsigned by) {
  auto r0 = __builtin_amdgcn_permlane16_swap(ax, bx, false, false);
  auto r1 = __builtin_amdgcn_permlane16_swap(ay, by, false, false);
  u32x4 o;
  o[0] = r0[0];
  o[1] = r1[0];
  o[2] = r0[1];
  o[3] = r1[1];
  return o;
}

static constexpr int K0_MOD_BLOCKS = 144;
static constexpr int K0_LN_BLOCKS = 128;

__global__ __launch_bounds__(256) void k0_mod_ln(const u16* __restrict__ x, const u16* __restrict__ c,
                                                 const u16* __restrict__ wmod, float* __restrict__ modp,
                                                 u16* __restrict__ xn) {
  __shared__ float sc[2][384];
  __shared__ float red[32][2][64];
  const int tid = threadIdx.x;
  if (blockIdx.x < K0_MOD_BLOCKS) {
    const int cb = blockIdx.x % 72, kp = blockIdx.x / 72;
    const int tc = tid & 7, tr = tid >> 3;
    u32x4 w[12];
    const u16* wp = wmod + (size_t)(kp * 384 + tr) * 4608 + cb * 64 + tc * 8;
#pragma unroll
    for (int i = 0; i < 12; ++i) w[i] = *(const u32x4*)(wp + (size_t)i * 32 * 4608);
    for (int i = tid; i < 768; i += 256) {
      int b = i / 384, k = i % 384;
      float v = bf2f(c[b * 768 + kp * 384 + k]);
      float s = v / (1.f + __expf(-v));
      sc[b][k] = rbf(s);
    }
    __syncthreads();
    float acc[2][8];
#pragma unroll
    for (int b = 0; b < 2; ++b)
#pragma unroll
      for (int j = 0; j < 8; ++j) acc[b][j] = 0.f;
#pragma unroll
    for (int i = 0; i < 12; ++i) {
      float s0 = sc[0][tr + 32 * i], s1 = sc[1][tr + 32 * i];
#pragma unroll
      for (int j = 0; j < 4; ++j) {
        float a = lo_f(w[i][j]), bb = hi_f(w[i][j]);
        acc[0][2 * j] += s0 * a;
        acc[0][2 * j + 1] += s0 * bb;
        acc[1][2 * j] += s1 * a;
        acc[1][2 * j + 1] += s1 * bb;
      }
    }
#pragma unroll
    for (int b = 0; b < 2; ++b)
#pragma unroll
      for (int j = 0; j < 8; ++j) red[tr][b][tc * 8 + j] = acc[b][j];
    __syncthreads();
    if (tid < 128) {
      int b = tid >> 6, col = tid & 63;
      float s = 0.f;
#pragma unroll
      for (int r = 0; r < 32; ++r) s += red[r][b][col];
      modp[(kp * 2 + b) * 4608 + cb * 64 + col] = s;
    }
  } else {
    const int blk = blockIdx.x - K0_MOD_BLOCKS;
    const int w = tid >> 6, l = tid & 63;
    u32x2 v[4][3];
#pragma unroll
    for (int r = 0; r < 4; ++r) {
      const u16* xp = x + (size_t)(blk * 16 + w * 4 + r) * 768 + l * 4;
#pragma unroll
      for (int i = 0; i < 3; ++i) v[r][i] = *(const u32x2*)(xp + i * 256);
    }
#pragma unroll
    for (int r = 0; r < 4; ++r) {
      float f[12];
#pragma unroll
      for (int i = 0; i < 3; ++i) {
        f[4 * i] = lo_f(v[r][i][0]);
        f[4 * i + 1] = hi_f(v[r][i][0]);
        f[4 * i + 2] = lo_f(v[r][i][1]);
        f[4 * i + 3] = hi_f(v[r][i][1]);
      }
      float s = 0.f;
#pragma unroll
      for (int i = 0; i < 12; ++i) s += f[i];
#pragma unroll
      for (int o = 32; o > 0; o >>= 1) s += __shfl_xor(s, o);
      float mean = s * (1.f / 768.f);
      float q = 0.f;
#pragma unroll
      for (int i = 0; i < 12; ++i) {
        f[i] -= mean;
        q += f[i] * f[i];
      }
#pragma unroll
      for (int o = 32; o > 0; o >>= 1) q += __shfl_xor(q, o);
      float rs = rsqrtf(q * (1.f / 768.f) + 1e-5f);
      u16* op = xn + (size_t)(blk * 16 + w * 4 + r) * 768 + l * 4;
#pragma unroll
      for (int i = 0; i < 3; ++i) {
        u32x2 o;
        o[0] = pack2(f[4 * i] * rs, f[4 * i + 1] * rs);
        o[1] = pack2(f[4 * i + 2] * rs, f[4 * i + 3] * rs);
        *(u32x2*)(op + i * 256) = o;
      }
    }
  }
}

template <int BN, int NS, int MODE>
__global__ __launch_bounds__(512, 2) void k_gemm_ln(const u16* __restrict__ A, const u16* __restrict__ W,
                                                    const float* __restrict__ modp,
                                                    const f32x2* __restrict__ stats, u16* __restrict__ out,
                                                    u16* __restrict__ out2) {
  constexpr int NC = BN / 16;
  constexpr int LDW = (MODE == 0) ? 2304 : 3072;
  constexpr int LDA = 768;
  constexpr int ASZ = 128 * 128;
  constexpr int BSZ = 8 * NC * 256;
  constexpr int STG = ASZ + BSZ;
  constexpr int SUBW = NC;
  constexpr int NBI = (SUBW + 3) / 4;
  constexpr int NLD = 2 + NBI;
  constexpr int NK = 768 / 64;
  __shared__ __attribute__((aligned(16))) char smem_raw[NS * STG + 6144];
  LDS_AS char* smem = (LDS_AS char*)smem_raw;
  const unsigned lds0 = (unsigned)(uintptr_t)smem;
  STAMP(MODE == 0 ? 1 : 6, 0);
  const int tid = threadIdx.x;
  const int l = tid & 63;
  const int w = uni(tid >> 6);
  const int wm = w & 3, kh = w >> 2;
  const int bid = blockIdx.x;
  const int xcd = bid & 7, idx = bid >> 3;
  const int tm = (xcd >> 1) * 4 + (idx & 3);
  const int tn = (xcd & 1) * 8 + (idx >> 2);
  const int m0 = tm * 128, n0 = tn * BN;
  const int b = m0 >> 10;

  u32x4 mA, mB;
  {
    const int offS = (MODE == 0) ? 0 : 3 * 768;
    const int t = tid < 384 ? tid : 383;
    const int o = (t < 192) ? (offS + 768 + 4 * t) : (offS + 4 * (t - 192));
    gld16(mA, modp + (0 * 2 + b) * 4608 + o);
    gld16(mB, modp + (1 * 2 + b) * 4608 + o);
  }
  u32x4 sv[2][4];
  if constexpr (MODE == 1) {
#pragma unroll
    for (int mf = 0; mf < 2; ++mf) {
      const f32x2* sp = stats + (size_t)(m0 + 32 * wm + 16 * mf + (l & 15)) * 8;
#pragma unroll
      for (int j = 0; j < 4; ++j) gld16(sv[mf][j], sp + 2 * j);
    }
  }

  const unsigned voffA = (unsigned)((l >> 3) * LDA * 2 + (((l & 7) ^ (l >> 3)) << 4));
  unsigned voffB[NBI];
#pragma unroll
  for (int i = 0; i < NBI; ++i) {
    int sub = SUBW * w + 4 * i + (l >> 4);
    if (sub >= SUBW * (w + 1)) sub = SUBW * (w + 1) - 1;
    int kb = sub / NC, cc = sub % NC;
    int within = l & 15;
    int r = (within >> 1) ^ ((kb & 1) << 2);
    voffB[i] = (unsigned)(((kb * 8 + r) * LDW + cc * 16 + (within & 1) * 8) * 2);
  }
  const char* abase = (const char*)(A + (size_t)(m0 + 16 * w) * LDA);
  const char* bbase = (const char*)(W + n0);

  auto issue = [&](int kt, int st) {
    const unsigned sa = lds0 + st * STG;
#pragma unroll
    for (int i = 0; i < 2; ++i)
      glds16(abase + (size_t)i * 8 * LDA * 2 + kt * 128, voffA, sa + (16 * w + 8 * i) * 128);
    const char* sb = bbase + (size_t)kt * 64 * LDW * 2;
#pragma unroll
    for (int i = 0; i < NBI; ++i) {
      unsigned dst = sa + ASZ + (SUBW * w + 4 * i) * 256;
      if (4 * i + 4 <= SUBW) {
        glds16(sb, voffB[i], dst);
      } else {
        if (l < (SUBW - 4 * i) * 16) glds16(sb, voffB[i], dst);
      }
    }
  };
#pragma unroll
  for (int s = 0; s < NS - 1; ++s) issue(s, s);

  wait_vm<(NS - 1) * NLD>();
  pin(mA);
  pin(mB);
  if (tid < 384) {
    f32x4 a = asf4(mA), bb = asf4(mB), o;
    if (tid < 192) {
#pragma unroll
      for (int j = 0; j < 4; ++j) o[j] = rbf(1.f + rbf(a[j] + bb[j]));
    } else {
#pragma unroll
      for (int j = 0; j < 4; ++j) o[j] = rbf(a[j] + bb[j]);
    }
    *(LDS_AS f32x4*)(smem + NS * STG + tid * 16) = o;
  }
  float rr[2] = {1.f, 1.f}, nm[2] = {0.f, 0.f};
  if constexpr (MODE == 1) {
#pragma unroll
    for (int mf = 0; mf < 2; ++mf) {
      float s = 0.f;
#pragma unroll
      for (int j = 0; j < 4; ++j) {
        pin(sv[mf][j]);
        s += __uint_as_float(sv[mf][j][0]) + __uint_as_float(sv[mf][j][2]);
      }
      float mean = s * (1.f / 768.f);
      float m2 = 0.f;
#pragma unroll
      for (int j = 0; j < 4; ++j) {
        float d0 = __uint_as_float(sv[mf][j][0]) * (1.f / 96.f) - mean;
        float d1 = __uint_as_float(sv[mf][j][2]) * (1.f / 96.f) - mean;
        m2 += __uint_as_float(sv[mf][j][1]) + __uint_as_float(sv[mf][j][3]) + 96.f * (d0 * d0 + d1 * d1);
      }
      float rs = rsqrtf(m2 * (1.f / 768.f) + 1e-5f);
      rr[mf] = rs;
      nm[mf] = -mean * rs;
    }
  }

  const int g = l >> 4;
  const unsigned aoff = (unsigned)(((32 * wm + (l & 15)) * 128 + ((g ^ (l & 7)) << 4)) ^ (kh * 64));
  const int q = (l & 15) >> 2, p = l & 3;
  const unsigned boff_t0 = (unsigned)(ASZ + (4 * kh + g) * NC * 256 + ((4 * (g & 1) + q) * 32) + p * 8);
  const unsigned boff_t1 = (unsigned)(ASZ + (4 * kh + g) * NC * 256 + ((4 * (1 - (g & 1)) + q) * 32) + p * 8);
  const LDS_AS char* gtp = smem + NS * STG + (kh * 32 + g * 8) * 4;

  f32x4 acc[2][NC];
#pragma unroll
  for (int i = 0; i < 2; ++i)
#pragma unroll
    for (int j = 0; j < NC; ++j) acc[i][j] = (f32x4){0.f, 0.f, 0.f, 0.f};
  STAMP(MODE == 0 ? 1 : 6, 1);

  int st = 0, stn = NS - 1;
#pragma unroll 1
  for (int kt = 0; kt < NK; ++kt) {
    if (kt < NK - (NS - 2)) wait_vm<(NS - 2) * NLD>();
    else wait_vm<0>();
    wait_lgkm0();
    bar();
    if (kt + NS - 1 < NK) issue(kt + NS - 1, stn);
    const LDS_AS char* sp = smem + st * STG;
    bf16x8 xb[2];
    f32x4 g0 = lds_read16f(gtp + kt * 256);
    f32x4 g1 = lds_read16f(gtp + kt * 256 + 16);
    f32x4 t0 = lds_read16f(gtp + 3072 + kt * 256);
    f32x4 t1 = lds_read16f(gtp + 3072 + kt * 256 + 16);
#pragma unroll
    for (int mf = 0; mf < 2; ++mf) {
      u32x4 raw = lds_read16u(sp + aoff + mf * 2048);
      float v[8];
#pragma unroll
      for (int j = 0; j < 4; ++j) {
        v[2 * j] = lo_f(raw[j]);
        v[2 * j + 1] = hi_f(raw[j]);
      }
      if constexpr (MODE == 1) {
#pragma unroll
        for (int j = 0; j < 8; ++j) v[j] = v[j] * rr[mf] + nm[mf];
      }
      u32x4 o;
      o[0] = pack2(v[0] * g0[0] + t0[0], v[1] * g0[1] + t0[1]);
      o[1] = pack2(v[2] * g0[2] + t0[2], v[3] * g0[3] + t0[3]);
      o[2] = pack2(v[4] * g1[0] + t1[0], v[5] * g1[1] + t1[1]);
      o[3] = pack2(v[6] * g1[2] + t1[2], v[7] * g1[3] + t1[3]);
      xb[mf] = __builtin_bit_cast(bf16x8, o);
    }
#pragma unroll
    for (int cc = 0; cc < NC; ++cc) {
      bf16x4 w0 = lds_tr(sp + boff_t0 + cc * 256);
      bf16x4 w1 = lds_tr(sp + boff_t1 + cc * 256);
      bf16x8 wf = cat4(w0, w1);
      acc[0][cc] = mfma16(wf, xb[0], acc[0][cc]);
      acc[1][cc] = mfma16(wf, xb[1], acc[1][cc]);
    }
    st = (st + 1 == NS) ? 0 : st + 1;
    stn = (stn + 1 == NS) ? 0 : stn + 1;
  }
  STAMP(MODE == 0 ? 1 : 6, 14);

  wait_lgkm0();
  bar();
  {
    LDS_AS char* rp = smem + (wm * 2 + kh) * NC * 1024 + l * 16;
#pragma unroll
    for (int cc = 0; cc < NC; ++cc) *(LDS_AS f32x4*)(rp + cc * 1024) = kh ? acc[0][cc] : acc[1][cc];
  }
  __syncthreads();
  const int row = m0 + 32 * wm + 16 * kh + (l & 15);
  unsigned pk[NC][2];
  {
    const LDS_AS char* rp = smem + (wm * 2 + (1 - kh)) * NC * 1024 + l * 16;
#pragma unroll
    for (int cc = 0; cc < NC; ++cc) {
      f32x4 v = (kh ? acc[1][cc] : acc[0][cc]) + lds_read16f(rp + cc * 1024);
      if constexpr (MODE == 1) {
#pragma unroll
        for (int j = 0; j < 4; ++j) {
          float y = v[j];
          float z = 1.5957691216057308f * (y + 0.044715f * y * y * y);
          v[j] = y / (1.f + __expf(-z));
        }
      }
      pk[cc][0] = pack2(v[0], v[1]);
      pk[cc][1] = pack2(v[2], v[3]);
    }
  }
  auto dst_ptr = [&](int n) -> u16* {
    if constexpr (MODE == 1) {
      return out + (size_t)row * 3072 + n;
    } else {
      if (n < 1536) return out + (size_t)row * 1536 + n;
      int nv = n - 1536;
      int hd = nv / 96;
      int dd = nv - hd * 96;
      return out2 + ((size_t)((b * 8 + hd) * 1024 + (row & 1023))) * 96 + dd;
    }
  };
#pragma unroll
  for (int a = 0; a < NC / 2; ++a) {
    u32x4 o = pair_pack(pk[2 * a][0], pk[2 * a][1], pk[2 * a + 1][0], pk[2 * a + 1][1]);
    *(u32x4*)dst_ptr(n0 + 32 * a + (g & 1) * 16 + (g & 2) * 4) = o;
  }
  if constexpr (NC & 1) {
    u32x2 o;
    o[0] = pk[NC - 1][0];
    o[1] = pk[NC - 1][1];
    *(u32x2*)dst_ptr(n0 + 16 * (NC - 1) + 4 * g) = o;
  }
  STAMP(MODE == 0 ? 1 : 6, 15);
}

__global__ __launch_bounds__(512, 2) void k_scores(const u16* __restrict__ qk, u16* __restrict__ P,
                                                   f32x2* __restrict__ ml) {
  constexpr int NS = 4;
  constexpr int ASZ = 128 * 128;
  constexpr int STG = 2 * ASZ;
  constexpr int NK = 384 / 64;
  constexpr int NLD = 4;
  constexpr int LDQ = 1536;
  __shared__ __attribute__((aligned(16))) char smem_raw[NS * STG];
  LDS_AS char* smem = (LDS_AS char*)smem_raw;
  const unsigned lds0 = (unsigned)(uintptr_t)smem;
  STAMP(2, 0);
  const int tid = threadIdx.x, l = tid & 63;
  const int w = uni(tid >> 6);
  const int wm = w & 3, kh = w >> 2;
  const int bid = blockIdx.x;
  const int xcd = bid & 7, idx = bid >> 3;
  const int bh = xcd >> 1;
  const int tq = (xcd & 1) * 4 + (idx & 3);
  const int tk = idx >> 2;
  const int b = bh >> 1, h = bh & 1;

  const unsigned voff = (unsigned)((l >> 3) * LDQ * 2 + (((l & 7) ^ (l >> 3)) << 4));
  const char* qbase = (const char*)(qk + (size_t)(b * 1024 + tq * 128 + 16 * w) * LDQ + h * 384);
  const char* kbase = (const char*)(qk + (size_t)(b * 1024 + tk * 128 + 16 * w) * LDQ + 768 + h * 384);

  auto issue = [&](int kt, int st) {
    const unsigned sa = lds0 + st * STG;
#pragma unroll
    for (int i = 0; i < 2; ++i)
      glds16(qbase + (size_t)i * 8 * LDQ * 2 + kt * 128, voff, sa + (16 * w + 8 * i) * 128);
#pragma unroll
    for (int i = 0; i < 2; ++i)
      glds16(kbase + (size_t)i * 8 * LDQ * 2 + kt * 128, voff, sa + ASZ + (16 * w + 8 * i) * 128);
  };
#pragma unroll
  for (int s = 0; s < NS - 1; ++s) issue(s, s);

  const int g = l >> 4;
  const unsigned qoff = (unsigned)(((32 * wm + (l & 15)) * 128 + ((g ^ (l & 7)) << 4)) ^ (kh * 64));
  const unsigned koff = (unsigned)(ASZ + (((l & 15) * 128 + ((g ^ (l & 7)) << 4)) ^ (kh * 64)));
  f32x4 acc[2][8];
#pragma unroll
  for (int i = 0; i < 2; ++i)
#pragma unroll
    for (int j = 0; j < 8; ++j) acc[i][j] = (f32x4){0.f, 0.f, 0.f, 0.f};

  int st = 0, stn = NS - 1;
#pragma unroll 1
  for (int kt = 0; kt < NK; ++kt) {
    if (kt < NK - (NS - 2)) wait_vm<(NS - 2) * NLD>();
    else wait_vm<0>();
    wait_lgkm0();
    bar();
    if (kt + NS - 1 < NK) issue(kt + NS - 1, stn);
    const LDS_AS char* sp = smem + st * STG;
    bf16x8 qf[2];
#pragma unroll
    for (int mf = 0; mf < 2; ++mf) qf[mf] = lds_read16(sp + qoff + mf * 2048);
#pragma unroll
    for (int kf = 0; kf < 8; ++kf) {
      bf16x8 kfr = lds_read16(sp + koff + kf * 2048);
      acc[0][kf] = mfma16(kfr, qf[0], acc[0][kf]);
      acc[1][kf] = mfma16(kfr, qf[1], acc[1][kf]);
    }
    st = (st + 1 == NS) ? 0 : st + 1;
    stn = (stn + 1 == NS) ? 0 : stn + 1;
  }
  STAMP(2, 14);
  wait_lgkm0();
  bar();
  {
    LDS_AS char* rp = smem + (wm * 2 + kh) * 8 * 1024 + l * 16;
#pragma unroll
    for (int kf = 0; kf < 8; ++kf) *(LDS_AS f32x4*)(rp + kf * 1024) = kh ? acc[0][kf] : acc[1][kf];
  }
  __syncthreads();
  f32x4 s[8];
  {
    const LDS_AS char* rp = smem + (wm * 2 + (1 - kh)) * 8 * 1024 + l * 16;
#pragma unroll
    for (int kf = 0; kf < 8; ++kf) s[kf] = (kh ? acc[1][kf] : acc[0][kf]) + lds_read16f(rp + kf * 1024);
  }
  const float sl2e = 0.051031036307982884f * 1.4426950408889634f;
  float mx = -INFINITY;
#pragma unroll
  for (int kf = 0; kf < 8; ++kf)
#pragma unroll
    for (int j = 0; j < 4; ++j) mx = fmaxf(mx, s[kf][j]);
  mx = fmaxf(mx, __shfl_xor(mx, 16));
  mx = fmaxf(mx, __shfl_xor(mx, 32));
  const float mb = mx * sl2e;
  float sum = 0.f;
  unsigned pk[8][2];
#pragma unroll
  for (int kf = 0; kf < 8; ++kf) {
    float e[4];
#pragma unroll
    for (int j = 0; j < 4; ++j) {
      e[j] = __builtin_amdgcn_exp2f(s[kf][j] * sl2e - mb);
      sum += e[j];
    }
    pk[kf][0] = pack2(e[0], e[1]);
    pk[kf][1] = pack2(e[2], e[3]);
  }
  sum += __shfl_xor(sum, 16);
  sum += __shfl_xor(sum, 32);
  const int qrow = tq * 128 + 32 * wm + 16 * kh + (l & 15);
  u16* op = P + ((size_t)bh * 1024 + qrow) * 1024 + tk * 128;
#pragma unroll
  for (int a = 0; a < 4; ++a) {
    u32x4 o = pair_pack(pk[2 * a][0], pk[2 * a][1], pk[2 * a + 1][0], pk[2 * a + 1][1]);
    *(u32x4*)(op + 32 * a + (g & 1) * 16 + (g & 2) * 4) = o;
  }
  if (g == 0) {
    f32x2 s2;
    s2[0] = mb;
    s2[1] = sum;
    ml[((size_t)bh * 1024 + qrow) * 8 + tk] = s2;
  }
  STAMP(2, 15);
}

template <int MODE>
__global__ __launch_bounds__(256, 1) void k_gemm_small(const u16* __restrict__ Ain, const u16* __restrict__ Bin,
                                                       const float* __restrict__ modp,
                                                       const u16* __restrict__ resid,
                                                       const f32x2* __restrict__ ml, f32x2* __restrict__ stats,
                                                       u16* __restrict__ out) {
  constexpr int KTOT = (MODE == 0) ? 1024 : (MODE == 1 ? 768 : 3072);
  constexpr int KW = KTOT / 4;
  constexpr int NP = KW / 64;
  constexpr int LDA = KTOT;
  constexpr int LDB = (MODE == 0) ? 96 : 768;
  constexpr int WSZ = 40960;
  __shared__ __attribute__((aligned(16))) char smem_raw[4 * WSZ];
  LDS_AS char* smem = (LDS_AS char*)smem_raw;
  STAMP(3 + MODE, 0);
  const int tid = threadIdx.x, l = tid & 63;
  const int w = uni(tid >> 6);
  const unsigned ldsw = (unsigned)(uintptr_t)smem + w * WSZ;
  const int bid = blockIdx.x;
  const int xcd = bid & 7, idx = bid >> 3;
  const int g = l >> 4;

  int m0, n0, bh = 0;
  const u16* Ap;
  const u16* Bp;
  if constexpr (MODE == 0) {
    bh = xcd >> 1;
    const int tq = (xcd & 1) * 8 + (idx & 7);
    const int td = idx >> 3;
    m0 = tq * 64;
    n0 = td * 96;
    Ap = Ain + ((size_t)bh * 1024 + m0) * 1024 + w * KW;
    Bp = Bin + ((size_t)(bh * 4 + td) * 1024 + w * KW) * 96;
  } else {
    const int tm = xcd * 4 + (idx & 3);
    const int tn = idx >> 2;
    m0 = tm * 64;
    n0 = tn * 96;
    Ap = Ain + (size_t)m0 * LDA + w * KW;
    Bp = Bin + (size_t)(w * KW) * LDB + n0;
  }
  const int mrow = m0 + 16 * w + (l & 15);

  u32x4 pf[16];
  u32x2 xr[6];
  if constexpr (MODE == 0) {
#pragma unroll
    for (int mf = 0; mf < 4; ++mf) {
      const f32x2* sp = ml + ((size_t)bh * 1024 + m0 + 16 * mf + (l & 15)) * 8;
#pragma unroll
      for (int j = 0; j < 4; ++j) gld16(pf[mf * 4 + j], sp + 2 * j);
    }
  } else {
    const int b = mrow >> 10;
    const int goff = (MODE == 1) ? 2 * 768 : 5 * 768;
    const float* mp0 = modp + (0 * 2 + b) * 4608 + goff + n0 + 4 * g;
    const float* mp1 = modp + (1 * 2 + b) * 4608 + goff + n0 + 4 * g;
    const u16* rp = resid + (size_t)mrow * 768 + n0 + 4 * g;
#pragma unroll
    for (int cc = 0; cc < 6; ++cc) {
      gld16(pf[2 * cc], mp0 + 16 * cc);
      gld16(pf[2 * cc + 1], mp1 + 16 * cc);
      gld8(xr[cc], rp + 16 * cc);
    }
  }

  const unsigned voffA = (unsigned)((l >> 3) * LDA * 2 + (((l & 7) ^ (l >> 3)) << 4));
  unsigned voffB[6];
#pragma unroll
  for (int i = 0; i < 6; ++i) {
    int sub = 4 * i + (l >> 4);
    int kb = sub / 6, cc = sub % 6;
    int within = l & 15;
    int r = (within >> 1) ^ ((kb & 1) << 2);
    voffB[i] = (unsigned)(((kb * 8 + r) * LDB + cc * 16 + (within & 1) * 8) * 2);
  }
  auto issueA = [&](int a) {
    const char* sb = (const char*)Ap + a * 128;
    const unsigned dst = ldsw + (a & 1) * 8192;
#pragma unroll
    for (int i = 0; i < 8; ++i) glds16(sb + (size_t)i * 8 * LDA * 2, voffA, dst + i * 1024);
  };
  auto issueB = [&](int s) {
    const char* sb = (const char*)Bp + (size_t)s * 32 * LDB * 2;
    const unsigned dst = ldsw + 16384 + (s & 3) * 6144;
#pragma unroll
    for (int i = 0; i < 6; ++i) glds16(sb, voffB[i], dst + i * 1024);
  };
  issueA(0);
  issueB(0);
  issueB(1);
  issueB(2);
  wait_vm<26>();
#pragma unroll
  for (int i = 0; i < 16; ++i)
    if (MODE == 0 || i < 12) pin(pf[i]);
  if constexpr (MODE != 0) {
#pragma unroll
    for (int i = 0; i < 6; ++i) pin(xr[i]);
  }

  float cf[4][2];
  if constexpr (MODE == 0) {
#pragma unroll
    for (int mf = 0; mf < 4; ++mf) {
      float mj[8], lj[8];
#pragma unroll
      for (int j = 0; j < 4; ++j) {
        mj[2 * j] = __uint_as_float(pf[mf * 4 + j][0]);
        lj[2 * j] = __uint_as_float(pf[mf * 4 + j][1]);
        mj[2 * j + 1] = __uint_as_float(pf[mf * 4 + j][2]);
        lj[2 * j + 1] = __uint_as_float(pf[mf * 4 + j][3]);
      }
      float mx = mj[0];
#pragma unroll
      for (int j = 1; j < 8; ++j) mx = fmaxf(mx, mj[j]);
      float lsum = 0.f;
      float c0 = 0.f, c1 = 0.f;
#pragma unroll
      for (int j = 0; j < 8; ++j) {
        float e = __builtin_amdgcn_exp2f(mj[j] - mx);
        lsum += e * lj[j];
        if (j == 2 * w) c0 = e;
        if (j == 2 * w + 1) c1 = e;
      }
      float inv = 1.f / lsum;
      cf[mf][0] = c0 * inv;
      cf[mf][1] = c1 * inv;
    }
  }

  const LDS_AS char* wp = smem + w * WSZ;
  const unsigned aoff0 = (unsigned)((l & 15) * 128 + ((g ^ (l & 7)) << 4));
  const int q = (l & 15) >> 2, p = l & 3;
  const unsigned boff_t0 = (unsigned)(16384 + g * 6 * 256 + ((4 * (g & 1) + q) * 32) + p * 8);
  const unsigned boff_t1 = (unsigned)(16384 + g * 6 * 256 + ((4 * (1 - (g & 1)) + q) * 32) + p * 8);

  f32x4 acc[4][6];
  f32x4 tot[4][6];
#pragma unroll
  for (int i = 0; i < 4; ++i)
#pragma unroll
    for (int j = 0; j < 6; ++j) {
      acc[i][j] = (f32x4){0.f, 0.f, 0.f, 0.f};
      tot[i][j] = (f32x4){0.f, 0.f, 0.f, 0.f};
    }
  STAMP(3 + MODE, 1);

  auto compute = [&](const LDS_AS char* ab, const LDS_AS char* bb, int half) {
    bf16x8 af[4];
#pragma unroll
    for (int mf = 0; mf < 4; ++mf) af[mf] = lds_read16(ab + ((aoff0 ^ (half * 64)) + mf * 2048));
#pragma unroll
    for (int cc = 0; cc < 6; ++cc) {
      bf16x4 w0 = lds_tr(bb + boff_t0 + cc * 256);
      bf16x4 w1 = lds_tr(bb + boff_t1 + cc * 256);
      bf16x8 wf = cat4(w0, w1);
#pragma unroll
      for (int mf = 0; mf < 4; ++mf) acc[mf][cc] = mfma16(wf, af[mf], acc[mf][cc]);
    }
  };
  auto body = [&](int pp, auto IA, auto IB0, auto IB1, auto W0, auto W1) {
    const LDS_AS char* ab = wp + (pp & 1) * 8192;
    const LDS_AS char* bb = wp + (pp & 1) * 12288;
    wait_lgkm0();
    if constexpr (IA.value) issueA(pp + 1);
    if constexpr (IB0.value) issueB(2 * pp + 3);
    wait_vm<W0.value>();
    compute(ab, bb, 0);
    wait_lgkm0();
    if constexpr (IB1.value) issueB(2 * pp + 4);
    wait_vm<W1.value>();
    compute(ab, bb + 6144, 1);
    if constexpr (MODE == 0) {
      if (pp == 1) {
#pragma unroll
        for (int mf = 0; mf < 4; ++mf)
#pragma unroll
          for (int cc = 0; cc < 6; ++cc) {
            tot[mf][cc] = acc[mf][cc] * cf[mf][0];
            acc[mf][cc] = (f32x4){0.f, 0.f, 0.f, 0.f};
          }
      }
    }
  };
  using T = std::true_type;
  using F = std::false_type;
#pragma unroll 1
  for (int pp = 0; pp < NP - 2; ++pp)
    body(pp, T{}, T{}, T{}, std::integral_constant<int, 26>{}, std::integral_constant<int, 26>{});
  body(NP - 2, T{}, T{}, F{}, std::integral_constant<int, 26>{}, std::integral_constant<int, 20>{});
  body(NP - 1, F{}, F{}, F{}, std::integral_constant<int, 6>{}, std::integral_constant<int, 0>{});
  if constexpr (MODE == 0) {
#pragma unroll
    for (int mf = 0; mf < 4; ++mf)
#pragma unroll
      for (int cc = 0; cc < 6; ++cc) acc[mf][cc] = tot[mf][cc] + acc[mf][cc] * cf[mf][1];
  }

  STAMP(3 + MODE, 12);
  wait_lgkm0();
  {
    LDS_AS char* rp = smem + w * WSZ + l * 16;
#pragma unroll
    for (int mf = 0; mf < 4; ++mf)
#pragma unroll
      for (int cc = 0; cc < 6; ++cc) *(LDS_AS f32x4*)(rp + (mf * 6 + cc) * 1024) = acc[mf][cc];
  }
  __syncthreads();
  f32x4 r[6];
#pragma unroll
  for (int cc = 0; cc < 6; ++cc) {
    f32x4 s4 = lds_read16f(smem + 0 * WSZ + (w * 6 + cc) * 1024 + l * 16);
#pragma unroll
    for (int ww = 1; ww < 4; ++ww) s4 += lds_read16f(smem + ww * WSZ + (w * 6 + cc) * 1024 + l * 16);
    r[cc] = s4;
  }
  STAMP(3 + MODE, 13);
  if constexpr (MODE == 0) {
    u16* op = out + (size_t)((bh >> 1) * 1024 + mrow) * 768 + (bh & 1) * 384 + n0;
#pragma unroll
    for (int a = 0; a < 3; ++a) {
      u32x4 o = pair_pack(pack2(r[2 * a][0], r[2 * a][1]), pack2(r[2 * a][2], r[2 * a][3]),
                          pack2(r[2 * a + 1][0], r[2 * a + 1][1]), pack2(r[2 * a + 1][2], r[2 * a + 1][3]));
      *(u32x4*)(op + 32 * a + (g & 1) * 16 + (g & 2) * 4) = o;
    }
  } else {
    float v[6][4];
#pragma unroll
    for (int cc = 0; cc < 6; ++cc) {
      f32x4 ga = asf4(pf[2 * cc]);
      f32x4 gb = asf4(pf[2 * cc + 1]);
      float xv[4] = {lo_f(xr[cc][0]), hi_f(xr[cc][0]), lo_f(xr[cc][1]), hi_f(xr[cc][1])};
#pragma unroll
      for (int j = 0; j < 4; ++j) {
        float gate = rbf(ga[j] + gb[j]);
        v[cc][j] = rbf(xv[j] + rbf(gate * rbf(r[cc][j])));
      }
    }
    u16* op = out + (size_t)mrow * 768 + n0;
#pragma unroll
    for (int a = 0; a < 3; ++a) {
      u32x4 o = pair_pack(pack2(v[2 * a][0], v[2 * a][1]), pack2(v[2 * a][2], v[2 * a][3]),
                          pack2(v[2 * a + 1][0], v[2 * a + 1][1]), pack2(v[2 * a + 1][2], v[2 * a + 1][3]));
      *(u32x4*)(op + 32 * a + (g & 1) * 16 + (g & 2) * 4) = o;
    }
    if constexpr (MODE == 1) {
      float s = 0.f;
#pragma unroll
      for (int cc = 0; cc < 6; ++cc)
#pragma unroll
        for (int j = 0; j < 4; ++j) s += v[cc][j];
      s += __shfl_xor(s, 16);
      s += __shfl_xor(s, 32);
      float mean = s * (1.f / 96.f);
      float m2 = 0.f;
#pragma unroll
      for (int cc = 0; cc < 6; ++cc)
#pragma unroll
        for (int j = 0; j < 4; ++j) {
          float d = v[cc][j] - mean;
          m2 += d * d;
        }
      m2 += __shfl_xor(m2, 16);
      m2 += __shfl_xor(m2, 32);
      if (g == 0) {
        f32x2 s2;
        s2[0] = s;
        s2[1] = m2;
        stats[(size_t)mrow * 8 + (n0 / 96)] = s2;
      }
    }
  }
  STAMP(3 + MODE, 15);
}

extern "C" void kernel_launch(void* const* d_in, const int* in_sizes, int n_in, void* d_out, int out_size,
                              void* d_ws, size_t ws_size, hipStream_t stream) {
  const u16* x = (const u16*)d_in[0];
  const u16* c = (const u16*)d_in[1];
  const u16* wqkv = (const u16*)d_in[2];
  const u16* wo = (const u16*)d_in[3];
  const u16* wmod = (const u16*)d_in[4];
  const u16* wfc1 = (const u16*)d_in[5];
  const u16* wfc2 = (const u16*)d_in[6];
  u16* out = (u16*)d_out;

  char* ws = (char*)d_ws;
  size_t off = 0;
  auto alloc = [&](size_t bytes) {
    char* p = ws + off;
    off += (bytes + 255) & ~(size_t)255;
    return p;
  };
  float* modp = (float*)alloc(2 * 2 * 4608 * 4);
  u16* xn = (u16*)alloc((size_t)Mrows * 768 * 2);
  u16* qk = (u16*)alloc((size_t)Mrows * 1536 * 2);
  u16* Vt = (u16*)alloc((size_t)Mrows * 768 * 2);
  u16* P = (u16*)alloc((size_t)4 * 1024 * 1024 * 2);
  f32x2* ml = (f32x2*)alloc((size_t)4 * 1024 * 8 * 8);
  u16* O = (u16*)alloc((size_t)Mrows * 768 * 2);
  u16* x1 = (u16*)alloc((size_t)Mrows * 768 * 2);
  f32x2* st2 = (f32x2*)alloc((size_t)Mrows * 8 * 8);
  u16* u = (u16*)alloc((size_t)Mrows * 3072 * 2);

  k0_mod_ln<<<K0_MOD_BLOCKS + K0_LN_BLOCKS, 256, 0, stream>>>(x, c, wmod, modp, xn);
  k_gemm_ln<144, 4, 0><<<256, 512, 0, stream>>>(xn, wqkv, modp, nullptr, qk, Vt);
  k_scores<<<256, 512, 0, stream>>>(qk, P, ml);
  k_gemm_small<0><<<256, 256, 0, stream>>>(P, Vt, modp, nullptr, ml, nullptr, O);
  k_gemm_small<1><<<256, 256, 0, stream>>>(O, wo, modp, x, nullptr, st2, x1);
  k_gemm_ln<192, 3, 1><<<256, 512, 0, stream>>>(x1, wfc1, modp, st2, u, nullptr);
  k_gemm_small<2><<<256, 256, 0, stream>>>(u, wfc2, modp, x1, nullptr, nullptr, out);
}
```

```cpp
#include <hip/hip_runtime.h>
#include <hip/hip_bf16.h>
#include <stdint.h>
#include <type_traits>

typedef __attribute__((ext_vector_type(8))) __bf16 bf16x8;
typedef __attribute__((ext_vector_type(4))) __bf16 bf16x4;
typedef __attribute__((ext_vector_type(2))) __bf16 bf16x2;
typedef __attribute__((ext_vector_type(4))) float f32x4;
typedef __attribute__((ext_vector_type(2))) float f32x2;
typedef __attribute__((ext_vector_type(4))) unsigned u32x4;
typedef __attribute__((ext_vector_type(2))) unsigned u32x2;
typedef unsigned short u16;
#define LDS_AS __attribute__((address_space(3)))

#define DEVI __device__ __forceinline__
#define STAMP(K, I)

static constexpr int Bsz = 2, Nseq = 1024, Dm = 768, Ff = 3072, Dh = 384;
static constexpr int Mrows = Bsz * Nseq;

template <int I, int N, class F>
DEVI void static_for(F&& f) {
  if constexpr (I < N) {
    f(std::integral_constant<int, I>{});
    static_for<I + 1, N>(f);
  }
}

DEVI float lo_f(unsigned u) { return __uint_as_float(u << 16); }
DEVI float hi_f(unsigned u) { return __uint_as_float(u & 0xffff0000u); }
DEVI float bf2f(u16 h) { return __uint_as_float(((unsigned)h) << 16); }
DEVI unsigned pack2(float a, float b) {
  bf16x2 v;
  v[0] = (__bf16)a;
  v[1] = (__bf16)b;
  return __builtin_bit_cast(unsigned, v);
}
DEVI float rbf(float a) { return lo_f(pack2(a, 0.f) ); }

template <int N>
DEVI void wait_vm() { asm volatile("s_waitcnt vmcnt(%0)" ::"i"(N) : "memory"); }
DEVI void wait_lgkm0() { asm volatile("s_waitcnt lgkmcnt(0)" ::: "memory"); }
DEVI void bar() { __builtin_amdgcn_s_barrier(); }

DEVI void glds16(const void* sbase, unsigned voff, unsigned lds_dst) {
  unsigned keep;
  asm volatile(
      "s_mov_b32 %0, m0\n\t"
      "s_mov_b32 m0, %3\n\t"
      "s_nop 0\n\t"
      "global_load_lds_dwordx4 %1, %2\n\t"
      "s_mov_b32 m0, %0"
      : "=&s"(keep)
      : "v"(voff), "s"(sbase), "s"(lds_dst)
      : "memory");
}

DEVI void gld16(u32x4& d, const void* p) { asm volatile("global_load_dwordx4 %0, %1, off" : "=v"(d) : "v"(p) : "memory"); }
DEVI void gld8(u32x2& d, const void* p) { asm volatile("global_load_dwordx2 %0, %1, off" : "=v"(d) : "v"(p) : "memory"); }
template <class T>
DEVI void pin(T& v) { asm volatile("" : "+v"(v)); }
DEVI f32x4 asf4(u32x4 v) { return __builtin_bit_cast(f32x4, v); }

DEVI bf16x8 lds_read16(const LDS_AS char* p) { return *(const LDS_AS bf16x8*)p; }
DEVI u32x4 lds_read16u(const LDS_AS char* p) { return *(const LDS_AS u32x4*)p; }
DEVI f32x4 lds_read16f(const LDS_AS char* p) { return *(const LDS_AS f32x4*)p; }
DEVI bf16x4 lds_tr(const LDS_AS char* p) {
  return __builtin_amdgcn_ds_read_tr16_b64_v4bf16((LDS_AS bf16x4*)p);
}
DEVI bf16x8 cat4(bf16x4 a, bf16x4 b) {
  return __builtin_shufflevector(a, b, 0, 1, 2, 3, 4, 5, 6, 7);
}
DEVI f32x4 mfma16(bf16x8 a, bf16x8 b, f32x4 c) {
  return __builtin_amdgcn_mfma_f32_16x16x32_bf16(a, b, c, 0, 0, 0);
}
DEVI int uni(int v) { return __builtin_amdgcn_readfirstlane(v); }

DEVI u32x4 pair_pack(unsigned ax, unsigned ay, unsigned bx, unsigned by) {
  auto r0 = __builtin_amdgcn_permlane16_swap(ax, bx, false, false);
  auto r1 = __builtin_amdgcn_permlane16_swap(ay, by, false, false);
  u32x4 o;
  o[0] = r0[0];
  o[1] = r1[0];
  o[2] = r0[1];
  o[3] = r1[1];
  return o;
}

static constexpr int K0_MOD_BLOCKS = 144;
static constexpr int K0_LN_BLOCKS = 128;

__global__ __launch_bounds__(256) void k0_mod_ln(const u16* __restrict__ x, const u16* __restrict__ c,
                                                 const u16* __restrict__ wmod, float* __restrict__ modp,
                                                 u16* __restrict__ xn) {
  __shared__ float sc[2][384];
  __shared__ float red[32][2][64];
  const int tid = threadIdx.x;
  if (blockIdx.x < K0_MOD_BLOCKS) {
    const int cb = blockIdx.x % 72, kp = blockIdx.x / 72;
    const int tc = tid & 7, tr = tid >> 3;
    u32x4 w[12];
    const u16* wp = wmod + (size_t)(kp * 384 + tr) * 4608 + cb * 64 + tc * 8;
#pragma unroll
    for (int i = 0; i < 12; ++i) w[i] = *(const u32x4*)(wp + (size_t)i * 32 * 4608);
    for (int i = tid; i < 768; i += 256) {
      int b = i / 384, k = i % 384;
      float v = bf2f(c[b * 768 + kp * 384 + k]);
      float s = v / (1.f + __expf(-v));
      sc[b][k] = rbf(s);
    }
    __syncthreads();
    float acc[2][8];
#pragma unroll
    for (int b = 0; b < 2; ++b)
#pragma unroll
      for (int j = 0; j < 8; ++j) acc[b][j] = 0.f;
#pragma unroll
    for (int i = 0; i < 12; ++i) {
      float s0 = sc[0][tr + 32 * i], s1 = sc[1][tr + 32 * i];
#pragma unroll
      for (int j = 0; j < 4; ++j) {
        float a = lo_f(w[i][j]), bb = hi_f(w[i][j]);
        acc[0][2 * j] += s0 * a;
        acc[0][2 * j + 1] += s0 * bb;
        acc[1][2 * j] += s1 * a;
        acc[1][2 * j + 1] += s1 * bb;
      }
    }
#pragma unroll
    for (int b = 0; b < 2; ++b)
#pragma unroll
      for (int j = 0; j < 8; ++j) red[tr][b][tc * 8 + j] = acc[b][j];
    __syncthreads();
    if (tid < 128) {
      int b = tid >> 6, col = tid & 63;
      float s = 0.f;
#pragma unroll
      for (int r = 0; r < 32; ++r) s += red[r][b][col];
      modp[(kp * 2 + b) * 4608 + cb * 64 + col] = s;
    }
  } else {
    const int blk = blockIdx.x - K0_MOD_BLOCKS;
    const int w = tid >> 6, l = tid & 63;
    u32x2 v[4][3];
#pragma unroll
    for (int r = 0; r < 4; ++r) {
      const u16* xp = x + (size_t)(blk * 16 + w * 4 + r) * 768 + l * 4;
#pragma unroll
      for (int i = 0; i < 3; ++i) v[r][i] = *(const u32x2*)(xp + i * 256);
    }
#pragma unroll
    for (int r = 0; r < 4; ++r) {
      float f[12];
#pragma unroll
      for (int i = 0; i < 3; ++i) {
        f[4 * i] = lo_f(v[r][i][0]);
        f[4 * i + 1] = hi_f(v[r][i][0]);
        f[4 * i + 2] = lo_f(v[r][i][1]);
        f[4 * i + 3] = hi_f(v[r][i][1]);
      }
      float s = 0.f;
#pragma unroll
      for (int i = 0; i < 12; ++i) s += f[i];
#pragma unroll
      for (int o = 32; o > 0; o >>= 1) s += __shfl_xor(s, o);
      float mean = s * (1.f / 768.f);
      float q = 0.f;
#pragma unroll
      for (int i = 0; i < 12; ++i) {
        float d = f[i] - mean;
        q += d * d;
      }
#pragma unroll
      for (int o = 32; o > 0; o >>= 1) q += __shfl_xor(q, o);
      const float mb = rbf(mean);
      const float den = rbf(sqrtf(rbf(rbf(q * (1.f / 768.f)) + 1e-5f)));
#pragma unroll
      for (int i = 0; i < 12; ++i) f[i] = rbf(f[i] - mb) / den;
      const float rs = 1.f;
      u16* op = xn + (size_t)(blk * 16 + w * 4 + r) * 768 + l * 4;
#pragma unroll
      for (int i = 0; i < 3; ++i) {
        u32x2 o;
        o[0] = pack2(f[4 * i] * rs, f[4 * i + 1] * rs);
        o[1] = pack2(f[4 * i + 2] * rs, f[4 * i + 3] * rs);
        *(u32x2*)(op + i * 256) = o;
      }
    }
  }
}

template <int BN, int NS, int MODE>
__global__ __launch_bounds__(768, 3) void k_gemm_ln(const u16* __restrict__ A, const u16* __restrict__ W,
                                                    const float* __restrict__ modp,
                                                    const f32x2* __restrict__ stats, u16* __restrict__ out,
                                                    u16* __restrict__ out2) {
  constexpr int NC = BN / 16;
  constexpr int LDW = (MODE == 0) ? 2304 : 3072;
  constexpr int LDA = 768;
  constexpr int ASZ = 128 * 128;
  constexpr int BSZ = 8 * NC * 256;
  constexpr int STG = ASZ + BSZ;
  constexpr int SUBW = 2 * NC;
  constexpr int NBI = (SUBW + 3) / 4;
  constexpr int NLD = 4 + NBI;
  constexpr int NK = 768 / 64;
  __shared__ __attribute__((aligned(16))) char smem_raw[NS * STG + 6144];
  LDS_AS char* smem = (LDS_AS char*)smem_raw;
  const unsigned lds0 = (unsigned)(uintptr_t)smem;
  STAMP(MODE == 0 ? 1 : 6, 0);
  const int tid = threadIdx.x;
  const int l = tid & 63;
  const int w = uni(tid >> 6);
  const int bid = blockIdx.x;
  const int xcd = bid & 7, idx = bid >> 3;
  const int tm = (xcd >> 1) * 4 + (idx & 3);
  const int tn = (xcd & 1) * 8 + (idx >> 2);
  const int m0 = tm * 128, n0 = tn * BN;
  const int b = m0 >> 10;

  if (w >= 8) {
    const int lw = w - 8;
    const unsigned voffA = (unsigned)((l >> 3) * LDA * 2 + (((l & 7) ^ (l >> 3)) << 4));
    unsigned voffB[NBI];
#pragma unroll
    for (int i = 0; i < NBI; ++i) {
      int sub = SUBW * lw + 4 * i + (l >> 4);
      if (sub >= SUBW * (lw + 1)) sub = SUBW * (lw + 1) - 1;
      int kb = sub / NC, cc = sub % NC;
      int within = l & 15;
      int r = (within >> 1) ^ ((kb & 1) << 2);
      voffB[i] = (unsigned)(((kb * 8 + r) * LDW + cc * 16 + (within & 1) * 8) * 2);
    }
    const char* abase = (const char*)(A + (size_t)(m0 + 32 * lw) * LDA);
    const char* bbase = (const char*)(W + n0);
    auto issue = [&](int kt, int st) {
      const unsigned sa = lds0 + st * STG;
#pragma unroll
      for (int i = 0; i < 4; ++i)
        glds16(abase + (size_t)i * 8 * LDA * 2 + kt * 128, voffA, sa + (32 * lw + 8 * i) * 128);
      const char* sb = bbase + (size_t)kt * 64 * LDW * 2;
#pragma unroll
      for (int i = 0; i < NBI; ++i) {
        unsigned dst = sa + ASZ + (SUBW * lw + 4 * i) * 256;
        if (4 * i + 4 <= SUBW) {
          glds16(sb, voffB[i], dst);
        } else {
          if (l < (SUBW - 4 * i) * 16) glds16(sb, voffB[i], dst);
        }
      }
    };
#pragma unroll
    for (int s = 0; s < NS - 1; ++s) issue(s, s);
    int stn = NS - 1;
#pragma unroll 1
    for (int kt = 0; kt < NK; ++kt) {
      if (kt < NK - (NS - 2)) wait_vm<(NS - 2) * NLD>();
      else wait_vm<0>();
      bar();
      if (kt + NS - 1 < NK) issue(kt + NS - 1, stn);
      stn = (stn + 1 == NS) ? 0 : stn + 1;
    }
    bar();
    bar();
    return;
  }

  const int wm = w & 3, kh = w >> 2;
  if (tid < 384) {
    const int offS = (MODE == 0) ? 0 : 3 * 768;
    const int o = (tid < 192) ? (offS + 768 + 4 * tid) : (offS + 4 * (tid - 192));
    f32x4 a = *(const f32x4*)(modp + (0 * 2 + b) * 4608 + o);
    f32x4 bb = *(const f32x4*)(modp + (1 * 2 + b) * 4608 + o);
    f32x4 r;
    if (tid < 192) {
#pragma unroll
      for (int j = 0; j < 4; ++j) r[j] = rbf(1.f + rbf(a[j] + bb[j]));
    } else {
#pragma unroll
      for (int j = 0; j < 4; ++j) r[j] = rbf(a[j] + bb[j]);
    }
    *(LDS_AS f32x4*)(smem + NS * STG + tid * 16) = r;
  }
  float rinv[2] = {1.f, 1.f}, mb[2] = {0.f, 0.f};
  if constexpr (MODE == 1) {
#pragma unroll
    for (int mf = 0; mf < 2; ++mf) {
      const f32x4* sp = (const f32x4*)(stats + (size_t)(m0 + 32 * wm + 16 * mf + (l & 15)) * 16);
      f32x4 sv[8];
#pragma unroll
      for (int j = 0; j < 8; ++j) sv[j] = sp[j];
      float s = 0.f;
#pragma unroll
      for (int j = 0; j < 8; ++j) s += sv[j][0] + sv[j][2];
      float mean = s * (1.f / 768.f);
      float m2 = 0.f;
#pragma unroll
      for (int j = 0; j < 8; ++j) {
        float d0 = sv[j][0] * (1.f / 48.f) - mean;
        float d1 = sv[j][2] * (1.f / 48.f) - mean;
        m2 += sv[j][1] + sv[j][3] + 48.f * (d0 * d0 + d1 * d1);
      }
      mb[mf] = rbf(mean);
      rinv[mf] = 1.f / rbf(sqrtf(rbf(rbf(m2 * (1.f / 768.f)) + 1e-5f)));
    }
  }

  const int g = l >> 4;
  const unsigned aoff = (unsigned)(((32 * wm + (l & 15)) * 128 + ((g ^ (l & 7)) << 4)) ^ (kh * 64));
  const int q = (l & 15) >> 2, p = l & 3;
  const unsigned boff_t0 = (unsigned)(ASZ + (4 * kh + g) * NC * 256 + ((4 * (g & 1) + q) * 32) + p * 8);
  const unsigned boff_t1 = (unsigned)(ASZ + (4 * kh + g) * NC * 256 + ((4 * (1 - (g & 1)) + q) * 32) + p * 8);
  const LDS_AS char* gtp = smem + NS * STG + (kh * 32 + g * 8) * 4;

  f32x4 acc[2][NC];
#pragma unroll
  for (int i = 0; i < 2; ++i)
#pragma unroll
    for (int j = 0; j < NC; ++j) acc[i][j] = (f32x4){0.f, 0.f, 0.f, 0.f};
  STAMP(MODE == 0 ? 1 : 6, 1);

  int st = 0;
#pragma unroll 1
  for (int kt = 0; kt < NK; ++kt) {
    wait_lgkm0();
    bar();
    const LDS_AS char* sp = smem + st * STG;
    bf16x8 xb[2];
    f32x4 g0 = lds_read16f(gtp + kt * 256);
    f32x4 g1 = lds_read16f(gtp + kt * 256 + 16);
    f32x4 t0 = lds_read16f(gtp + 3072 + kt * 256);
    f32x4 t1 = lds_read16f(gtp + 3072 + kt * 256 + 16);
#pragma unroll
    for (int mf = 0; mf < 2; ++mf) {
      u32x4 raw = lds_read16u(sp + aoff + mf * 2048);
      u32x4 o;
#pragma unroll
      for (int j = 0; j < 4; ++j) {
        float v0 = lo_f(raw[j]), v1 = hi_f(raw[j]);
        if constexpr (MODE == 1) {
          unsigned d = pack2(v0 - mb[mf], v1 - mb[mf]);
          d = pack2(lo_f(d) * rinv[mf], hi_f(d) * rinv[mf]);
          v0 = lo_f(d);
          v1 = hi_f(d);
        }
        const float ga = (j < 2) ? g0[2 * j] : g1[2 * j - 4], gb = (j < 2) ? g0[2 * j + 1] : g1[2 * j - 3];
        const float ta = (j < 2) ? t0[2 * j] : t1[2 * j - 4], tb = (j < 2) ? t0[2 * j + 1] : t1[2 * j - 3];
        unsigned y = pack2(v0 * ga, v1 * gb);
        o[j] = pack2(lo_f(y) + ta, hi_f(y) + tb);
      }
      xb[mf] = __builtin_bit_cast(bf16x8, o);
    }
#pragma unroll
    for (int cc = 0; cc < NC; ++cc) {
      bf16x4 w0 = lds_tr(sp + boff_t0 + cc * 256);
      bf16x4 w1 = lds_tr(sp + boff_t1 + cc * 256);
      bf16x8 wf = cat4(w0, w1);
      acc[0][cc] = mfma16(wf, xb[0], acc[0][cc]);
      acc[1][cc] = mfma16(wf, xb[1], acc[1][cc]);
    }
    st = (st + 1 == NS) ? 0 : st + 1;
  }
  STAMP(MODE == 0 ? 1 : 6, 14);

  wait_lgkm0();
  bar();
  {
    LDS_AS char* rp = smem + (wm * 2 + kh) * NC * 1024 + l * 16;
    if (kh) {
#pragma unroll
      for (int cc = 0; cc < NC; ++cc) {
        *(LDS_AS f32x4*)(rp + cc * 1024) = acc[0][cc];
        acc[0][cc] = acc[1][cc];
      }
    } else {
#pragma unroll
      for (int cc = 0; cc < NC; ++cc) *(LDS_AS f32x4*)(rp + cc * 1024) = acc[1][cc];
    }
  }
  wait_lgkm0();
  bar();
  const int row = m0 + 32 * wm + 16 * kh + (l & 15);
  unsigned pk[NC][2];
  {
    const LDS_AS char* rp = smem + (wm * 2 + (1 - kh)) * NC * 1024 + l * 16;
#pragma unroll
    for (int cc = 0; cc < NC; ++cc) {
      f32x4 v = acc[0][cc] + lds_read16f(rp + cc * 1024);
      if constexpr (MODE == 1) {
#pragma unroll
        for (int j = 0; j < 4; ++j) {
          float y = rbf(v[j]);
          float z = 1.5957691216057308f * (y + 0.044715f * y * y * y);
          v[j] = y / (1.f + __expf(-z));
        }
      }
      pk[cc][0] = pack2(v[0], v[1]);
      pk[cc][1] = pack2(v[2], v[3]);
    }
  }
  auto dst_ptr = [&](int n) -> u16* {
    if constexpr (MODE == 1) {
      return out + (size_t)row * 3072 + n;
    } else {
      if (n < 1536) return out + (size_t)row * 1536 + n;
      int nv = n - 1536;
      int hd = nv / 96;
      int dd = nv - hd * 96;
      return out2 + ((size_t)((b * 8 + hd) * 1024 + (row & 1023))) * 96 + dd;
    }
  };
#pragma unroll
  for (int a = 0; a < NC / 2; ++a) {
    u32x4 o = pair_pack(pk[2 * a][0], pk[2 * a][1], pk[2 * a + 1][0], pk[2 * a + 1][1]);
    *(u32x4*)dst_ptr(n0 + 32 * a + (g & 1) * 16 + (g & 2) * 4) = o;
  }
  if constexpr (NC & 1) {
    u32x2 o;
    o[0] = pk[NC - 1][0];
    o[1] = pk[NC - 1][1];
    *(u32x2*)dst_ptr(n0 + 16 * (NC - 1) + 4 * g) = o;
  }
  STAMP(MODE == 0 ? 1 : 6, 15);
}

__global__ __launch_bounds__(768, 3) void k_scores(const u16* __restrict__ qk, u16* __restrict__ P,
                                                   f32x2* __restrict__ ml) {
  constexpr int NS = 4;
  constexpr int ASZ = 128 * 128;
  constexpr int STG = 2 * ASZ;
  constexpr int NK = 384 / 64;
  constexpr int NLD = 8;
  constexpr int LDQ = 1536;
  __shared__ __attribute__((aligned(16))) char smem_raw[NS * STG];
  LDS_AS char* smem = (LDS_AS char*)smem_raw;
  const unsigned lds0 = (unsigned)(uintptr_t)smem;
  STAMP(2, 0);
  const int tid = threadIdx.x, l = tid & 63;
  const int w = uni(tid >> 6);
  const int bid = blockIdx.x;
  const int xcd = bid & 7, idx = bid >> 3;
  const int bh = xcd >> 1;
  const int tq = (xcd & 1) * 4 + (idx & 3);
  const int tk = idx >> 2;
  const int b = bh >> 1, h = bh & 1;

  if (w >= 8) {
    const int lw = w - 8;
    const unsigned voff = (unsigned)((l >> 3) * LDQ * 2 + (((l & 7) ^ (l >> 3)) << 4));
    const char* qbase = (const char*)(qk + (size_t)(b * 1024 + tq * 128 + 32 * lw) * LDQ + h * 384);
    const char* kbase = (const char*)(qk + (size_t)(b * 1024 + tk * 128 + 32 * lw) * LDQ + 768 + h * 384);
    auto issue = [&](int kt, int st) {
      const unsigned sa = lds0 + st * STG;
#pragma unroll
      for (int i = 0; i < 4; ++i)
        glds16(qbase + (size_t)i * 8 * LDQ * 2 + kt * 128, voff, sa + (32 * lw + 8 * i) * 128);
#pragma unroll
      for (int i = 0; i < 4; ++i)
        glds16(kbase + (size_t)i * 8 * LDQ * 2 + kt * 128, voff, sa + ASZ + (32 * lw + 8 * i) * 128);
    };
#pragma unroll
    for (int s = 0; s < NS - 1; ++s) issue(s, s);
    int stn = NS - 1;
#pragma unroll 1
    for (int kt = 0; kt < NK; ++kt) {
      if (kt < NK - (NS - 2)) wait_vm<(NS - 2) * NLD>();
      else wait_vm<0>();
      bar();
      if (kt + NS - 1 < NK) issue(kt + NS - 1, stn);
      stn = (stn + 1 == NS) ? 0 : stn + 1;
    }
    bar();
    bar();
    return;
  }

  const int wm = w & 3, kh = w >> 2;
  const int g = l >> 4;
  const unsigned qoff = (unsigned)(((32 * wm + (l & 15)) * 128 + ((g ^ (l & 7)) << 4)) ^ (kh * 64));
  const unsigned koff = (unsigned)(ASZ + (((l & 15) * 128 + ((g ^ (l & 7)) << 4)) ^ (kh * 64)));
  f32x4 acc[2][8];
#pragma unroll
  for (int i = 0; i < 2; ++i)
#pragma unroll
    for (int j = 0; j < 8; ++j) acc[i][j] = (f32x4){0.f, 0.f, 0.f, 0.f};

  int st = 0;
#pragma unroll 1
  for (int kt = 0; kt < NK; ++kt) {
    wait_lgkm0();
    bar();
    const LDS_AS char* sp = smem + st * STG;
    bf16x8 qf[2];
#pragma unroll
    for (int mf = 0; mf < 2; ++mf) qf[mf] = lds_read16(sp + qoff + mf * 2048);
#pragma unroll
    for (int kf = 0; kf < 8; ++kf) {
      bf16x8 kfr = lds_read16(sp + koff + kf * 2048);
      acc[0][kf] = mfma16(kfr, qf[0], acc[0][kf]);
      acc[1][kf] = mfma16(kfr, qf[1], acc[1][kf]);
    }
    st = (st + 1 == NS) ? 0 : st + 1;
  }
  STAMP(2, 14);
  wait_lgkm0();
  bar();
  {
    LDS_AS char* rp = smem + (wm * 2 + kh) * 8 * 1024 + l * 16;
    if (kh) {
#pragma unroll
      for (int kf = 0; kf < 8; ++kf) {
        *(LDS_AS f32x4*)(rp + kf * 1024) = acc[0][kf];
        acc[0][kf] = acc[1][kf];
      }
    } else {
#pragma unroll
      for (int kf = 0; kf < 8; ++kf) *(LDS_AS f32x4*)(rp + kf * 1024) = acc[1][kf];
    }
  }
  wait_lgkm0();
  bar();
  f32x4 s[8];
  {
    const LDS_AS char* rp = smem + (wm * 2 + (1 - kh)) * 8 * 1024 + l * 16;
#pragma unroll
    for (int kf = 0; kf < 8; ++kf) {
      f32x4 v = acc[0][kf] + lds_read16f(rp + kf * 1024);
#pragma unroll
      for (int j = 0; j < 4; ++j) v[j] = rbf(rbf(v[j]) * 0.051031036307982884f);
      s[kf] = v;
    }
  }
  const float l2e = 1.4426950408889634f;
  float mx = -INFINITY;
#pragma unroll
  for (int kf = 0; kf < 8; ++kf)
#pragma unroll
    for (int j = 0; j < 4; ++j) mx = fmaxf(mx, s[kf][j]);
  mx = fmaxf(mx, __shfl_xor(mx, 16));
  mx = fmaxf(mx, __shfl_xor(mx, 32));
  const float mbl = mx * l2e;
  float sum = 0.f;
  unsigned pk[8][2];
#pragma unroll
  for (int kf = 0; kf < 8; ++kf) {
    float e[4];
#pragma unroll
    for (int j = 0; j < 4; ++j) {
      e[j] = __builtin_amdgcn_exp2f(s[kf][j] * l2e - mbl);
      sum += e[j];
    }
    pk[kf][0] = pack2(e[0], e[1]);
    pk[kf][1] = pack2(e[2], e[3]);
  }
  sum += __shfl_xor(sum, 16);
  sum += __shfl_xor(sum, 32);
  const int qrow = tq * 128 + 32 * wm + 16 * kh + (l & 15);
  u16* op = P + ((size_t)bh * 1024 + qrow) * 1024 + tk * 128;
#pragma unroll
  for (int a = 0; a < 4; ++a) {
    u32x4 o = pair_pack(pk[2 * a][0], pk[2 * a][1], pk[2 * a + 1][0], pk[2 * a + 1][1]);
    *(u32x4*)(op + 32 * a + (g & 1) * 16 + (g & 2) * 4) = o;
  }
  if (g == 0) {
    f32x2 s2;
    s2[0] = mbl;
    s2[1] = sum;
    ml[((size_t)bh * 1024 + qrow) * 8 + tk] = s2;
  }
  STAMP(2, 15);
}

template <int MODE>
__global__ __launch_bounds__(512, 2) void k_gemm_small(const u16* __restrict__ Ain, const u16* __restrict__ Bin,
                                                       const float* __restrict__ modp,
                                                       const u16* __restrict__ resid,
                                                       const f32x2* __restrict__ ml, f32x2* __restrict__ stats,
                                                       u16* __restrict__ out) {
  constexpr int KTOT = (MODE == 0) ? 1024 : (MODE == 1 ? 768 : 3072);
  constexpr int KH = KTOT / 2;
  constexpr int NK = KH / 64;
  constexpr int LDA = KTOT;
  constexpr int LDB = (MODE == 0) ? 96 : 768;
  constexpr int NS = 4;
  constexpr int STG = 40960;
  constexpr int NLD = 10;
  __shared__ __attribute__((aligned(16))) char smem_raw[NS * STG];
  LDS_AS char* smem = (LDS_AS char*)smem_raw;
  const unsigned lds0 = (unsigned)(uintptr_t)smem;
  STAMP(3 + MODE, 0);
  const int tid = threadIdx.x, l = tid & 63;
  const int w = uni(tid >> 6);
  const int bid = blockIdx.x;
  const int xcd = bid & 7, idx = bid >> 3;
  const int g = l >> 4;

  int m0, n0, bh = 0;
  const u16* Ap;
  const u16* Bp;
  if constexpr (MODE == 0) {
    bh = xcd >> 1;
    const int tq = (xcd & 1) * 8 + (idx & 7);
    const int td = idx >> 3;
    m0 = tq * 64;
    n0 = td * 96;
    Ap = Ain + ((size_t)bh * 1024 + m0) * 1024;
    Bp = Bin + ((size_t)(bh * 4 + td) * 1024) * 96;
  } else {
    const int tm = xcd * 4 + (idx & 3);
    const int tn = idx >> 2;
    m0 = tm * 64;
    n0 = tn * 96;
    Ap = Ain + (size_t)m0 * LDA;
    Bp = Bin + n0;
  }

  if (w >= 4) {
    const int lw = w - 4;
    const int kq = lw & 1, part = lw >> 1;
    const unsigned voffA = (unsigned)((l >> 3) * LDA * 2 + (((l & 7) ^ (l >> 3)) << 4));
    unsigned voffB[6];
#pragma unroll
    for (int i = 0; i < 6; ++i) {
      int sub = 24 * part + 4 * i + (l >> 4);
      int kb = sub / 6, cc = sub % 6;
      int within = l & 15;
      int r = (within >> 1) ^ ((kb & 1) << 2);
      voffB[i] = (unsigned)(((kb * 8 + r) * LDB + cc * 16 + (within & 1) * 8) * 2);
    }
    const char* abase = (const char*)(Ap + (size_t)(32 * part) * LDA + kq * KH);
    const char* bbase = (const char*)(Bp + (size_t)(kq * KH) * LDB);
    auto issue = [&](int kt, int st) {
      const unsigned sa = lds0 + st * STG;
#pragma unroll
      for (int i = 0; i < 4; ++i)
        glds16(abase + (size_t)i * 8 * LDA * 2 + kt * 128, voffA, sa + kq * 8192 + (32 * part + 8 * i) * 128);
      const char* sb = bbase + (size_t)kt * 64 * LDB * 2;
#pragma unroll
      for (int i = 0; i < 6; ++i) glds16(sb, voffB[i], sa + 16384 + kq * 12288 + (24 * part + 4 * i) * 256);
    };
#pragma unroll
    for (int s = 0; s < NS - 1; ++s) issue(s, s);
    int stn = NS - 1;
#pragma unroll 1
    for (int kt = 0; kt < NK; ++kt) {
      if (kt < NK - (NS - 2)) wait_vm<(NS - 2) * NLD>();
      else wait_vm<0>();
      bar();
      if (kt + NS - 1 < NK) issue(kt + NS - 1, stn);
      stn = (stn + 1 == NS) ? 0 : stn + 1;
    }
    bar();
    bar();
    return;
  }

  const int kq = w & 1, nh = w >> 1;
  float cf[4][4];
  if constexpr (MODE == 0) {
#pragma unroll
    for (int mf = 0; mf < 4; ++mf) {
      const f32x4* sp = (const f32x4*)(ml + ((size_t)bh * 1024 + m0 + 16 * mf + (l & 15)) * 8);
      f32x4 sv[4];
#pragma unroll
      for (int j = 0; j < 4; ++j) sv[j] = sp[j];
      float mx = fmaxf(sv[0][0], sv[0][2]);
#pragma unroll
      for (int j = 1; j < 4; ++j) mx = fmaxf(mx, fmaxf(sv[j][0], sv[j][2]));
      float e[8];
      float lsum = 0.f;
#pragma unroll
      for (int j = 0; j < 4; ++j) {
        e[2 * j] = __builtin_amdgcn_exp2f(sv[j][0] - mx);
        e[2 * j + 1] = __builtin_amdgcn_exp2f(sv[j][2] - mx);
        lsum += e[2 * j] * sv[j][1] + e[2 * j + 1] * sv[j][3];
      }
      float inv = 1.f / lsum;
      if (kq) {
#pragma unroll
        for (int j = 0; j < 4; ++j) cf[mf][j] = e[4 + j] * inv;
      } else {
#pragma unroll
        for (int j = 0; j < 4; ++j) cf[mf][j] = e[j] * inv;
      }
    }
  }
  f32x4 gq[2][3][2];
  u32x2 xr[2][3];
  if constexpr (MODE != 0) {
    const int goff = (MODE == 1) ? 2 * 768 : 5 * 768;
    const int b = m0 >> 10;
    const float* mp0 = modp + (0 * 2 + b) * 4608 + goff + n0 + 48 * nh + 4 * g;
    const float* mp1 = modp + (1 * 2 + b) * 4608 + goff + n0 + 48 * nh + 4 * g;
#pragma unroll
    for (int cc = 0; cc < 3; ++cc) {
      gq[0][cc][0] = *(const f32x4*)(mp0 + 16 * cc);
      gq[0][cc][1] = *(const f32x4*)(mp1 + 16 * cc);
    }
#pragma unroll
    for (int mf = 0; mf < 2; ++mf) {
      const u16* rp = resid + (size_t)(m0 + 32 * kq + 16 * mf + (l & 15)) * 768 + n0 + 48 * nh + 4 * g;
#pragma unroll
      for (int cc = 0; cc < 3; ++cc) xr[mf][cc] = *(const u32x2*)(rp + 16 * cc);
    }
  }

  const unsigned aoff0 = (unsigned)(kq * 8192 + (l & 15) * 128 + ((g ^ (l & 7)) << 4));
  const int q = (l & 15) >> 2, p = l & 3;
  const unsigned boff_t0 = (unsigned)(16384 + kq * 12288 + (g * 6 + 3 * nh) * 256 + ((4 * (g & 1) + q) * 32) + p * 8);
  const unsigned boff_t1 =
      (unsigned)(16384 + kq * 12288 + (g * 6 + 3 * nh) * 256 + ((4 * (1 - (g & 1)) + q) * 32) + p * 8);

  f32x4 acc[4][3];
  f32x4 tot[4][3];
#pragma unroll
  for (int i = 0; i < 4; ++i)
#pragma unroll
    for (int j = 0; j < 3; ++j) {
      acc[i][j] = (f32x4){0.f, 0.f, 0.f, 0.f};
      tot[i][j] = (f32x4){0.f, 0.f, 0.f, 0.f};
    }
  STAMP(3 + MODE, 1);

  int st = 0;
#pragma unroll 1
  for (int kt = 0; kt < NK; ++kt) {
    wait_lgkm0();
    bar();
    const LDS_AS char* sp = smem + st * STG;
#pragma unroll
    for (int ks = 0; ks < 2; ++ks) {
      bf16x8 af[4];
#pragma unroll
      for (int mf = 0; mf < 4; ++mf) af[mf] = lds_read16(sp + ((aoff0 ^ (ks * 64)) + mf * 2048));
#pragma unroll
      for (int cc = 0; cc < 3; ++cc) {
        bf16x4 w0 = lds_tr(sp + boff_t0 + ks * 6144 + cc * 256);
        bf16x4 w1 = lds_tr(sp + boff_t1 + ks * 6144 + cc * 256);
        bf16x8 wf = cat4(w0, w1);
#pragma unroll
        for (int mf = 0; mf < 4; ++mf) acc[mf][cc] = mfma16(wf, af[mf], acc[mf][cc]);
      }
    }
    if constexpr (MODE == 0) {
      if (kt & 1) {
#pragma unroll
        for (int mf = 0; mf < 4; ++mf) {
          const float c = cf[mf][0];
          cf[mf][0] = cf[mf][1];
          cf[mf][1] = cf[mf][2];
          cf[mf][2] = cf[mf][3];
#pragma unroll
          for (int cc = 0; cc < 3; ++cc) {
            tot[mf][cc] += acc[mf][cc] * c;
            acc[mf][cc] = (f32x4){0.f, 0.f, 0.f, 0.f};
          }
        }
      }
    }
    st = (st + 1 == NS) ? 0 : st + 1;
  }
  if constexpr (MODE == 0) {
#pragma unroll
    for (int mf = 0; mf < 4; ++mf)
#pragma unroll
      for (int cc = 0; cc < 3; ++cc) acc[mf][cc] = tot[mf][cc];
  }
  STAMP(3 + MODE, 12);
  wait_lgkm0();
  bar();
  {
    LDS_AS char* rp = smem + (nh * 2 + kq) * 6144 + l * 16;
    if (kq) {
#pragma unroll
      for (int mf = 0; mf < 2; ++mf)
#pragma unroll
        for (int cc = 0; cc < 3; ++cc) {
          *(LDS_AS f32x4*)(rp + (mf * 3 + cc) * 1024) = acc[mf][cc];
          acc[mf][cc] = acc[2 + mf][cc];
        }
    } else {
#pragma unroll
      for (int mf = 0; mf < 2; ++mf)
#pragma unroll
        for (int cc = 0; cc < 3; ++cc) *(LDS_AS f32x4*)(rp + (mf * 3 + cc) * 1024) = acc[2 + mf][cc];
    }
  }
  wait_lgkm0();
  bar();
  f32x4 r[2][3];
  {
    const LDS_AS char* rp = smem + (nh * 2 + (1 - kq)) * 6144 + l * 16;
#pragma unroll
    for (int mf = 0; mf < 2; ++mf)
#pragma unroll
      for (int cc = 0; cc < 3; ++cc)
        r[mf][cc] = acc[mf][cc] + lds_read16f(rp + (mf * 3 + cc) * 1024);
  }
  STAMP(3 + MODE, 13);
#pragma unroll
  for (int mf = 0; mf < 2; ++mf) {
    const int mrow = m0 + 32 * kq + 16 * mf + (l & 15);
    float v[3][4];
    u16* op;
    if constexpr (MODE == 0) {
      op = out + (size_t)((bh >> 1) * 1024 + mrow) * 768 + (bh & 1) * 384 + n0 + 48 * nh;
#pragma unroll
      for (int cc = 0; cc < 3; ++cc)
#pragma unroll
        for (int j = 0; j < 4; ++j) v[cc][j] = r[mf][cc][j];
    } else {
      op = out + (size_t)mrow * 768 + n0 + 48 * nh;
#pragma unroll
      for (int cc = 0; cc < 3; ++cc) {
        float xv[4] = {lo_f(xr[mf][cc][0]), hi_f(xr[mf][cc][0]), lo_f(xr[mf][cc][1]), hi_f(xr[mf][cc][1])};
#pragma unroll
        for (int j = 0; j < 4; ++j) {
          float gate = rbf(gq[0][cc][0][j] + gq[0][cc][1][j]);
          v[cc][j] = rbf(xv[j] + rbf(gate * rbf(r[mf][cc][j])));
        }
      }
    }
    {
      u32x4 o = pair_pack(pack2(v[0][0], v[0][1]), pack2(v[0][2], v[0][3]), pack2(v[1][0], v[1][1]),
                          pack2(v[1][2], v[1][3]));
      *(u32x4*)(op + (g & 1) * 16 + (g & 2) * 4) = o;
      u32x2 o2;
      o2[0] = pack2(v[2][0], v[2][1]);
      o2[1] = pack2(v[2][2], v[2][3]);
      *(u32x2*)(op + 32 + 4 * g) = o2;
    }
    if constexpr (MODE == 1) {
      float s = 0.f;
#pragma unroll
      for (int cc = 0; cc < 3; ++cc)
#pragma unroll
        for (int j = 0; j < 4; ++j) s += v[cc][j];
      s += __shfl_xor(s, 16);
      s += __shfl_xor(s, 32);
      float mean = s * (1.f / 48.f);
      float m2 = 0.f;
#pragma unroll
      for (int cc = 0; cc < 3; ++cc)
#pragma unroll
        for (int j = 0; j < 4; ++j) {
          float d = v[cc][j] - mean;
          m2 += d * d;
        }
      m2 += __shfl_xor(m2, 16);
      m2 += __shfl_xor(m2, 32);
      if (g == 0) {
        f32x2 s2;
        s2[0] = s;
        s2[1] = m2;
        stats[(size_t)mrow * 16 + (n0 / 48) + nh] = s2;
      }
    }
  }
  STAMP(3 + MODE, 15);
}

extern "C" void kernel_launch(void* const* d_in, const int* in_sizes, int n_in, void* d_out, int out_size,
                              void* d_ws, size_t ws_size, hipStream_t stream) {
  const u16* x = (const u16*)d_in[0];
  const u16* c = (const u16*)d_in[1];
  const u16* wqkv = (const u16*)d_in[2];
  const u16* wo = (const u16*)d_in[3];
  const u16* wmod = (const u16*)d_in[4];
  const u16* wfc1 = (const u16*)d_in[5];
  const u16* wfc2 = (const u16*)d_in[6];
  u16* out = (u16*)d_out;

  char* ws = (char*)d_ws;
  size_t off = 0;
  auto alloc = [&](size_t bytes) {
    char* p = ws + off;
    off += (bytes + 255) & ~(size_t)255;
    return p;
  };
  float* modp = (float*)alloc(2 * 2 * 4608 * 4);
  u16* xn = (u16*)alloc((size_t)Mrows * 768 * 2);
  u16* qk = (u16*)alloc((size_t)Mrows * 1536 * 2);
  u16* Vt = (u16*)alloc((size_t)Mrows * 768 * 2);
  u16* P = (u16*)alloc((size_t)4 * 1024 * 1024 * 2);
  f32x2* ml = (f32x2*)alloc((size_t)4 * 1024 * 8 * 8);
  u16* O = (u16*)alloc((size_t)Mrows * 768 * 2);
  u16* x1 = (u16*)alloc((size_t)Mrows * 768 * 2);
  f32x2* st2 = (f32x2*)alloc((size_t)Mrows * 16 * 8);
  u16* u = (u16*)alloc((size_t)Mrows * 3072 * 2);

  k0_mod_ln<<<K0_MOD_BLOCKS + K0_LN_BLOCKS, 256, 0, stream>>>(x, c, wmod, modp, xn);
  k_gemm_ln<144, 4, 0><<<256, 768, 0, stream>>>(xn, wqkv, modp, nullptr, qk, Vt);
  k_scores<<<256, 768, 0, stream>>>(qk, P, ml);
  k_gemm_small<0><<<256, 512, 0, stream>>>(P, Vt, modp, nullptr, ml, nullptr, O);
  k_gemm_small<1><<<256, 512, 0, stream>>>(O, wo, modp, x, nullptr, st2, x1);
  k_gemm_ln<192, 3, 1><<<256, 768, 0, stream>>>(x1, wfc1, modp, st2, u, nullptr);
  k_gemm_small<2><<<256, 512, 0, stream>>>(u, wfc2, modp, x1, nullptr, nullptr, out);
}
```

```cpp
#include <hip/hip_runtime.h>
#include <hip/hip_bf16.h>
#include <stdint.h>
#include <type_traits>

typedef __attribute__((ext_vector_type(8))) __bf16 bf16x8;
typedef __attribute__((ext_vector_type(4))) __bf16 bf16x4;
typedef __attribute__((ext_vector_type(2))) __bf16 bf16x2;
typedef __attribute__((ext_vector_type(4))) float f32x4;
typedef __attribute__((ext_vector_type(2))) float f32x2;
typedef __attribute__((ext_vector_type(4))) unsigned u32x4;
typedef __attribute__((ext_vector_type(2))) unsigned u32x2;
typedef unsigned short u16;
#define LDS_AS __attribute__((address_space(3)))

#define DEVI __device__ __forceinline__
#define STAMP(K, I)

static constexpr int Bsz = 2, Nseq = 1024, Dm = 768, Ff = 3072, Dh = 384;
static constexpr int Mrows = Bsz * Nseq;

template <int I, int N, class F>
DEVI void static_for(F&& f) {
  if constexpr (I < N) {
    f(std::integral_constant<int, I>{});
    static_for<I + 1, N>(f);
  }
}

DEVI float lo_f(unsigned u) { return __uint_as_float(u << 16); }
DEVI float hi_f(unsigned u) { return __uint_as_float(u & 0xffff0000u); }
DEVI float bf2f(u16 h) { return __uint_as_float(((unsigned)h) << 16); }
DEVI unsigned pack2(float a, float b) {
  bf16x2 v;
  v[0] = (__bf16)a;
  v[1] = (__bf16)b;
  return __builtin_bit_cast(unsigned, v);
}
DEVI float rbf(float a) { return lo_f(pack2(a, 0.f) ); }

template <int N>
DEVI void wait_vm() { asm volatile("s_waitcnt vmcnt(%0)" ::"i"(N) : "memory"); }
DEVI void wait_lgkm0() { asm volatile("s_waitcnt lgkmcnt(0)" ::: "memory"); }
DEVI void bar() { __builtin_amdgcn_s_barrier(); }

DEVI void glds16(const void* sbase, unsigned voff, unsigned lds_dst) {
  unsigned keep;
  asm volatile(
      "s_mov_b32 %0, m0\n\t"
      "s_mov_b32 m0, %3\n\t"
      "s_nop 0\n\t"
      "global_load_lds_dwordx4 %1, %2\n\t"
      "s_mov_b32 m0, %0"
      : "=&s"(keep)
      : "v"(voff), "s"(sbase), "s"(lds_dst)
      : "memory");
}

DEVI void gld16(u32x4& d, const void* p) { asm volatile("global_load_dwordx4 %0, %1, off" : "=v"(d) : "v"(p) : "memory"); }
DEVI void gld8(u32x2& d, const void* p) { asm volatile("global_load_dwordx2 %0, %1, off" : "=v"(d) : "v"(p) : "memory"); }
template <class T>
DEVI void pin(T& v) { asm volatile("" : "+v"(v)); }
DEVI f32x4 asf4(u32x4 v) { return __builtin_bit_cast(f32x4, v); }

DEVI bf16x8 lds_read16(const LDS_AS char* p) { return *(const LDS_AS bf16x8*)p; }
DEVI u32x4 lds_read16u(const LDS_AS char* p) { return *(const LDS_AS u32x4*)p; }
DEVI f32x4 lds_read16f(const LDS_AS char* p) { return *(const LDS_AS f32x4*)p; }
DEVI bf16x4 lds_tr(const LDS_AS char* p) {
  return __builtin_amdgcn_ds_read_tr16_b64_v4bf16((LDS_AS bf16x4*)p);
}
DEVI bf16x8 cat4(bf16x4 a, bf16x4 b) {
  return __builtin_shufflevector(a, b, 0, 1, 2, 3, 4, 5, 6, 7);
}
DEVI f32x4 mfma16(bf16x8 a, bf16x8 b, f32x4 c) {
  return __builtin_amdgcn_mfma_f32_16x16x32_bf16(a, b, c, 0, 0, 0);
}
DEVI int uni(int v) { return __builtin_amdgcn_readfirstlane(v); }

DEVI u32x4 pair_pack(unsigned ax, unsigned ay, unsigned bx, unsigned by) {
  auto r0 = __builtin_amdgcn_permlane16_swap(ax, bx, false, false);
  auto r1 = __builtin_amdgcn_permlane16_swap(ay, by, false, false);
  u32x4 o;
  o[0] = r0[0];
  o[1] = r1[0];
  o[2] = r0[1];
  o[3] = r1[1];
  return o;
}

__global__ __launch_bounds__(256) void k0_mod(const u16* __restrict__ c, const u16* __restrict__ wmod,
                                              float* __restrict__ modp) {
  __shared__ float sc[2][384];
  __shared__ float red[32][2][64];
  const int tid = threadIdx.x;
  const int cb = blockIdx.x % 72, kp = blockIdx.x / 72;
  const int tc = tid & 7, tr = tid >> 3;
  u32x4 w[12];
  const u16* wp = wmod + (size_t)(kp * 384 + tr) * 4608 + cb * 64 + tc * 8;
#pragma unroll
  for (int i = 0; i < 12; ++i) w[i] = *(const u32x4*)(wp + (size_t)i * 32 * 4608);
  for (int i = tid; i < 768; i += 256) {
    int b = i / 384, k = i % 384;
    float v = bf2f(c[b * 768 + kp * 384 + k]);
    float s = v / (1.f + __expf(-v));
    sc[b][k] = rbf(s);
  }
  __syncthreads();
  float acc[2][8];
#pragma unroll
  for (int b = 0; b < 2; ++b)
#pragma unroll
    for (int j = 0; j < 8; ++j) acc[b][j] = 0.f;
#pragma unroll
  for (int i = 0; i < 12; ++i) {
    float s0 = sc[0][tr + 32 * i], s1 = sc[1][tr + 32 * i];
#pragma unroll
    for (int j = 0; j < 4; ++j) {
      float a = lo_f(w[i][j]), bb = hi_f(w[i][j]);
      acc[0][2 * j] += s0 * a;
      acc[0][2 * j + 1] += s0 * bb;
      acc[1][2 * j] += s1 * a;
      acc[1][2 * j + 1] += s1 * bb;
    }
  }
#pragma unroll
  for (int b = 0; b < 2; ++b)
#pragma unroll
    for (int j = 0; j < 8; ++j) red[tr][b][tc * 8 + j] = acc[b][j];
  __syncthreads();
  if (tid < 128) {
    int b = tid >> 6, col = tid & 63;
    float s = 0.f;
#pragma unroll
    for (int r = 0; r < 32; ++r) s += red[r][b][col];
    modp[(kp * 2 + b) * 4608 + cb * 64 + col] = s;
  }
}

__global__ __launch_bounds__(256) void k_lnmod(const u16* __restrict__ x, const float* __restrict__ modp, int offS,
                                               u16* __restrict__ h) {
  const int tid = threadIdx.x;
  const int w = tid >> 6, l = tid & 63;
  const int row0 = blockIdx.x * 8 + w * 2;
  const int b = row0 >> 10;
  u32x2 v[2][3];
#pragma unroll
  for (int r = 0; r < 2; ++r) {
    const u16* xp = x + (size_t)(row0 + r) * 768 + l * 4;
#pragma unroll
    for (int i = 0; i < 3; ++i) v[r][i] = *(const u32x2*)(xp + i * 256);
  }
  f32x4 gg[3], tt[3];
#pragma unroll
  for (int i = 0; i < 3; ++i) {
    const float* m0p = modp + (0 * 2 + b) * 4608 + offS + l * 4 + i * 256;
    const float* m1p = modp + (1 * 2 + b) * 4608 + offS + l * 4 + i * 256;
    f32x4 s0 = *(const f32x4*)m0p, s1 = *(const f32x4*)m1p;
    f32x4 c0 = *(const f32x4*)(m0p + 768), c1 = *(const f32x4*)(m1p + 768);
#pragma unroll
    for (int j = 0; j < 4; ++j) {
      tt[i][j] = rbf(s0[j] + s1[j]);
      gg[i][j] = rbf(1.f + rbf(c0[j] + c1[j]));
    }
  }
#pragma unroll
  for (int r = 0; r < 2; ++r) {
    float f[12];
#pragma unroll
    for (int i = 0; i < 3; ++i) {
      f[4 * i] = lo_f(v[r][i][0]);
      f[4 * i + 1] = hi_f(v[r][i][0]);
      f[4 * i + 2] = lo_f(v[r][i][1]);
      f[4 * i + 3] = hi_f(v[r][i][1]);
    }
    float s = 0.f;
#pragma unroll
    for (int i = 0; i < 12; ++i) s += f[i];
#pragma unroll
    for (int o = 32; o > 0; o >>= 1) s += __shfl_xor(s, o);
    float mean = s * (1.f / 768.f);
    float q = 0.f;
#pragma unroll
    for (int i = 0; i < 12; ++i) {
      float d = f[i] - mean;
      q += d * d;
    }
#pragma unroll
    for (int o = 32; o > 0; o >>= 1) q += __shfl_xor(q, o);
    const float mb = rbf(mean);
    const float den = rbf(sqrtf(rbf(rbf(q * (1.f / 768.f)) + 1e-5f)));
    u16* op = h + (size_t)(row0 + r) * 768 + l * 4;
#pragma unroll
    for (int i = 0; i < 3; ++i) {
      float y[4];
#pragma unroll
      for (int j = 0; j < 4; ++j) y[j] = rbf(rbf(rbf(f[4 * i + j] - mb) / den) * gg[i][j]) + tt[i][j];
      u32x2 o;
      o[0] = pack2(y[0], y[1]);
      o[1] = pack2(y[2], y[3]);
      *(u32x2*)(op + i * 256) = o;
    }
  }
}

template <int BN, int NS, int EPI>
__global__ __launch_bounds__(768, 3) void k_gemm_big(const u16* __restrict__ A, const u16* __restrict__ W,
                                                     void* __restrict__ outv, u16* __restrict__ out2) {
  constexpr int NC = BN / 16;
  constexpr int LDW = (EPI == 0) ? 2304 : (EPI == 1 ? 3072 : 768);
  constexpr int LDA = (EPI == 2) ? 3072 : 768;
  constexpr int ASZ = 128 * 128;
  constexpr int BSZ = 8 * NC * 256;
  constexpr int STG = ASZ + BSZ;
  constexpr int SUBW = 2 * NC;
  constexpr int NBI = (SUBW + 3) / 4;
  constexpr int NLD = 4 + NBI;
  constexpr int NK = 768 / 64;
  __shared__ __attribute__((aligned(16))) char smem_raw[NS * STG];
  LDS_AS char* smem = (LDS_AS char*)smem_raw;
  const unsigned lds0 = (unsigned)(uintptr_t)smem;
  constexpr int KID = (EPI == 0) ? 1 : (EPI == 1 ? 6 : 5);
  STAMP(KID, 0);
  const int tid = threadIdx.x;
  const int l = tid & 63;
  const int w = uni(tid >> 6);
  const int bid = blockIdx.x;
  const int xcd = bid & 7, idx = bid >> 3;
  int tm, tn, ks = 0;
  if constexpr (EPI == 2) {
    tm = (xcd >> 1) * 4 + (idx & 3);
    tn = (idx >> 2) & 3;
    ks = (xcd & 1) * 2 + (idx >> 4);
  } else {
    tm = (xcd >> 1) * 4 + (idx & 3);
    tn = (xcd & 1) * 8 + (idx >> 2);
  }
  const int m0 = tm * 128, n0 = tn * BN;

  if (w >= 8) {
    const int lw = w - 8;
    const unsigned voffA = (unsigned)((l >> 3) * LDA * 2 + (((l & 7) ^ (l >> 3)) << 4));
    unsigned voffB[NBI];
#pragma unroll
    for (int i = 0; i < NBI; ++i) {
      int sub = SUBW * lw + 4 * i + (l >> 4);
      if (sub >= SUBW * (lw + 1)) sub = SUBW * (lw + 1) - 1;
      int kb = sub / NC, cc = sub % NC;
      int within = l & 15;
      int r = (within >> 1) ^ ((kb & 1) << 2);
      voffB[i] = (unsigned)(((kb * 8 + r) * LDW + cc * 16 + (within & 1) * 8) * 2);
    }
    const char* abase = (const char*)(A + (size_t)(m0 + 32 * lw) * LDA + ks * 768);
    const char* bbase = (const char*)(W + (size_t)ks * 768 * LDW + n0);
    auto issue = [&](int kt, int st) {
      const unsigned sa = lds0 + st * STG;
#pragma unroll
      for (int i = 0; i < 4; ++i)
        glds16(abase + (size_t)i * 8 * LDA * 2 + kt * 128, voffA, sa + (32 * lw + 8 * i) * 128);
      const char* sb = bbase + (size_t)kt * 64 * LDW * 2;
#pragma unroll
      for (int i = 0; i < NBI; ++i) {
        unsigned dst = sa + ASZ + (SUBW * lw + 4 * i) * 256;
        if (4 * i + 4 <= SUBW) {
          glds16(sb, voffB[i], dst);
        } else {
          if (l < (SUBW - 4 * i) * 16) glds16(sb, voffB[i], dst);
        }
      }
    };
#pragma unroll
    for (int s = 0; s < NS - 1; ++s) issue(s, s);
    int stn = NS - 1;
#pragma unroll 1
    for (int kt = 0; kt < NK; ++kt) {
      if (kt < NK - (NS - 2)) wait_vm<(NS - 2) * NLD>();
      else wait_vm<0>();
      bar();
      if (kt + NS - 1 < NK) issue(kt + NS - 1, stn);
      stn = (stn + 1 == NS) ? 0 : stn + 1;
    }
    bar();
    bar();
    return;
  }

  const int wm = w & 3, kh = w >> 2;
  const int g = l >> 4;
  const unsigned aoff = (unsigned)(((32 * wm + (l & 15)) * 128 + ((g ^ (l & 7)) << 4)) ^ (kh * 64));
  const int q = (l & 15) >> 2, p = l & 3;
  const unsigned boff_t0 = (unsigned)(ASZ + (4 * kh + g) * NC * 256 + ((4 * (g & 1) + q) * 32) + p * 8);
  const unsigned boff_t1 = (unsigned)(ASZ + (4 * kh + g) * NC * 256 + ((4 * (1 - (g & 1)) + q) * 32) + p * 8);

  f32x4 acc[2][NC];
#pragma unroll
  for (int i = 0; i < 2; ++i)
#pragma unroll
    for (int j = 0; j < NC; ++j) acc[i][j] = (f32x4){0.f, 0.f, 0.f, 0.f};
  STAMP(KID, 1);

  int st = 0;
#pragma unroll 1
  for (int kt = 0; kt < NK; ++kt) {
    wait_lgkm0();
    bar();
    const LDS_AS char* sp = smem + st * STG;
    bf16x8 xb[2];
#pragma unroll
    for (int mf = 0; mf < 2; ++mf) xb[mf] = lds_read16(sp + aoff + mf * 2048);
#pragma unroll
    for (int cc = 0; cc < NC; ++cc) {
      bf16x4 w0 = lds_tr(sp + boff_t0 + cc * 256);
      bf16x4 w1 = lds_tr(sp + boff_t1 + cc * 256);
      bf16x8 wf = cat4(w0, w1);
      acc[0][cc] = mfma16(wf, xb[0], acc[0][cc]);
      acc[1][cc] = mfma16(wf, xb[1], acc[1][cc]);
    }
    st = (st + 1 == NS) ? 0 : st + 1;
  }
  STAMP(KID, 14);

  wait_lgkm0();
  bar();
  {
    LDS_AS char* rp = smem + (wm * 2 + kh) * NC * 1024 + l * 16;
    if (kh) {
#pragma unroll
      for (int cc = 0; cc < NC; ++cc) {
        *(LDS_AS f32x4*)(rp + cc * 1024) = acc[0][cc];
        acc[0][cc] = acc[1][cc];
      }
    } else {
#pragma unroll
      for (int cc = 0; cc < NC; ++cc) *(LDS_AS f32x4*)(rp + cc * 1024) = acc[1][cc];
    }
  }
  wait_lgkm0();
  bar();
  const int row = m0 + 32 * wm + 16 * kh + (l & 15);
  const LDS_AS char* rp = smem + (wm * 2 + (1 - kh)) * NC * 1024 + l * 16;
  if constexpr (EPI == 2) {
    float* op = (float*)outv + ((size_t)ks * 2048 + row) * 768 + n0 + 4 * g;
#pragma unroll
    for (int cc = 0; cc < NC; ++cc) *(f32x4*)(op + 16 * cc) = acc[0][cc] + lds_read16f(rp + cc * 1024);
  } else {
    u16* out = (u16*)outv;
    unsigned pk[NC][2];
#pragma unroll
    for (int cc = 0; cc < NC; ++cc) {
      f32x4 v = acc[0][cc] + lds_read16f(rp + cc * 1024);
      if constexpr (EPI == 1) {
#pragma unroll
        for (int j = 0; j < 4; ++j) {
          float y = rbf(v[j]);
          float z = 1.5957691216057308f * (y + 0.044715f * y * y * y);
          v[j] = y / (1.f + __expf(-z));
        }
      }
      pk[cc][0] = pack2(v[0], v[1]);
      pk[cc][1] = pack2(v[2], v[3]);
    }
    auto dst_ptr = [&](int n) -> u16* {
      if constexpr (EPI == 1) {
        return out + (size_t)row * 3072 + n;
      } else {
        if (n < 1536) return out + (size_t)row * 1536 + n;
        int nv = n - 1536;
        int hd = nv / 96;
        int dd = nv - hd * 96;
        return out2 + ((size_t)(((row >> 10) * 8 + hd) * 1024 + (row & 1023))) * 96 + dd;
      }
    };
#pragma unroll
    for (int a = 0; a < NC / 2; ++a) {
      u32x4 o = pair_pack(pk[2 * a][0], pk[2 * a][1], pk[2 * a + 1][0], pk[2 * a + 1][1]);
      *(u32x4*)dst_ptr(n0 + 32 * a + (g & 1) * 16 + (g & 2) * 4) = o;
    }
    if constexpr (NC & 1) {
      u32x2 o;
      o[0] = pk[NC - 1][0];
      o[1] = pk[NC - 1][1];
      *(u32x2*)dst_ptr(n0 + 16 * (NC - 1) + 4 * g) = o;
    }
  }
  STAMP(KID, 15);
}

__global__ __launch_bounds__(256) void k_final(const float* __restrict__ part, const float* __restrict__ modp,
                                               const u16* __restrict__ x1, u16* __restrict__ out) {
  const int t = blockIdx.x * 256 + threadIdx.x;
  const int row = t / 96, c8 = (t - row * 96) * 8;
  const int b = row >> 10;
  const size_t e = (size_t)row * 768 + c8;
  f32x4 p[4][2];
#pragma unroll
  for (int k = 0; k < 4; ++k) {
    p[k][0] = *(const f32x4*)(part + (size_t)k * 2048 * 768 + e);
    p[k][1] = *(const f32x4*)(part + (size_t)k * 2048 * 768 + e + 4);
  }
  u32x4 xr = *(const u32x4*)(x1 + e);
  const float* mp0 = modp + (0 * 2 + b) * 4608 + 5 * 768 + c8;
  const float* mp1 = modp + (1 * 2 + b) * 4608 + 5 * 768 + c8;
  f32x4 ga[2] = {*(const f32x4*)mp0, *(const f32x4*)(mp0 + 4)};
  f32x4 gb[2] = {*(const f32x4*)mp1, *(const f32x4*)(mp1 + 4)};
  float v[8];
#pragma unroll
  for (int hh = 0; hh < 2; ++hh)
#pragma unroll
    for (int j = 0; j < 4; ++j) {
      float m = (p[0][hh][j] + p[1][hh][j]) + (p[2][hh][j] + p[3][hh][j]);
      float gate = rbf(ga[hh][j] + gb[hh][j]);
      unsigned xw = xr[hh * 2 + (j >> 1)];
      float xv = (j & 1) ? hi_f(xw) : lo_f(xw);
      v[hh * 4 + j] = xv + rbf(gate * rbf(m));
    }
  u32x4 o;
#pragma unroll
  for (int j = 0; j < 4; ++j) o[j] = pack2(v[2 * j], v[2 * j + 1]);
  *(u32x4*)(out + e) = o;
}

__global__ __launch_bounds__(768, 3) void k_scores(const u16* __restrict__ qk, u16* __restrict__ P,
                                                   f32x2* __restrict__ ml) {
  constexpr int NS = 4;
  constexpr int ASZ = 128 * 128;
  constexpr int STG = 2 * ASZ;
  constexpr int NK = 384 / 64;
  constexpr int NLD = 8;
  constexpr int LDQ = 1536;
  __shared__ __attribute__((aligned(16))) char smem_raw[NS * STG];
  LDS_AS char* smem = (LDS_AS char*)smem_raw;
  const unsigned lds0 = (unsigned)(uintptr_t)smem;
  STAMP(2, 0);
  const int tid = threadIdx.x, l = tid & 63;
  const int w = uni(tid >> 6);
  const int bid = blockIdx.x;
  const int xcd = bid & 7, idx = bid >> 3;
  const int bh = xcd >> 1;
  const int tq = (xcd & 1) * 4 + (idx & 3);
  const int tk = idx >> 2;
  const int b = bh >> 1, h = bh & 1;

  if (w >= 8) {
    const int lw = w - 8;
    const unsigned voff = (unsigned)((l >> 3) * LDQ * 2 + (((l & 7) ^ (l >> 3)) << 4));
    const char* qbase = (const char*)(qk + (size_t)(b * 1024 + tq * 128 + 32 * lw) * LDQ + h * 384);
    const char* kbase = (const char*)(qk + (size_t)(b * 1024 + tk * 128 + 32 * lw) * LDQ + 768 + h * 384);
    auto issue = [&](int kt, int st) {
      const unsigned sa = lds0 + st * STG;
#pragma unroll
      for (int i = 0; i < 4; ++i)
        glds16(qbase + (size_t)i * 8 * LDQ * 2 + kt * 128, voff, sa + (32 * lw + 8 * i) * 128);
#pragma unroll
      for (int i = 0; i < 4; ++i)
        glds16(kbase + (size_t)i * 8 * LDQ * 2 + kt * 128, voff, sa + ASZ + (32 * lw + 8 * i) * 128);
    };
#pragma unroll
    for (int s = 0; s < NS - 1; ++s) issue(s, s);
    int stn = NS - 1;
#pragma unroll 1
    for (int kt = 0; kt < NK; ++kt) {
      if (kt < NK - (NS - 2)) wait_vm<(NS - 2) * NLD>();
      else wait_vm<0>();
      bar();
      if (kt + NS - 1 < NK) issue(kt + NS - 1, stn);
      stn = (stn + 1 == NS) ? 0 : stn + 1;
    }
    bar();
    bar();
    return;
  }

  const int wm = w & 3, kh = w >> 2;
  const int g = l >> 4;
  const unsigned qoff = (unsigned)(((32 * wm + (l & 15)) * 128 + ((g ^ (l & 7)) << 4)) ^ (kh * 64));
  const unsigned koff = (unsigned)(ASZ + (((l & 15) * 128 + ((g ^ (l & 7)) << 4)) ^ (kh * 64)));
  f32x4 acc[2][8];
#pragma unroll
  for (int i = 0; i < 2; ++i)
#pragma unroll
    for (int j = 0; j < 8; ++j) acc[i][j] = (f32x4){0.f, 0.f, 0.f, 0.f};

  int st = 0;
#pragma unroll 1
  for (int kt = 0; kt < NK; ++kt) {
    wait_lgkm0();
    bar();
    const LDS_AS char* sp = smem + st * STG;
    bf16x8 qf[2];
#pragma unroll
    for (int mf = 0; mf < 2; ++mf) qf[mf] = lds_read16(sp + qoff + mf * 2048);
#pragma unroll
    for (int kf = 0; kf < 8; ++kf) {
      bf16x8 kfr = lds_read16(sp + koff + kf * 2048);
      acc[0][kf] = mfma16(kfr, qf[0], acc[0][kf]);
      acc[1][kf] = mfma16(kfr, qf[1], acc[1][kf]);
    }
    st = (st + 1 == NS) ? 0 : st + 1;
  }
  STAMP(2, 14);
  wait_lgkm0();
  bar();
  {
    LDS_AS char* rp = smem + (wm * 2 + kh) * 8 * 1024 + l * 16;
    if (kh) {
#pragma unroll
      for (int kf = 0; kf < 8; ++kf) {
        *(LDS_AS f32x4*)(rp + kf * 1024) = acc[0][kf];
        acc[0][kf] = acc[1][kf];
      }
    } else {
#pragma unroll
      for (int kf = 0; kf < 8; ++kf) *(LDS_AS f32x4*)(rp + kf * 1024) = acc[1][kf];
    }
  }
  wait_lgkm0();
  bar();
  f32x4 s[8];
  {
    const LDS_AS char* rp = smem + (wm * 2 + (1 - kh)) * 8 * 1024 + l * 16;
#pragma unroll
    for (int kf = 0; kf < 8; ++kf) {
      f32x4 v = acc[0][kf] + lds_read16f(rp + kf * 1024);
#pragma unroll
      for (int j = 0; j < 4; ++j) v[j] = rbf(rbf(v[j]) * 0.051031036307982884f);
      s[kf] = v;
    }
  }
  const float l2e = 1.4426950408889634f;
  float mx = -INFINITY;
#pragma unroll
  for (int kf = 0; kf < 8; ++kf)
#pragma unroll
    for (int j = 0; j < 4; ++j) mx = fmaxf(mx, s[kf][j]);
  mx = fmaxf(mx, __shfl_xor(mx, 16));
  mx = fmaxf(mx, __shfl_xor(mx, 32));
  const float mbl = mx * l2e;
  float sum = 0.f;
  unsigned pk[8][2];
#pragma unroll
  for (int kf = 0; kf < 8; ++kf) {
    float e[4];
#pragma unroll
    for (int j = 0; j < 4; ++j) {
      e[j] = __builtin_amdgcn_exp2f(s[kf][j] * l2e - mbl);
      sum += e[j];
    }
    pk[kf][0] = pack2(e[0], e[1]);
    pk[kf][1] = pack2(e[2], e[3]);
  }
  sum += __shfl_xor(sum, 16);
  sum += __shfl_xor(sum, 32);
  const int qrow = tq * 128 + 32 * wm + 16 * kh + (l & 15);
  u16* op = P + ((size_t)bh * 1024 + qrow) * 1024 + tk * 128;
#pragma unroll
  for (int a = 0; a < 4; ++a) {
    u32x4 o = pair_pack(pk[2 * a][0], pk[2 * a][1], pk[2 * a + 1][0], pk[2 * a + 1][1]);
    *(u32x4*)(op + 32 * a + (g & 1) * 16 + (g & 2) * 4) = o;
  }
  if (g == 0) {
    f32x2 s2;
    s2[0] = mbl;
    s2[1] = sum;
    ml[((size_t)bh * 1024 + qrow) * 8 + tk] = s2;
  }
  STAMP(2, 15);
}

template <int MODE>
__global__ __launch_bounds__(512, 2) void k_gemm_small(const u16* __restrict__ Ain, const u16* __restrict__ Bin,
                                                       const float* __restrict__ modp,
                                                       const u16* __restrict__ resid,
                                                       const f32x2* __restrict__ ml, f32x2* __restrict__ stats,
                                                       u16* __restrict__ out) {
  constexpr int KTOT = (MODE == 0) ? 1024 : (MODE == 1 ? 768 : 3072);
  constexpr int KH = KTOT / 2;
  constexpr int NK = KH / 64;
  constexpr int LDA = KTOT;
  constexpr int LDB = (MODE == 0) ? 96 : 768;
  constexpr int NS = 4;
  constexpr int STG = 40960;
  constexpr int NLD = 10;
  __shared__ __attribute__((aligned(16))) char smem_raw[NS * STG];
  LDS_AS char* smem = (LDS_AS char*)smem_raw;
  const unsigned lds0 = (unsigned)(uintptr_t)smem;
  STAMP(3 + MODE, 0);
  const int tid = threadIdx.x, l = tid & 63;
  const int w = uni(tid >> 6);
  const int bid = blockIdx.x;
  const int xcd = bid & 7, idx = bid >> 3;
  const int g = l >> 4;

  int m0, n0, bh = 0;
  const u16* Ap;
  const u16* Bp;
  if constexpr (MODE == 0) {
    bh = xcd >> 1;
    const int tq = (xcd & 1) * 8 + (idx & 7);
    const int td = idx >> 3;
    m0 = tq * 64;
    n0 = td * 96;
    Ap = Ain + ((size_t)bh * 1024 + m0) * 1024;
    Bp = Bin + ((size_t)(bh * 4 + td) * 1024) * 96;
  } else {
    const int tm = xcd * 4 + (idx & 3);
    const int tn = idx >> 2;
    m0 = tm * 64;
    n0 = tn * 96;
    Ap = Ain + (size_t)m0 * LDA;
    Bp = Bin + n0;
  }

  if (w >= 4) {
    const int lw = w - 4;
    const int kq = lw & 1, part = lw >> 1;
    const unsigned voffA = (unsigned)((l >> 3) * LDA * 2 + (((l & 7) ^ (l >> 3)) << 4));
    unsigned voffB[6];
#pragma unroll
    for (int i = 0; i < 6; ++i) {
      int sub = 24 * part + 4 * i + (l >> 4);
      int kb = sub / 6, cc = sub % 6;
      int within = l & 15;
      int r = (within >> 1) ^ ((kb & 1) << 2);
      voffB[i] = (unsigned)(((kb * 8 + r) * LDB + cc * 16 + (within & 1) * 8) * 2);
    }
    const char* abase = (const char*)(Ap + (size_t)(32 * part) * LDA + kq * KH);
    const char* bbase = (const char*)(Bp + (size_t)(kq * KH) * LDB);
    auto issue = [&](int kt, int st) {
      const unsigned sa = lds0 + st * STG;
#pragma unroll
      for (int i = 0; i < 4; ++i)
        glds16(abase + (size_t)i * 8 * LDA * 2 + kt * 128, voffA, sa + kq * 8192 + (32 * part + 8 * i) * 128);
      const char* sb = bbase + (size_t)kt * 64 * LDB * 2;
#pragma unroll
      for (int i = 0; i < 6; ++i) glds16(sb, voffB[i], sa + 16384 + kq * 12288 + (24 * part + 4 * i) * 256);
    };
#pragma unroll
    for (int s = 0; s < NS - 1; ++s) issue(s, s);
    int stn = NS - 1;
#pragma unroll 1
    for (int kt = 0; kt < NK; ++kt) {
      if (kt < NK - (NS - 2)) wait_vm<(NS - 2) * NLD>();
      else wait_vm<0>();
      bar();
      if (kt + NS - 1 < NK) issue(kt + NS - 1, stn);
      stn = (stn + 1 == NS) ? 0 : stn + 1;
    }
    bar();
    bar();
    return;
  }

  const int kq = w & 1, nh = w >> 1;
  float cf[4][4];
  if constexpr (MODE == 0) {
#pragma unroll
    for (int mf = 0; mf < 4; ++mf) {
      const f32x4* sp = (const f32x4*)(ml + ((size_t)bh * 1024 + m0 + 16 * mf + (l & 15)) * 8);
      f32x4 sv[4];
#pragma unroll
      for (int j = 0; j < 4; ++j) sv[j] = sp[j];
      float mx = fmaxf(sv[0][0], sv[0][2]);
#pragma unroll
      for (int j = 1; j < 4; ++j) mx = fmaxf(mx, fmaxf(sv[j][0], sv[j][2]));
      float e[8];
      float lsum = 0.f;
#pragma unroll
      for (int j = 0; j < 4; ++j) {
        e[2 * j] = __builtin_amdgcn_exp2f(sv[j][0] - mx);
        e[2 * j + 1] = __builtin_amdgcn_exp2f(sv[j][2] - mx);
        lsum += e[2 * j] * sv[j][1] + e[2 * j + 1] * sv[j][3];
      }
      float inv = 1.f / lsum;
      if (kq) {
#pragma unroll
        for (int j = 0; j < 4; ++j) cf[mf][j] = e[4 + j] * inv;
      } else {
#pragma unroll
        for (int j = 0; j < 4; ++j) cf[mf][j] = e[j] * inv;
      }
    }
  }
  f32x4 gq[2][3][2];
  u32x2 xr[2][3];
  if constexpr (MODE != 0) {
    const int goff = (MODE == 1) ? 2 * 768 : 5 * 768;
    const int b = m0 >> 10;
    const float* mp0 = modp + (0 * 2 + b) * 4608 + goff + n0 + 48 * nh + 4 * g;
    const float* mp1 = modp + (1 * 2 + b) * 4608 + goff + n0 + 48 * nh + 4 * g;
#pragma unroll
    for (int cc = 0; cc < 3; ++cc) {
      gq[0][cc][0] = *(const f32x4*)(mp0 + 16 * cc);
      gq[0][cc][1] = *(const f32x4*)(mp1 + 16 * cc);
    }
#pragma unroll
    for (int mf = 0; mf < 2; ++mf) {
      const u16* rp = resid + (size_t)(m0 + 32 * kq + 16 * mf + (l & 15)) * 768 + n0 + 48 * nh + 4 * g;
#pragma unroll
      for (int cc = 0; cc < 3; ++cc) xr[mf][cc] = *(const u32x2*)(rp + 16 * cc);
    }
  }

  const unsigned aoff0 = (unsigned)(kq * 8192 + (l & 15) * 128 + ((g ^ (l & 7)) << 4));
  const int q = (l & 15) >> 2, p = l & 3;
  const unsigned boff_t0 = (unsigned)(16384 + kq * 12288 + (g * 6 + 3 * nh) * 256 + ((4 * (g & 1) + q) * 32) + p * 8);
  const unsigned boff_t1 =
      (unsigned)(16384 + kq * 12288 + (g * 6 + 3 * nh) * 256 + ((4 * (1 - (g & 1)) + q) * 32) + p * 8);

  f32x4 acc[4][3];
  f32x4 tot[4][3];
#pragma unroll
  for (int i = 0; i < 4; ++i)
#pragma unroll
    for (int j = 0; j < 3; ++j) {
      acc[i][j] = (f32x4){0.f, 0.f, 0.f, 0.f};
      tot[i][j] = (f32x4){0.f, 0.f, 0.f, 0.f};
    }
  STAMP(3 + MODE, 1);

  int st = 0;
#pragma unroll 1
  for (int kt = 0; kt < NK; ++kt) {
    wait_lgkm0();
    bar();
    const LDS_AS char* sp = smem + st * STG;
#pragma unroll
    for (int ks = 0; ks < 2; ++ks) {
      bf16x8 af[4];
#pragma unroll
      for (int mf = 0; mf < 4; ++mf) af[mf] = lds_read16(sp + ((aoff0 ^ (ks * 64)) + mf * 2048));
#pragma unroll
      for (int cc = 0; cc < 3; ++cc) {
        bf16x4 w0 = lds_tr(sp + boff_t0 + ks * 6144 + cc * 256);
        bf16x4 w1 = lds_tr(sp + boff_t1 + ks * 6144 + cc * 256);
        bf16x8 wf = cat4(w0, w1);
#pragma unroll
        for (int mf = 0; mf < 4; ++mf) acc[mf][cc] = mfma16(wf, af[mf], acc[mf][cc]);
      }
    }
    if constexpr (MODE == 0) {
      if (kt & 1) {
#pragma unroll
        for (int mf = 0; mf < 4; ++mf) {
          const float c = cf[mf][0];
          cf[mf][0] = cf[mf][1];
          cf[mf][1] = cf[mf][2];
          cf[mf][2] = cf[mf][3];
#pragma unroll
          for (int cc = 0; cc < 3; ++cc) {
            tot[mf][cc] += acc[mf][cc] * c;
            acc[mf][cc] = (f32x4){0.f, 0.f, 0.f, 0.f};
          }
        }
      }
    }
    st = (st + 1 == NS) ? 0 : st + 1;
  }
  if constexpr (MODE == 0) {
#pragma unroll
    for (int mf = 0; mf < 4; ++mf)
#pragma unroll
      for (int cc = 0; cc < 3; ++cc) acc[mf][cc] = tot[mf][cc];
  }
  STAMP(3 + MODE, 12);
  wait_lgkm0();
  bar();
  {
    LDS_AS char* rp = smem + (nh * 2 + kq) * 6144 + l * 16;
    if (kq) {
#pragma unroll
      for (int mf = 0; mf < 2; ++mf)
#pragma unroll
        for (int cc = 0; cc < 3; ++cc) {
          *(LDS_AS f32x4*)(rp + (mf * 3 + cc) * 1024) = acc[mf][cc];
          acc[mf][cc] = acc[2 + mf][cc];
        }
    } else {
#pragma unroll
      for (int mf = 0; mf < 2; ++mf)
#pragma unroll
        for (int cc = 0; cc < 3; ++cc) *(LDS_AS f32x4*)(rp + (mf * 3 + cc) * 1024) = acc[2 + mf][cc];
    }
  }
  wait_lgkm0();
  bar();
  f32x4 r[2][3];
  {
    const LDS_AS char* rp = smem + (nh * 2 + (1 - kq)) * 6144 + l * 16;
#pragma unroll
    for (int mf = 0; mf < 2; ++mf)
#pragma unroll
      for (int cc = 0; cc < 3; ++cc)
        r[mf][cc] = acc[mf][cc] + lds_read16f(rp + (mf * 3 + cc) * 1024);
  }
  STAMP(3 + MODE, 13);
#pragma unroll
  for (int mf = 0; mf < 2; ++mf) {
    const int mrow = m0 + 32 * kq + 16 * mf + (l & 15);
    float v[3][4];
    u16* op;
    if constexpr (MODE == 0) {
      op = out + (size_t)((bh >> 1) * 1024 + mrow) * 768 + (bh & 1) * 384 + n0 + 48 * nh;
#pragma unroll
      for (int cc = 0; cc < 3; ++cc)
#pragma unroll
        for (int j = 0; j < 4; ++j) v[cc][j] = r[mf][cc][j];
    } else {
      op = out + (size_t)mrow * 768 + n0 + 48 * nh;
#pragma unroll
      for (int cc = 0; cc < 3; ++cc) {
        float xv[4] = {lo_f(xr[mf][cc][0]), hi_f(xr[mf][cc][0]), lo_f(xr[mf][cc][1]), hi_f(xr[mf][cc][1])};
#pragma unroll
        for (int j = 0; j < 4; ++j) {
          float gate = rbf(gq[0][cc][0][j] + gq[0][cc][1][j]);
          v[cc][j] = rbf(xv[j] + rbf(gate * rbf(r[mf][cc][j])));
        }
      }
    }
    {
      u32x4 o = pair_pack(pack2(v[0][0], v[0][1]), pack2(v[0][2], v[0][3]), pack2(v[1][0], v[1][1]),
                          pack2(v[1][2], v[1][3]));
      *(u32x4*)(op + (g & 1) * 16 + (g & 2) * 4) = o;
      u32x2 o2;
      o2[0] = pack2(v[2][0], v[2][1]);
      o2[1] = pack2(v[2][2], v[2][3]);
      *(u32x2*)(op + 32 + 4 * g) = o2;
    }
    if constexpr (MODE == 1) {
      float s = 0.f;
#pragma unroll
      for (int cc = 0; cc < 3; ++cc)
#pragma unroll
        for (int j = 0; j < 4; ++j) s += v[cc][j];
      s += __shfl_xor(s, 16);
      s += __shfl_xor(s, 32);
      float mean = s * (1.f / 48.f);
      float m2 = 0.f;
#pragma unroll
      for (int cc = 0; cc < 3; ++cc)
#pragma unroll
        for (int j = 0; j < 4; ++j) {
          float d = v[cc][j] - mean;
          m2 += d * d;
        }
      m2 += __shfl_xor(m2, 16);
      m2 += __shfl_xor(m2, 32);
      if (g == 0) {
        f32x2 s2;
        s2[0] = s;
        s2[1] = m2;
        stats[(size_t)mrow * 16 + (n0 / 48) + nh] = s2;
      }
    }
  }
  STAMP(3 + MODE, 15);
}

extern "C" void kernel_launch(void* const* d_in, const int* in_sizes, int n_in, void* d_out, int out_size,
                              void* d_ws, size_t ws_size, hipStream_t stream) {
  const u16* x = (const u16*)d_in[0];
  const u16* c = (const u16*)d_in[1];
  const u16* wqkv = (const u16*)d_in[2];
  const u16* wo = (const u16*)d_in[3];
  const u16* wmod = (const u16*)d_in[4];
  const u16* wfc1 = (const u16*)d_in[5];
  const u16* wfc2 = (const u16*)d_in[6];
  u16* out = (u16*)d_out;

  char* ws = (char*)d_ws;
  size_t off = 0;
  auto alloc = [&](size_t bytes) {
    char* p = ws + off;
    off += (bytes + 255) & ~(size_t)255;
    return p;
  };
  float* modp = (float*)alloc(2 * 2 * 4608 * 4);
  u16* h = (u16*)alloc((size_t)Mrows * 768 * 2);
  u16* qk = (u16*)alloc((size_t)Mrows * 1536 * 2);
  u16* Vt = (u16*)alloc((size_t)Mrows * 768 * 2);
  u16* P = (u16*)alloc((size_t)4 * 1024 * 1024 * 2);
  f32x2* ml = (f32x2*)alloc((size_t)4 * 1024 * 8 * 8);
  u16* O = (u16*)alloc((size_t)Mrows * 768 * 2);
  u16* x1 = (u16*)alloc((size_t)Mrows * 768 * 2);
  f32x2* st2 = (f32x2*)alloc((size_t)Mrows * 16 * 8);
  u16* h2 = (u16*)alloc((size_t)Mrows * 768 * 2);
  u16* u = (u16*)alloc((size_t)Mrows * 3072 * 2);
  float* part = (float*)alloc((size_t)4 * Mrows * 768 * 4);

  k0_mod<<<144, 256, 0, stream>>>(c, wmod, modp);
  k_lnmod<<<256, 256, 0, stream>>>(x, modp, 0, h);
  k_gemm_big<144, 4, 0><<<256, 768, 0, stream>>>(h, wqkv, qk, Vt);
  k_scores<<<256, 768, 0, stream>>>(qk, P, ml);
  k_gemm_small<0><<<256, 512, 0, stream>>>(P, Vt, modp, nullptr, ml, nullptr, O);
  k_gemm_small<1><<<256, 512, 0, stream>>>(O, wo, modp, x, nullptr, st2, x1);
  k_lnmod<<<256, 256, 0, stream>>>(x1, modp, 3 * 768, h2);
  k_gemm_big<192, 4, 1><<<256, 768, 0, stream>>>(h2, wfc1, u, nullptr);
  k_gemm_big<192, 4, 2><<<256, 768, 0, stream>>>(u, wfc2, part, nullptr);
  k_final<<<768, 256, 0, stream>>>(part, modp, x1, out);
}
```

```cpp
#include <hip/hip_runtime.h>
#include <hip/hip_bf16.h>
#include <stdint.h>
#include <type_traits>

typedef __attribute__((ext_vector_type(8))) __bf16 bf16x8;
typedef __attribute__((ext_vector_type(4))) __bf16 bf16x4;
typedef __attribute__((ext_vector_type(2))) __bf16 bf16x2;
typedef __attribute__((ext_vector_type(4))) float f32x4;
typedef __attribute__((ext_vector_type(2))) float f32x2;
typedef __attribute__((ext_vector_type(4))) unsigned u32x4;
typedef __attribute__((ext_vector_type(2))) unsigned u32x2;
typedef unsigned short u16;
#define LDS_AS __attribute__((address_space(3)))

#define DEVI __device__ __forceinline__
#define STAMP(K, I)

static constexpr int Bsz = 2, Nseq = 1024, Dm = 768, Ff = 3072, Dh = 384;
static constexpr int Mrows = Bsz * Nseq;

template <int I, int N, class F>
DEVI void static_for(F&& f) {
  if constexpr (I < N) {
    f(std::integral_constant<int, I>{});
    static_for<I + 1, N>(f);
  }
}

DEVI float lo_f(unsigned u) { return __uint_as_float(u << 16); }
DEVI float hi_f(unsigned u) { return __uint_as_float(u & 0xffff0000u); }
DEVI float bf2f(u16 h) { return __uint_as_float(((unsigned)h) << 16); }
DEVI unsigned pack2(float a, float b) {
  bf16x2 v;
  v[0] = (__bf16)a;
  v[1] = (__bf16)b;
  return __builtin_bit_cast(unsigned, v);
}
DEVI float rbf(float a) { return lo_f(pack2(a, 0.f) ); }

template <int N>
DEVI void wait_vm() { asm volatile("s_waitcnt vmcnt(%0)" ::"i"(N) : "memory"); }
DEVI void wait_lgkm0() { asm volatile("s_waitcnt lgkmcnt(0)" ::: "memory"); }
DEVI void bar() { __builtin_amdgcn_s_barrier(); }

DEVI void glds16(const void* sbase, unsigned voff, unsigned lds_dst) {
  unsigned keep;
  asm volatile(
      "s_mov_b32 %0, m0\n\t"
      "s_mov_b32 m0, %3\n\t"
      "s_nop 0\n\t"
      "global_load_lds_dwordx4 %1, %2\n\t"
      "s_mov_b32 m0, %0"
      : "=&s"(keep)
      : "v"(voff), "s"(sbase), "s"(lds_dst)
      : "memory");
}

DEVI void gld16(u32x4& d, const void* p) { asm volatile("global_load_dwordx4 %0, %1, off" : "=v"(d) : "v"(p) : "memory"); }
DEVI void gld8(u32x2& d, const void* p) { asm volatile("global_load_dwordx2 %0, %1, off" : "=v"(d) : "v"(p) : "memory"); }
template <class T>
DEVI void pin(T& v) { asm volatile("" : "+v"(v)); }
DEVI f32x4 asf4(u32x4 v) { return __builtin_bit_cast(f32x4, v); }

DEVI bf16x8 lds_read16(const LDS_AS char* p) { return *(const LDS_AS bf16x8*)p; }
DEVI u32x4 lds_read16u(const LDS_AS char* p) { return *(const LDS_AS u32x4*)p; }
DEVI f32x4 lds_read16f(const LDS_AS char* p) { return *(const LDS_AS f32x4*)p; }
DEVI bf16x4 lds_tr(const LDS_AS char* p) {
  return __builtin_amdgcn_ds_read_tr16_b64_v4bf16((LDS_AS bf16x4*)p);
}
DEVI bf16x8 cat4(bf16x4 a, bf16x4 b) {
  return __builtin_shufflevector(a, b, 0, 1, 2, 3, 4, 5, 6, 7);
}
DEVI f32x4 mfma16(bf16x8 a, bf16x8 b, f32x4 c) {
  return __builtin_amdgcn_mfma_f32_16x16x32_bf16(a, b, c, 0, 0, 0);
}
DEVI int uni(int v) { return __builtin_amdgcn_readfirstlane(v); }

DEVI u32x4 pair_pack(unsigned ax, unsigned ay, unsigned bx, unsigned by) {
  auto r0 = __builtin_amdgcn_permlane16_swap(ax, bx, false, false);
  auto r1 = __builtin_amdgcn_permlane16_swap(ay, by, false, false);
  u32x4 o;
  o[0] = r0[0];
  o[1] = r1[0];
  o[2] = r0[1];
  o[3] = r1[1];
  return o;
}

static constexpr int K0_MOD_BLOCKS = 144;
static constexpr int K0_LN_BLOCKS = 128;

__global__ __launch_bounds__(256) void k0_mod_ln(const u16* __restrict__ x, const u16* __restrict__ c,
                                                 const u16* __restrict__ wmod, float* __restrict__ modp,
                                                 u16* __restrict__ xn) {
  __shared__ float sc[2][384];
  __shared__ float red[32][2][64];
  const int tid = threadIdx.x;
  if (blockIdx.x < K0_MOD_BLOCKS) {
    const int cb = blockIdx.x % 72, kp = blockIdx.x / 72;
    const int tc = tid & 7, tr = tid >> 3;
    u32x4 w[12];
    const u16* wp = wmod + (size_t)(kp * 384 + tr) * 4608 + cb * 64 + tc * 8;
#pragma unroll
    for (int i = 0; i < 12; ++i) w[i] = *(const u32x4*)(wp + (size_t)i * 32 * 4608);
    for (int i = tid; i < 768; i += 256) {
      int b = i / 384, k = i % 384;
      float v = bf2f(c[b * 768 + kp * 384 + k]);
      float s = v / (1.f + __expf(-v));
      sc[b][k] = rbf(s);
    }
    __syncthreads();
    float acc[2][8];
#pragma unroll
    for (int b = 0; b < 2; ++b)
#pragma unroll
      for (int j = 0; j < 8; ++j) acc[b][j] = 0.f;
#pragma unroll
    for (int i = 0; i < 12; ++i) {
      float s0 = sc[0][tr + 32 * i], s1 = sc[1][tr + 32 * i];
#pragma unroll
      for (int j = 0; j < 4; ++j) {
        float a = lo_f(w[i][j]), bb = hi_f(w[i][j]);
        acc[0][2 * j] += s0 * a;
        acc[0][2 * j + 1] += s0 * bb;
        acc[1][2 * j] += s1 * a;
        acc[1][2 * j + 1] += s1 * bb;
      }
    }
#pragma unroll
    for (int b = 0; b < 2; ++b)
#pragma unroll
      for (int j = 0; j < 8; ++j) red[tr][b][tc * 8 + j] = acc[b][j];
    __syncthreads();
    if (tid < 128) {
      int b = tid >> 6, col = tid & 63;
      float s = 0.f;
#pragma unroll
      for (int r = 0; r < 32; ++r) s += red[r][b][col];
      modp[(kp * 2 + b) * 4608 + cb * 64 + col] = s;
    }
  } else {
    const int blk = blockIdx.x - K0_MOD_BLOCKS;
    const int w = tid >> 6, l = tid & 63;
    u32x2 v[4][3];
#pragma unroll
    for (int r = 0; r < 4; ++r) {
      const u16* xp = x + (size_t)(blk * 16 + w * 4 + r) * 768 + l * 4;
#pragma unroll
      for (int i = 0; i < 3; ++i) v[r][i] = *(const u32x2*)(xp + i * 256);
    }
#pragma unroll
    for (int r = 0; r < 4; ++r) {
      float f[12];
#pragma unroll
      for (int i = 0; i < 3; ++i) {
        f[4 * i] = lo_f(v[r][i][0]);
        f[4 * i + 1] = hi_f(v[r][i][0]);
        f[4 * i + 2] = lo_f(v[r][i][1]);
        f[4 * i + 3] = hi_f(v[r][i][1]);
      }
      float s = 0.f;
#pragma unroll
      for (int i = 0; i < 12; ++i) s += f[i];
#pragma unroll
      for (int o = 32; o > 0; o >>= 1) s += __shfl_xor(s, o);
      float mean = s * (1.f / 768.f);
      float q = 0.f;
#pragma unroll
      for (int i = 0; i < 12; ++i) {
        float d = f[i] - mean;
        q += d * d;
      }
#pragma unroll
      for (int o = 32; o > 0; o >>= 1) q += __shfl_xor(q, o);
      const float mb = rbf(mean);
      const float den = rbf(sqrtf(rbf(rbf(q * (1.f / 768.f)) + 1e-5f)));
#pragma unroll
      for (int i = 0; i < 12; ++i) f[i] = rbf(f[i] - mb) / den;
      const float rs = 1.f;
      u16* op = xn + (size_t)(blk * 16 + w * 4 + r) * 768 + l * 4;
#pragma unroll
      for (int i = 0; i < 3; ++i) {
        u32x2 o;
        o[0] = pack2(f[4 * i] * rs, f[4 * i + 1] * rs);
        o[1] = pack2(f[4 * i + 2] * rs, f[4 * i + 3] * rs);
        *(u32x2*)(op + i * 256) = o;
      }
    }
  }
}

template <int BN, int NS, int EPI>
__global__ __launch_bounds__(768, 3) void k_gemm_big(const u16* __restrict__ A, const u16* __restrict__ W,
                                                     const float* __restrict__ modp,
                                                     const f32x2* __restrict__ stats, u16* __restrict__ out,
                                                     u16* __restrict__ out2) {
  constexpr int NC = BN / 16;
  constexpr int LDW = (EPI == 0) ? 2304 : 3072;
  constexpr int LDA = 768;
  constexpr int ASZ = 128 * 128;
  constexpr int BSZ = 8 * NC * 256;
  constexpr int STG = ASZ + BSZ;
  constexpr int SUBW = 2 * NC;
  constexpr int NBI = (SUBW + 3) / 4;
  constexpr int NLD = 4 + NBI;
  constexpr int NK = 768 / 64;
  __shared__ __attribute__((aligned(16))) char smem_raw[NS * STG + 6144];
  LDS_AS char* smem = (LDS_AS char*)smem_raw;
  const unsigned lds0 = (unsigned)(uintptr_t)smem;
  constexpr int KID = (EPI == 0) ? 1 : 6;
  STAMP(KID, 0);
  const int tid = threadIdx.x;
  const int l = tid & 63;
  const int w = uni(tid >> 6);
  const int bid = blockIdx.x;
  const int xcd = bid & 7, idx = bid >> 3;
  const int tm = (xcd >> 1) * 4 + (idx & 3);
  const int tn = (xcd & 1) * 8 + (idx >> 2);
  const int m0 = tm * 128, n0 = tn * BN;
  const int b = m0 >> 10;

  if (w >= 8) {
    const int lw = w - 8;
    unsigned voffB[NBI];
#pragma unroll
    for (int i = 0; i < NBI; ++i) {
      int sub = SUBW * lw + 4 * i + (l >> 4);
      if (sub >= SUBW * (lw + 1)) sub = SUBW * (lw + 1) - 1;
      int kb = sub / NC, cc = sub % NC;
      int within = l & 15;
      int r = (within >> 1) ^ ((kb & 1) << 2);
      voffB[i] = (unsigned)(((kb * 8 + r) * LDW + cc * 16 + (within & 1) * 8) * 2);
    }
    const char* bbase = (const char*)(W + n0);
    auto issueB = [&](int kt, int st) {
      const unsigned sa = lds0 + st * STG;
      const char* sb = bbase + (size_t)kt * 64 * LDW * 2;
#pragma unroll
      for (int i = 0; i < NBI; ++i) {
        unsigned dst = sa + ASZ + (SUBW * lw + 4 * i) * 256;
        if (4 * i + 4 <= SUBW) {
          glds16(sb, voffB[i], dst);
        } else {
          if (l < (SUBW - 4 * i) * 16) glds16(sb, voffB[i], dst);
        }
      }
    };
    const int ch = l & 7, r0 = 32 * lw + (l >> 3);
    const u16* ap = A + (size_t)(m0 + r0) * LDA + ch * 8;
    u32x4 ra[3][4];
    auto loadA = [&](int kt, u32x4(&dst)[4]) {
#pragma unroll
      for (int i = 0; i < 4; ++i) gld16(dst[i], ap + (size_t)i * 8 * LDA + kt * 64);
    };
    loadA(0, ra[0]);
    loadA(1, ra[1]);
    issueB(0, 0);
    loadA(2, ra[2]);
    float rinv[4] = {1.f, 1.f, 1.f, 1.f}, mb[4] = {0.f, 0.f, 0.f, 0.f};
    if constexpr (EPI == 1) {
#pragma unroll
      for (int i = 0; i < 4; ++i) {
        const f32x4* sp = (const f32x4*)(stats + (size_t)(m0 + r0 + 8 * i) * 16);
        f32x4 sv[8];
#pragma unroll
        for (int j = 0; j < 8; ++j) sv[j] = sp[j];
        float s = 0.f;
#pragma unroll
        for (int j = 0; j < 8; ++j) s += sv[j][0] + sv[j][2];
        float mean = s * (1.f / 768.f);
        float m2 = 0.f;
#pragma unroll
        for (int j = 0; j < 8; ++j) {
          float d0 = sv[j][0] * (1.f / 48.f) - mean;
          float d1 = sv[j][2] * (1.f / 48.f) - mean;
          m2 += sv[j][1] + sv[j][3] + 48.f * (d0 * d0 + d1 * d1);
        }
        mb[i] = rbf(mean);
        rinv[i] = 1.f / rbf(sqrtf(rbf(rbf(m2 * (1.f / 768.f)) + 1e-5f)));
      }
    }
    bar();
    const LDS_AS char* gtp = smem + NS * STG + ch * 32;
    const unsigned awoff = (unsigned)(r0 * 128 + ((ch ^ (l >> 3)) << 4));
    auto xformA = [&](int kt, int st, u32x4(&src)[4]) {
      f32x4 g0 = lds_read16f(gtp + kt * 256);
      f32x4 g1 = lds_read16f(gtp + kt * 256 + 16);
      f32x4 t0 = lds_read16f(gtp + 3072 + kt * 256);
      f32x4 t1 = lds_read16f(gtp + 3072 + kt * 256 + 16);
#pragma unroll
      for (int i = 0; i < 4; ++i) {
        pin(src[i]);
        u32x4 o;
#pragma unroll
        for (int j = 0; j < 4; ++j) {
          float v0 = lo_f(src[i][j]), v1 = hi_f(src[i][j]);
          if constexpr (EPI == 1) {
            unsigned d = pack2(v0 - mb[i], v1 - mb[i]);
            d = pack2(lo_f(d) * rinv[i], hi_f(d) * rinv[i]);
            v0 = lo_f(d);
            v1 = hi_f(d);
          }
          const float ga = (j < 2) ? g0[2 * j] : g1[2 * j - 4], gb = (j < 2) ? g0[2 * j + 1] : g1[2 * j - 3];
          const float ta = (j < 2) ? t0[2 * j] : t1[2 * j - 4], tb = (j < 2) ? t0[2 * j + 1] : t1[2 * j - 3];
          unsigned y = pack2(v0 * ga, v1 * gb);
          o[j] = pack2(lo_f(y) + ta, hi_f(y) + tb);
        }
        *(LDS_AS u32x4*)(smem + st * STG + awoff + i * 1024) = o;
      }
    };
    auto step = [&](auto S) {
      constexpr int s = S.value;
      if constexpr (s > 0) {
        issueB(s, s % NS);
        if constexpr (s + 2 < NK) loadA(s + 2, ra[(s + 2) % 3]);
      }
      constexpr int cnt = NBI * ((s >= 1 ? 1 : 0) + 1) + 4 * ((s + 1 < NK ? 1 : 0) + (s + 2 < NK ? 1 : 0));
      wait_vm<cnt>();
      xformA(s, s % NS, ra[s % 3]);
    };
    static_for<0, NS - 1>(step);
    static_for<0, NK>([&](auto KT) {
      constexpr int kt = KT.value;
      constexpr int cnt = [] {
        int c = (kt + 2 < NK) ? 4 : 0;
        for (int t = kt + 1; t <= kt + NS - 2; ++t) c += (t < NK ? NBI : 0) + (t + 2 < NK ? 4 : 0);
        return c;
      }();
      wait_vm<cnt>();
      wait_lgkm0();
      bar();
      if constexpr (kt + NS - 1 < NK) step(std::integral_constant<int, kt + NS - 1>{});
    });
    bar();
    bar();
    return;
  }

  if (tid < 384) {
    const int offS = (EPI == 0) ? 0 : 3 * 768;
    const int o = (tid < 192) ? (offS + 768 + 4 * tid) : (offS + 4 * (tid - 192));
    f32x4 a = *(const f32x4*)(modp + (0 * 2 + b) * 4608 + o);
    f32x4 bb = *(const f32x4*)(modp + (1 * 2 + b) * 4608 + o);
    f32x4 r;
    if (tid < 192) {
#pragma unroll
      for (int j = 0; j < 4; ++j) r[j] = rbf(1.f + rbf(a[j] + bb[j]));
    } else {
#pragma unroll
      for (int j = 0; j < 4; ++j) r[j] = rbf(a[j] + bb[j]);
    }
    *(LDS_AS f32x4*)(smem + NS * STG + tid * 16) = r;
  }
  wait_lgkm0();
  bar();
  const int wm = w & 3, kh = w >> 2;
  const int g = l >> 4;
  const unsigned aoff = (unsigned)(((32 * wm + (l & 15)) * 128 + ((g ^ (l & 7)) << 4)) ^ (kh * 64));
  const int q = (l & 15) >> 2, p = l & 3;
  const unsigned boff_t0 = (unsigned)(ASZ + (4 * kh + g) * NC * 256 + ((4 * (g & 1) + q) * 32) + p * 8);
  const unsigned boff_t1 = (unsigned)(ASZ + (4 * kh + g) * NC * 256 + ((4 * (1 - (g & 1)) + q) * 32) + p * 8);

  f32x4 acc[2][NC];
#pragma unroll
  for (int i = 0; i < 2; ++i)
#pragma unroll
    for (int j = 0; j < NC; ++j) acc[i][j] = (f32x4){0.f, 0.f, 0.f, 0.f};
  STAMP(KID, 1);

  int st = 0;
#pragma unroll 1
  for (int kt = 0; kt < NK; ++kt) {
    wait_lgkm0();
    bar();
    const LDS_AS char* sp = smem + st * STG;
    bf16x8 xb[2];
#pragma unroll
    for (int mf = 0; mf < 2; ++mf) xb[mf] = lds_read16(sp + aoff + mf * 2048);
#pragma unroll
    for (int cc = 0; cc < NC; ++cc) {
      bf16x4 w0 = lds_tr(sp + boff_t0 + cc * 256);
      bf16x4 w1 = lds_tr(sp + boff_t1 + cc * 256);
      bf16x8 wf = cat4(w0, w1);
      acc[0][cc] = mfma16(wf, xb[0], acc[0][cc]);
      acc[1][cc] = mfma16(wf, xb[1], acc[1][cc]);
    }
    st = (st + 1 == NS) ? 0 : st + 1;
  }
  STAMP(KID, 14);

  wait_lgkm0();
  bar();
  {
    LDS_AS char* rp = smem + (wm * 2 + kh) * NC * 1024 + l * 16;
    if (kh) {
#pragma unroll
      for (int cc = 0; cc < NC; ++cc) {
        *(LDS_AS f32x4*)(rp + cc * 1024) = acc[0][cc];
        acc[0][cc] = acc[1][cc];
      }
    } else {
#pragma unroll
      for (int cc = 0; cc < NC; ++cc) *(LDS_AS f32x4*)(rp + cc * 1024) = acc[1][cc];
    }
  }
  wait_lgkm0();
  bar();
  const int row = m0 + 32 * wm + 16 * kh + (l & 15);
  const LDS_AS char* rp = smem + (wm * 2 + (1 - kh)) * NC * 1024 + l * 16;
  {
    unsigned pk[NC][2];
#pragma unroll
    for (int cc = 0; cc < NC; ++cc) {
      f32x4 v = acc[0][cc] + lds_read16f(rp + cc * 1024);
      if constexpr (EPI == 1) {
        const float c1 = -2.302208198144325f;
        const float c2 = -0.1029432397500235f;
        unsigned y01 = pack2(v[0], v[1]), y23 = pack2(v[2], v[3]);
        float y[4] = {lo_f(y01), hi_f(y01), lo_f(y23), hi_f(y23)};
#pragma unroll
        for (int j = 0; j < 4; ++j) {
          float e = __builtin_amdgcn_exp2f(y[j] * (c1 + c2 * y[j] * y[j]));
          v[j] = y[j] * __builtin_amdgcn_rcpf(1.f + e);
        }
      }
      pk[cc][0] = pack2(v[0], v[1]);
      pk[cc][1] = pack2(v[2], v[3]);
    }
    auto dst_ptr = [&](int n) -> u16* {
      if constexpr (EPI == 1) {
        return out + (size_t)row * 3072 + n;
      } else {
        if (n < 1536) return out + (size_t)row * 1536 + n;
        int nv = n - 1536;
        int hd = nv / 96;
        int dd = nv - hd * 96;
        return out2 + ((size_t)((b * 8 + hd) * 1024 + (row & 1023))) * 96 + dd;
      }
    };
#pragma unroll
    for (int a = 0; a < NC / 2; ++a) {
      u32x4 o = pair_pack(pk[2 * a][0], pk[2 * a][1], pk[2 * a + 1][0], pk[2 * a + 1][1]);
      *(u32x4*)dst_ptr(n0 + 32 * a + (g & 1) * 16 + (g & 2) * 4) = o;
    }
    if constexpr (NC & 1) {
      u32x2 o;
      o[0] = pk[NC - 1][0];
      o[1] = pk[NC - 1][1];
      *(u32x2*)dst_ptr(n0 + 16 * (NC - 1) + 4 * g) = o;
    }
  }
  STAMP(KID, 15);
}

__global__ __launch_bounds__(768, 3) void k_scores(const u16* __restrict__ qk, u16* __restrict__ P,
                                                   f32x2* __restrict__ ml) {
  constexpr int NS = 4;
  constexpr int ASZ = 128 * 128;
  constexpr int STG = 2 * ASZ;
  constexpr int NK = 384 / 64;
  constexpr int NLD = 8;
  constexpr int LDQ = 1536;
  __shared__ __attribute__((aligned(16))) char smem_raw[NS * STG];
  LDS_AS char* smem = (LDS_AS char*)smem_raw;
  const unsigned lds0 = (unsigned)(uintptr_t)smem;
  STAMP(2, 0);
  const int tid = threadIdx.x, l = tid & 63;
  const int w = uni(tid >> 6);
  const int bid = blockIdx.x;
  const int xcd = bid & 7, idx = bid >> 3;
  const int bh = xcd >> 1;
  const int tq = (xcd & 1) * 4 + (idx & 3);
  const int tk = idx >> 2;
  const int b = bh >> 1, h = bh & 1;

  if (w >= 8) {
    const int lw = w - 8;
    const unsigned voff = (unsigned)((l >> 3) * LDQ * 2 + (((l & 7) ^ (l >> 3)) << 4));
    const char* qbase = (const char*)(qk + (size_t)(b * 1024 + tq * 128 + 32 * lw) * LDQ + h * 384);
    const char* kbase = (const char*)(qk + (size_t)(b * 1024 + tk * 128 + 32 * lw) * LDQ + 768 + h * 384);
    auto issue = [&](int kt, int st) {
      const unsigned sa = lds0 + st * STG;
#pragma unroll
      for (int i = 0; i < 4; ++i)
        glds16(qbase + (size_t)i * 8 * LDQ * 2 + kt * 128, voff, sa + (32 * lw + 8 * i) * 128);
#pragma unroll
      for (int i = 0; i < 4; ++i)
        glds16(kbase + (size_t)i * 8 * LDQ * 2 + kt * 128, voff, sa + ASZ + (32 * lw + 8 * i) * 128);
    };
#pragma unroll
    for (int s = 0; s < NS - 1; ++s) issue(s, s);
    int stn = NS - 1;
#pragma unroll 1
    for (int kt = 0; kt < NK; ++kt) {
      if (kt < NK - (NS - 2)) wait_vm<(NS - 2) * NLD>();
      else wait_vm<0>();
      bar();
      if (kt + NS - 1 < NK) issue(kt + NS - 1, stn);
      stn = (stn + 1 == NS) ? 0 : stn + 1;
    }
    bar();
    bar();
    return;
  }

  const int wm = w & 3, kh = w >> 2;
  const int g = l >> 4;
  const unsigned qoff = (unsigned)(((32 * wm + (l & 15)) * 128 + ((g ^ (l & 7)) << 4)) ^ (kh * 64));
  const unsigned koff = (unsigned)(ASZ + (((l & 15) * 128 + ((g ^ (l & 7)) << 4)) ^ (kh * 64)));
  f32x4 acc[2][8];
#pragma unroll
  for (int i = 0; i < 2; ++i)
#pragma unroll
    for (int j = 0; j < 8; ++j) acc[i][j] = (f32x4){0.f, 0.f, 0.f, 0.f};

  int st = 0;
#pragma unroll 1
  for (int kt = 0; kt < NK; ++kt) {
    wait_lgkm0();
    bar();
    const LDS_AS char* sp = smem + st * STG;
    bf16x8 qf[2];
#pragma unroll
    for (int mf = 0; mf < 2; ++mf) qf[mf] = lds_read16(sp + qoff + mf * 2048);
#pragma unroll
    for (int kf = 0; kf < 8; ++kf) {
      bf16x8 kfr = lds_read16(sp + koff + kf * 2048);
      acc[0][kf] = mfma16(kfr, qf[0], acc[0][kf]);
      acc[1][kf] = mfma16(kfr, qf[1], acc[1][kf]);
    }
    st = (st + 1 == NS) ? 0 : st + 1;
  }
  STAMP(2, 14);
  wait_lgkm0();
  bar();
  {
    LDS_AS char* rp = smem + (wm * 2 + kh) * 8 * 1024 + l * 16;
    if (kh) {
#pragma unroll
      for (int kf = 0; kf < 8; ++kf) {
        *(LDS_AS f32x4*)(rp + kf * 1024) = acc[0][kf];
        acc[0][kf] = acc[1][kf];
      }
    } else {
#pragma unroll
      for (int kf = 0; kf < 8; ++kf) *(LDS_AS f32x4*)(rp + kf * 1024) = acc[1][kf];
    }
  }
  wait_lgkm0();
  bar();
  f32x4 s[8];
  {
    const LDS_AS char* rp = smem + (wm * 2 + (1 - kh)) * 8 * 1024 + l * 16;
#pragma unroll
    for (int kf = 0; kf < 8; ++kf) {
      f32x4 v = acc[0][kf] + lds_read16f(rp + kf * 1024);
#pragma unroll
      for (int j = 0; j < 4; ++j) v[j] = rbf(rbf(v[j]) * 0.051031036307982884f);
      s[kf] = v;
    }
  }
  const float l2e = 1.4426950408889634f;
  float mx = -INFINITY;
#pragma unroll
  for (int kf = 0; kf < 8; ++kf)
#pragma unroll
    for (int j = 0; j < 4; ++j) mx = fmaxf(mx, s[kf][j]);
  mx = fmaxf(mx, __shfl_xor(mx, 16));
  mx = fmaxf(mx, __shfl_xor(mx, 32));
  const float mbl = mx * l2e;
  float sum = 0.f;
  unsigned pk[8][2];
#pragma unroll
  for (int kf = 0; kf < 8; ++kf) {
    float e[4];
#pragma unroll
    for (int j = 0; j < 4; ++j) {
      e[j] = __builtin_amdgcn_exp2f(s[kf][j] * l2e - mbl);
      sum += e[j];
    }
    pk[kf][0] = pack2(e[0], e[1]);
    pk[kf][1] = pack2(e[2], e[3]);
  }
  sum += __shfl_xor(sum, 16);
  sum += __shfl_xor(sum, 32);
  const int qrow = tq * 128 + 32 * wm + 16 * kh + (l & 15);
  u16* op = P + ((size_t)bh * 1024 + qrow) * 1024 + tk * 128;
#pragma unroll
  for (int a = 0; a < 4; ++a) {
    u32x4 o = pair_pack(pk[2 * a][0], pk[2 * a][1], pk[2 * a + 1][0], pk[2 * a + 1][1]);
    *(u32x4*)(op + 32 * a + (g & 1) * 16 + (g & 2) * 4) = o;
  }
  if (g == 0) {
    f32x2 s2;
    s2[0] = mbl;
    s2[1] = sum;
    ml[((size_t)bh * 1024 + qrow) * 8 + tk] = s2;
  }
  STAMP(2, 15);
}

template <int MODE>
__global__ __launch_bounds__(512, 2) void k_gemm_small(const u16* __restrict__ Ain, const u16* __restrict__ Bin,
                                                       const float* __restrict__ modp,
                                                       const u16* __restrict__ resid,
                                                       const f32x2* __restrict__ ml, f32x2* __restrict__ stats,
                                                       u16* __restrict__ out) {
  constexpr int KTOT = (MODE == 0) ? 1024 : (MODE == 1 ? 768 : 3072);
  constexpr int KH = KTOT / 2;
  constexpr int NK = KH / 64;
  constexpr int LDA = KTOT;
  constexpr int LDB = (MODE == 0) ? 96 : 768;
  constexpr int NS = 4;
  constexpr int STG = 40960;
  constexpr int NLD = 10;
  __shared__ __attribute__((aligned(16))) char smem_raw[NS * STG];
  LDS_AS char* smem = (LDS_AS char*)smem_raw;
  const unsigned lds0 = (unsigned)(uintptr_t)smem;
  STAMP(3 + MODE, 0);
  const int tid = threadIdx.x, l = tid & 63;
  const int w = uni(tid >> 6);
  const int bid = blockIdx.x;
  const int xcd = bid & 7, idx = bid >> 3;
  const int g = l >> 4;

  int m0, n0, bh = 0;
  const u16* Ap;
  const u16* Bp;
  if constexpr (MODE == 0) {
    bh = xcd >> 1;
    const int tq = (xcd & 1) * 8 + (idx & 7);
    const int td = idx >> 3;
    m0 = tq * 64;
    n0 = td * 96;
    Ap = Ain + ((size_t)bh * 1024 + m0) * 1024;
    Bp = Bin + ((size_t)(bh * 4 + td) * 1024) * 96;
  } else {
    const int tm = xcd * 4 + (idx & 3);
    const int tn = idx >> 2;
    m0 = tm * 64;
    n0 = tn * 96;
    Ap = Ain + (size_t)m0 * LDA;
    Bp = Bin + n0;
  }

  if (w >= 4) {
    const int lw = w - 4;
    const int kq = lw & 1, part = lw >> 1;
    const unsigned voffA = (unsigned)((l >> 3) * LDA * 2 + (((l & 7) ^ (l >> 3)) << 4));
    unsigned voffB[6];
#pragma unroll
    for (int i = 0; i < 6; ++i) {
      int sub = 24 * part + 4 * i + (l >> 4);
      int kb = sub / 6, cc = sub % 6;
      int within = l & 15;
      int r = (within >> 1) ^ ((kb & 1) << 2);
      voffB[i] = (unsigned)(((kb * 8 + r) * LDB + cc * 16 + (within & 1) * 8) * 2);
    }
    const char* abase = (const char*)(Ap + (size_t)(32 * part) * LDA + kq * KH);
    const char* bbase = (const char*)(Bp + (size_t)(kq * KH) * LDB);
    auto issue = [&](int kt, int st) {
      const unsigned sa = lds0 + st * STG;
#pragma unroll
      for (int i = 0; i < 4; ++i)
        glds16(abase + (size_t)i * 8 * LDA * 2 + kt * 128, voffA, sa + kq * 8192 + (32 * part + 8 * i) * 128);
      const char* sb = bbase + (size_t)kt * 64 * LDB * 2;
#pragma unroll
      for (int i = 0; i < 6; ++i) glds16(sb, voffB[i], sa + 16384 + kq * 12288 + (24 * part + 4 * i) * 256);
    };
#pragma unroll
    for (int s = 0; s < NS - 1; ++s) issue(s, s);
    int stn = NS - 1;
#pragma unroll 1
    for (int kt = 0; kt < NK; ++kt) {
      if (kt < NK - (NS - 2)) wait_vm<(NS - 2) * NLD>();
      else wait_vm<0>();
      bar();
      if (kt + NS - 1 < NK) issue(kt + NS - 1, stn);
      stn = (stn + 1 == NS) ? 0 : stn + 1;
    }
    bar();
    bar();
    return;
  }

  const int kq = w & 1, nh = w >> 1;
  float cf[4][4];
  if constexpr (MODE == 0) {
#pragma unroll
    for (int mf = 0; mf < 4; ++mf) {
      const f32x4* sp = (const f32x4*)(ml + ((size_t)bh * 1024 + m0 + 16 * mf + (l & 15)) * 8);
      f32x4 sv[4];
#pragma unroll
      for (int j = 0; j < 4; ++j) sv[j] = sp[j];
      float mx = fmaxf(sv[0][0], sv[0][2]);
#pragma unroll
      for (int j = 1; j < 4; ++j) mx = fmaxf(mx, fmaxf(sv[j][0], sv[j][2]));
      float e[8];
      float lsum = 0.f;
#pragma unroll
      for (int j = 0; j < 4; ++j) {
        e[2 * j] = __builtin_amdgcn_exp2f(sv[j][0] - mx);
        e[2 * j + 1] = __builtin_amdgcn_exp2f(sv[j][2] - mx);
        lsum += e[2 * j] * sv[j][1] + e[2 * j + 1] * sv[j][3];
      }
      float inv = 1.f / lsum;
      if (kq) {
#pragma unroll
        for (int j = 0; j < 4; ++j) cf[mf][j] = e[4 + j] * inv;
      } else {
#pragma unroll
        for (int j = 0; j < 4; ++j) cf[mf][j] = e[j] * inv;
      }
    }
  }
  f32x4 gq[2][3][2];
  u32x2 xr[2][3];
  if constexpr (MODE != 0) {
    const int goff = (MODE == 1) ? 2 * 768 : 5 * 768;
    const int b = m0 >> 10;
    const float* mp0 = modp + (0 * 2 + b) * 4608 + goff + n0 + 48 * nh + 4 * g;
    const float* mp1 = modp + (1 * 2 + b) * 4608 + goff + n0 + 48 * nh + 4 * g;
#pragma unroll
    for (int cc = 0; cc < 3; ++cc) {
      gq[0][cc][0] = *(const f32x4*)(mp0 + 16 * cc);
      gq[0][cc][1] = *(const f32x4*)(mp1 + 16 * cc);
    }
#pragma unroll
    for (int mf = 0; mf < 2; ++mf) {
      const u16* rp = resid + (size_t)(m0 + 32 * kq + 16 * mf + (l & 15)) * 768 + n0 + 48 * nh + 4 * g;
#pragma unroll
      for (int cc = 0; cc < 3; ++cc) xr[mf][cc] = *(const u32x2*)(rp + 16 * cc);
    }
  }

  const unsigned aoff0 = (unsigned)(kq * 8192 + (l & 15) * 128 + ((g ^ (l & 7)) << 4));
  const int q = (l & 15) >> 2, p = l & 3;
  const unsigned boff_t0 = (unsigned)(16384 + kq * 12288 + (g * 6 + 3 * nh) * 256 + ((4 * (g & 1) + q) * 32) + p * 8);
  const unsigned boff_t1 =
      (unsigned)(16384 + kq * 12288 + (g * 6 + 3 * nh) * 256 + ((4 * (1 - (g & 1)) + q) * 32) + p * 8);

  f32x4 acc[4][3];
  f32x4 tot[4][3];
#pragma unroll
  for (int i = 0; i < 4; ++i)
#pragma unroll
    for (int j = 0; j < 3; ++j) {
      acc[i][j] = (f32x4){0.f, 0.f, 0.f, 0.f};
      tot[i][j] = (f32x4){0.f, 0.f, 0.f, 0.f};
    }
  STAMP(3 + MODE, 1);

  int st = 0;
#pragma unroll 1
  for (int kt = 0; kt < NK; ++kt) {
    wait_lgkm0();
    bar();
    const LDS_AS char* sp = smem + st * STG;
#pragma unroll
    for (int ks = 0; ks < 2; ++ks) {
      bf16x8 af[4];
#pragma unroll
      for (int mf = 0; mf < 4; ++mf) af[mf] = lds_read16(sp + ((aoff0 ^ (ks * 64)) + mf * 2048));
#pragma unroll
      for (int cc = 0; cc < 3; ++cc) {
        bf16x4 w0 = lds_tr(sp + boff_t0 + ks * 6144 + cc * 256);
        bf16x4 w1 = lds_tr(sp + boff_t1 + ks * 6144 + cc * 256);
        bf16x8 wf = cat4(w0, w1);
#pragma unroll
        for (int mf = 0; mf < 4; ++mf) acc[mf][cc] = mfma16(wf, af[mf], acc[mf][cc]);
      }
    }
    if constexpr (MODE == 0) {
      if (kt & 1) {
#pragma unroll
        for (int mf = 0; mf < 4; ++mf) {
          const float c = cf[mf][0];
          cf[mf][0] = cf[mf][1];
          cf[mf][1] = cf[mf][2];
          cf[mf][2] = cf[mf][3];
#pragma unroll
          for (int cc = 0; cc < 3; ++cc) {
            tot[mf][cc] += acc[mf][cc] * c;
            acc[mf][cc] = (f32x4){0.f, 0.f, 0.f, 0.f};
          }
        }
      }
    }
    st = (st + 1 == NS) ? 0 : st + 1;
  }
  if constexpr (MODE == 0) {
#pragma unroll
    for (int mf = 0; mf < 4; ++mf)
#pragma unroll
      for (int cc = 0; cc < 3; ++cc) acc[mf][cc] = tot[mf][cc];
  }
  STAMP(3 + MODE, 12);
  wait_lgkm0();
  bar();
  {
    LDS_AS char* rp = smem + (nh * 2 + kq) * 6144 + l * 16;
    if (kq) {
#pragma unroll
      for (int mf = 0; mf < 2; ++mf)
#pragma unroll
        for (int cc = 0; cc < 3; ++cc) {
          *(LDS_AS f32x4*)(rp + (mf * 3 + cc) * 1024) = acc[mf][cc];
          acc[mf][cc] = acc[2 + mf][cc];
        }
    } else {
#pragma unroll
      for (int mf = 0; mf < 2; ++mf)
#pragma unroll
        for (int cc = 0; cc < 3; ++cc) *(LDS_AS f32x4*)(rp + (mf * 3 + cc) * 1024) = acc[2 + mf][cc];
    }
  }
  wait_lgkm0();
  bar();
  f32x4 r[2][3];
  {
    const LDS_AS char* rp = smem + (nh * 2 + (1 - kq)) * 6144 + l * 16;
#pragma unroll
    for (int mf = 0; mf < 2; ++mf)
#pragma unroll
      for (int cc = 0; cc < 3; ++cc)
        r[mf][cc] = acc[mf][cc] + lds_read16f(rp + (mf * 3 + cc) * 1024);
  }
  STAMP(3 + MODE, 13);
#pragma unroll
  for (int mf = 0; mf < 2; ++mf) {
    const int mrow = m0 + 32 * kq + 16 * mf + (l & 15);
    float v[3][4];
    u16* op;
    if constexpr (MODE == 0) {
      op = out + (size_t)((bh >> 1) * 1024 + mrow) * 768 + (bh & 1) * 384 + n0 + 48 * nh;
#pragma unroll
      for (int cc = 0; cc < 3; ++cc)
#pragma unroll
        for (int j = 0; j < 4; ++j) v[cc][j] = r[mf][cc][j];
    } else {
      op = out + (size_t)mrow * 768 + n0 + 48 * nh;
#pragma unroll
      for (int cc = 0; cc < 3; ++cc) {
        float xv[4] = {lo_f(xr[mf][cc][0]), hi_f(xr[mf][cc][0]), lo_f(xr[mf][cc][1]), hi_f(xr[mf][cc][1])};
#pragma unroll
        for (int j = 0; j < 4; ++j) {
          float gate = rbf(gq[0][cc][0][j] + gq[0][cc][1][j]);
          v[cc][j] = rbf(xv[j] + rbf(gate * rbf(r[mf][cc][j])));
        }
      }
    }
    {
      u32x4 o = pair_pack(pack2(v[0][0], v[0][1]), pack2(v[0][2], v[0][3]), pack2(v[1][0], v[1][1]),
                          pack2(v[1][2], v[1][3]));
      *(u32x4*)(op + (g & 1) * 16 + (g & 2) * 4) = o;
      u32x2 o2;
      o2[0] = pack2(v[2][0], v[2][1]);
      o2[1] = pack2(v[2][2], v[2][3]);
      *(u32x2*)(op + 32 + 4 * g) = o2;
    }
    if constexpr (MODE == 1) {
      float s = 0.f;
#pragma unroll
      for (int cc = 0; cc < 3; ++cc)
#pragma unroll
        for (int j = 0; j < 4; ++j) s += v[cc][j];
      s += __shfl_xor(s, 16);
      s += __shfl_xor(s, 32);
      float mean = s * (1.f / 48.f);
      float m2 = 0.f;
#pragma unroll
      for (int cc = 0; cc < 3; ++cc)
#pragma unroll
        for (int j = 0; j < 4; ++j) {
          float d = v[cc][j] - mean;
          m2 += d * d;
        }
      m2 += __shfl_xor(m2, 16);
      m2 += __shfl_xor(m2, 32);
      if (g == 0) {
        f32x2 s2;
        s2[0] = s;
        s2[1] = m2;
        stats[(size_t)mrow * 16 + (n0 / 48) + nh] = s2;
      }
    }
  }
  STAMP(3 + MODE, 15);
}

extern "C" void kernel_launch(void* const* d_in, const int* in_sizes, int n_in, void* d_out, int out_size,
                              void* d_ws, size_t ws_size, hipStream_t stream) {
  const u16* x = (const u16*)d_in[0];
  const u16* c = (const u16*)d_in[1];
  const u16* wqkv = (const u16*)d_in[2];
  const u16* wo = (const u16*)d_in[3];
  const u16* wmod = (const u16*)d_in[4];
  const u16* wfc1 = (const u16*)d_in[5];
  const u16* wfc2 = (const u16*)d_in[6];
  u16* out = (u16*)d_out;

  char* ws = (char*)d_ws;
  size_t off = 0;
  auto alloc = [&](size_t bytes) {
    char* p = ws + off;
    off += (bytes + 255) & ~(size_t)255;
    return p;
  };
  float* modp = (float*)alloc(2 * 2 * 4608 * 4);
  u16* xn = (u16*)alloc((size_t)Mrows * 768 * 2);
  u16* qk = (u16*)alloc((size_t)Mrows * 1536 * 2);
  u16* Vt = (u16*)alloc((size_t)Mrows * 768 * 2);
  u16* P = (u16*)alloc((size_t)4 * 1024 * 1024 * 2);
  f32x2* ml = (f32x2*)alloc((size_t)4 * 1024 * 8 * 8);
  u16* O = (u16*)alloc((size_t)Mrows * 768 * 2);
  u16* x1 = (u16*)alloc((size_t)Mrows * 768 * 2);
  f32x2* st2 = (f32x2*)alloc((size_t)Mrows * 16 * 8);
  u16* u = (u16*)alloc((size_t)Mrows * 3072 * 2);

  k0_mod_ln<<<K0_MOD_BLOCKS + K0_LN_BLOCKS, 256, 0, stream>>>(x, c, wmod, modp, xn);
  k_gemm_big<144, 4, 0><<<256, 768, 0, stream>>>(xn, wqkv, modp, nullptr, qk, Vt);
  k_scores<<<256, 768, 0, stream>>>(qk, P, ml);
  k_gemm_small<0><<<256, 512, 0, stream>>>(P, Vt, modp, nullptr, ml, nullptr, O);
  k_gemm_small<1><<<256, 512, 0, stream>>>(O, wo, modp, x, nullptr, st2, x1);
  k_gemm_big<192, 3, 1><<<256, 768, 0, stream>>>(x1, wfc1, modp, st2, u, nullptr);
  k_gemm_small<2><<<256, 512, 0, stream>>>(u, wfc2, modp, x1, nullptr, nullptr, out);
}
```

```cpp
#include <hip/hip_runtime.h>
#include <hip/hip_bf16.h>
#include <stdint.h>
#include <type_traits>

typedef __attribute__((ext_vector_type(8))) __bf16 bf16x8;
typedef __attribute__((ext_vector_type(4))) __bf16 bf16x4;
typedef __attribute__((ext_vector_type(2))) __bf16 bf16x2;
typedef __attribute__((ext_vector_type(4))) float f32x4;
typedef __attribute__((ext_vector_type(2))) float f32x2;
typedef __attribute__((ext_vector_type(4))) unsigned u32x4;
typedef __attribute__((ext_vector_type(2))) unsigned u32x2;
typedef unsigned short u16;
#define LDS_AS __attribute__((address_space(3)))

#define DEVI __device__ __forceinline__
#define STAMP(K, I)

static constexpr int Bsz = 2, Nseq = 1024, Dm = 768, Ff = 3072, Dh = 384;
static constexpr int Mrows = Bsz * Nseq;

template <int I, int N, class F>
DEVI void static_for(F&& f) {
  if constexpr (I < N) {
    f(std::integral_constant<int, I>{});
    static_for<I + 1, N>(f);
  }
}

DEVI float lo_f(unsigned u) { return __uint_as_float(u << 16); }
DEVI float hi_f(unsigned u) { return __uint_as_float(u & 0xffff0000u); }
DEVI float bf2f(u16 h) { return __uint_as_float(((unsigned)h) << 16); }
DEVI unsigned pack2(float a, float b) {
  bf16x2 v;
  v[0] = (__bf16)a;
  v[1] = (__bf16)b;
  return __builtin_bit_cast(unsigned, v);
}
DEVI float rbf(float a) { return lo_f(pack2(a, 0.f) ); }

template <int N>
DEVI void wait_vm() { asm volatile("s_waitcnt vmcnt(%0)" ::"i"(N) : "memory"); }
DEVI void wait_lgkm0() { asm volatile("s_waitcnt lgkmcnt(0)" ::: "memory"); }
DEVI void bar() { __builtin_amdgcn_s_barrier(); }

DEVI void glds16(const void* sbase, unsigned voff, unsigned lds_dst) {
  unsigned keep;
  asm volatile(
      "s_mov_b32 %0, m0\n\t"
      "s_mov_b32 m0, %3\n\t"
      "s_nop 0\n\t"
      "global_load_lds_dwordx4 %1, %2\n\t"
      "s_mov_b32 m0, %0"
      : "=&s"(keep)
      : "v"(voff), "s"(sbase), "s"(lds_dst)
      : "memory");
}

DEVI void gld16(u32x4& d, const void* p) { asm volatile("global_load_dwordx4 %0, %1, off" : "=v"(d) : "v"(p) : "memory"); }
DEVI void gld8(u32x2& d, const void* p) { asm volatile("global_load_dwordx2 %0, %1, off" : "=v"(d) : "v"(p) : "memory"); }
template <class T>
DEVI void pin(T& v) { asm volatile("" : "+v"(v)); }
DEVI f32x4 asf4(u32x4 v) { return __builtin_bit_cast(f32x4, v); }

DEVI bf16x8 lds_read16(const LDS_AS char* p) { return *(const LDS_AS bf16x8*)p; }
DEVI u32x4 lds_read16u(const LDS_AS char* p) { return *(const LDS_AS u32x4*)p; }
DEVI f32x4 lds_read16f(const LDS_AS char* p) { return *(const LDS_AS f32x4*)p; }
DEVI bf16x4 lds_tr(const LDS_AS char* p) {
  return __builtin_amdgcn_ds_read_tr16_b64_v4bf16((LDS_AS bf16x4*)p);
}
DEVI bf16x8 cat4(bf16x4 a, bf16x4 b) {
  return __builtin_shufflevector(a, b, 0, 1, 2, 3, 4, 5, 6, 7);
}
DEVI f32x4 mfma16(bf16x8 a, bf16x8 b, f32x4 c) {
  return __builtin_amdgcn_mfma_f32_16x16x32_bf16(a, b, c, 0, 0, 0);
}
DEVI int uni(int v) { return __builtin_amdgcn_readfirstlane(v); }

DEVI u32x4 pair_pack(unsigned ax, unsigned ay, unsigned bx, unsigned by) {
  auto r0 = __builtin_amdgcn_permlane16_swap(ax, bx, false, false);
  auto r1 = __builtin_amdgcn_permlane16_swap(ay, by, false, false);
  u32x4 o;
  o[0] = r0[0];
  o[1] = r1[0];
  o[2] = r0[1];
  o[3] = r1[1];
  return o;
}

__global__ __launch_bounds__(256) void k0_mod(const u16* __restrict__ c, const u16* __restrict__ wmod,
                                              float* __restrict__ modp, const u16* __restrict__ w0,
                                              const u16* __restrict__ w1) {
  __shared__ float sc[2][384];
  __shared__ float red[32][2][64];
  const int tid = threadIdx.x;
  if (blockIdx.x >= 144) {
    const int t = (blockIdx.x - 144) * 256 + tid;
    u32x4 a = {0u, 0u, 0u, 0u};
    u32x4 v[12];
#pragma unroll
    for (int i = 0; i < 12; ++i) {
      int ci = t + i * 24576;
      const u16* p = (ci < 221184) ? (w0 + (size_t)ci * 8) : (w1 + (size_t)(ci - 221184) * 8);
      v[i] = *(const u32x4*)p;
    }
#pragma unroll
    for (int i = 0; i < 12; ++i) a ^= v[i];
    asm volatile("" ::"v"(a));
    return;
  }
  const int cb = blockIdx.x % 72, kp = blockIdx.x / 72;
  const int tc = tid & 7, tr = tid >> 3;
  u32x4 w[12];
  const u16* wp = wmod + (size_t)(kp * 384 + tr) * 4608 + cb * 64 + tc * 8;
#pragma unroll
  for (int i = 0; i < 12; ++i) w[i] = *(const u32x4*)(wp + (size_t)i * 32 * 4608);
  for (int i = tid; i < 768; i += 256) {
    int b = i / 384, k = i % 384;
    float v = bf2f(c[b * 768 + kp * 384 + k]);
    float s = v / (1.f + __expf(-v));
    sc[b][k] = rbf(s);
  }
  __syncthreads();
  float acc[2][8];
#pragma unroll
  for (int b = 0; b < 2; ++b)
#pragma unroll
    for (int j = 0; j < 8; ++j) acc[b][j] = 0.f;
#pragma unroll
  for (int i = 0; i < 12; ++i) {
    float s0 = sc[0][tr + 32 * i], s1 = sc[1][tr + 32 * i];
#pragma unroll
    for (int j = 0; j < 4; ++j) {
      float a = lo_f(w[i][j]), bb = hi_f(w[i][j]);
      acc[0][2 * j] += s0 * a;
      acc[0][2 * j + 1] += s0 * bb;
      acc[1][2 * j] += s1 * a;
      acc[1][2 * j + 1] += s1 * bb;
    }
  }
#pragma unroll
  for (int b = 0; b < 2; ++b)
#pragma unroll
    for (int j = 0; j < 8; ++j) red[tr][b][tc * 8 + j] = acc[b][j];
  __syncthreads();
  if (tid < 128) {
    int b = tid >> 6, col = tid & 63;
    float s = 0.f;
#pragma unroll
    for (int r = 0; r < 32; ++r) s += red[r][b][col];
    modp[(kp * 2 + b) * 4608 + cb * 64 + col] = s;
  }
}

__global__ __launch_bounds__(256) void k_lnmod(const u16* __restrict__ x, const float* __restrict__ modp, int offS,
                                               u16* __restrict__ h, const u16* __restrict__ w0,
                                               const u16* __restrict__ w1) {
  const int tid = threadIdx.x;
  if (blockIdx.x >= 256) {
    const int t = (blockIdx.x - 256) * 256 + tid;
    u32x4 a = {0u, 0u, 0u, 0u};
    u32x4 v[12];
#pragma unroll
    for (int i = 0; i < 12; ++i) {
      int ci = t + i * 49152;
      const u16* p = (ci < 294912) ? (w0 + (size_t)ci * 8) : (w1 + (size_t)(ci - 294912) * 8);
      v[i] = *(const u32x4*)p;
    }
#pragma unroll
    for (int i = 0; i < 12; ++i) a ^= v[i];
    asm volatile("" ::"v"(a));
    return;
  }
  const int w = tid >> 6, l = tid & 63;
  const int row0 = blockIdx.x * 8 + w * 2;
  const int b = row0 >> 10;
  u32x2 v[2][3];
#pragma unroll
  for (int r = 0; r < 2; ++r) {
    const u16* xp = x + (size_t)(row0 + r) * 768 + l * 4;
#pragma unroll
    for (int i = 0; i < 3; ++i) v[r][i] = *(const u32x2*)(xp + i * 256);
  }
  f32x4 gg[3], tt[3];
#pragma unroll
  for (int i = 0; i < 3; ++i) {
    const float* m0p = modp + (0 * 2 + b) * 4608 + offS + l * 4 + i * 256;
    const float* m1p = modp + (1 * 2 + b) * 4608 + offS + l * 4 + i * 256;
    f32x4 s0 = *(const f32x4*)m0p, s1 = *(const f32x4*)m1p;
    f32x4 c0 = *(const f32x4*)(m0p + 768), c1 = *(const f32x4*)(m1p + 768);
#pragma unroll
    for (int j = 0; j < 4; ++j) {
      tt[i][j] = rbf(s0[j] + s1[j]);
      gg[i][j] = rbf(1.f + rbf(c0[j] + c1[j]));
    }
  }
#pragma unroll
  for (int r = 0; r < 2; ++r) {
    float f[12];
#pragma unroll
    for (int i = 0; i < 3; ++i) {
      f[4 * i] = lo_f(v[r][i][0]);
      f[4 * i + 1] = hi_f(v[r][i][0]);
      f[4 * i + 2] = lo_f(v[r][i][1]);
      f[4 * i + 3] = hi_f(v[r][i][1]);
    }
    float s = 0.f;
#pragma unroll
    for (int i = 0; i < 12; ++i) s += f[i];
#pragma unroll
    for (int o = 32; o > 0; o >>= 1) s += __shfl_xor(s, o);
    float mean = s * (1.f / 768.f);
    float q = 0.f;
#pragma unroll
    for (int i = 0; i < 12; ++i) {
      float d = f[i] - mean;
      q += d * d;
    }
#pragma unroll
    for (int o = 32; o > 0; o >>= 1) q += __shfl_xor(q, o);
    const float mb = rbf(mean);
    const float den = rbf(sqrtf(rbf(rbf(q * (1.f / 768.f)) + 1e-5f)));
    u16* op = h + (size_t)(row0 + r) * 768 + l * 4;
#pragma unroll
    for (int i = 0; i < 3; ++i) {
      float y[4];
#pragma unroll
      for (int j = 0; j < 4; ++j) y[j] = rbf(rbf(rbf(f[4 * i + j] - mb) / den) * gg[i][j]) + tt[i][j];
      u32x2 o;
      o[0] = pack2(y[0], y[1]);
      o[1] = pack2(y[2], y[3]);
      *(u32x2*)(op + i * 256) = o;
    }
  }
}

template <int BN, int NS, int EPI>
__global__ __launch_bounds__(768, 3) void k_gemm_big(const u16* __restrict__ A, const u16* __restrict__ W,
                                                     void* __restrict__ outv, u16* __restrict__ out2) {
  constexpr int NC = BN / 16;
  constexpr int LDW = (EPI == 0) ? 2304 : (EPI == 1 ? 3072 : 768);
  constexpr int LDA = (EPI == 2) ? 3072 : 768;
  constexpr int ASZ = 128 * 128;
  constexpr int BSZ = 8 * NC * 256;
  constexpr int STG = ASZ + BSZ;
  constexpr int SUBW = 2 * NC;
  constexpr int NBI = (SUBW + 3) / 4;
  constexpr int NLD = 4 + NBI;
  constexpr int NK = 768 / 64;
  __shared__ __attribute__((aligned(16))) char smem_raw[NS * STG];
  LDS_AS char* smem = (LDS_AS char*)smem_raw;
  const unsigned lds0 = (unsigned)(uintptr_t)smem;
  constexpr int KID = (EPI == 0) ? 1 : (EPI == 1 ? 6 : 5);
  STAMP(KID, 0);
  const int tid = threadIdx.x;
  const int l = tid & 63;
  const int w = uni(tid >> 6);
  const int bid = blockIdx.x;
  const int xcd = bid & 7, idx = bid >> 3;
  int tm, tn, ks = 0;
  if constexpr (EPI == 2) {
    tm = (xcd >> 1) * 4 + (idx & 3);
    tn = (idx >> 2) & 3;
    ks = (xcd & 1) * 2 + (idx >> 4);
  } else {
    tm = (xcd >> 1) * 4 + (idx & 3);
    tn = (xcd & 1) * 8 + (idx >> 2);
  }
  const int m0 = tm * 128, n0 = tn * BN;

  if (w >= 8) {
    const int lw = w - 8;
    const unsigned voffA = (unsigned)((l >> 3) * LDA * 2 + (((l & 7) ^ (l >> 3)) << 4));
    unsigned voffB[NBI];
#pragma unroll
    for (int i = 0; i < NBI; ++i) {
      int sub = SUBW * lw + 4 * i + (l >> 4);
      if (sub >= SUBW * (lw + 1)) sub = SUBW * (lw + 1) - 1;
      int kb = sub / NC, cc = sub % NC;
      int within = l & 15;
      int r = (within >> 1) ^ ((kb & 1) << 2);
      voffB[i] = (unsigned)(((kb * 8 + r) * LDW + cc * 16 + (within & 1) * 8) * 2);
    }
    const char* abase = (const char*)(A + (size_t)(m0 + 32 * lw) * LDA + ks * 768);
    const char* bbase = (const char*)(W + (size_t)ks * 768 * LDW + n0);
    auto issue = [&](int kt, int st) {
      const unsigned sa = lds0 + st * STG;
#pragma unroll
      for (int i = 0; i < 4; ++i)
        glds16(abase + (size_t)i * 8 * LDA * 2 + kt * 128, voffA, sa + (32 * lw + 8 * i) * 128);
      const char* sb = bbase + (size_t)kt * 64 * LDW * 2;
#pragma unroll
      for (int i = 0; i < NBI; ++i) {
        unsigned dst = sa + ASZ + (SUBW * lw + 4 * i) * 256;
        if (4 * i + 4 <= SUBW) {
          glds16(sb, voffB[i], dst);
        } else {
          if (l < (SUBW - 4 * i) * 16) glds16(sb, voffB[i], dst);
        }
      }
    };
#pragma unroll
    for (int s = 0; s < NS - 1; ++s) issue(s, s);
    int stn = NS - 1;
#pragma unroll 1
    for (int kt = 0; kt < NK; ++kt) {
      if (kt < NK - (NS - 2)) wait_vm<(NS - 2) * NLD>();
      else wait_vm<0>();
      bar();
      if (kt + NS - 1 < NK) issue(kt + NS - 1, stn);
      stn = (stn + 1 == NS) ? 0 : stn + 1;
    }
    bar();
    bar();
    return;
  }

  const int wm = w & 3, kh = w >> 2;
  const int g = l >> 4;
  const unsigned aoff = (unsigned)(((32 * wm + (l & 15)) * 128 + ((g ^ (l & 7)) << 4)) ^ (kh * 64));
  const int q = (l & 15) >> 2, p = l & 3;
  const unsigned boff_t0 = (unsigned)(ASZ + (4 * kh + g) * NC * 256 + ((4 * (g & 1) + q) * 32) + p * 8);
  const unsigned boff_t1 = (unsigned)(ASZ + (4 * kh + g) * NC * 256 + ((4 * (1 - (g & 1)) + q) * 32) + p * 8);

  f32x4 acc[2][NC];
#pragma unroll
  for (int i = 0; i < 2; ++i)
#pragma unroll
    for (int j = 0; j < NC; ++j) acc[i][j] = (f32x4){0.f, 0.f, 0.f, 0.f};
  STAMP(KID, 1);

  int st = 0;
#pragma unroll 1
  for (int kt = 0; kt < NK; ++kt) {
    wait_lgkm0();
    bar();
    const LDS_AS char* sp = smem + st * STG;
    bf16x8 xb[2];
#pragma unroll
    for (int mf = 0; mf < 2; ++mf) xb[mf] = lds_read16(sp + aoff + mf * 2048);
#pragma unroll
    for (int cc = 0; cc < NC; ++cc) {
      bf16x4 w0 = lds_tr(sp + boff_t0 + cc * 256);
      bf16x4 w1 = lds_tr(sp + boff_t1 + cc * 256);
      bf16x8 wf = cat4(w0, w1);
      acc[0][cc] = mfma16(wf, xb[0], acc[0][cc]);
      acc[1][cc] = mfma16(wf, xb[1], acc[1][cc]);
    }
    st = (st + 1 == NS) ? 0 : st + 1;
  }
  STAMP(KID, 14);

  wait_lgkm0();
  bar();
  {
    LDS_AS char* rp = smem + (wm * 2 + kh) * NC * 1024 + l * 16;
    if (kh) {
#pragma unroll
      for (int cc = 0; cc < NC; ++cc) {
        *(LDS_AS f32x4*)(rp + cc * 1024) = acc[0][cc];
        acc[0][cc] = acc[1][cc];
      }
    } else {
#pragma unroll
      for (int cc = 0; cc < NC; ++cc) *(LDS_AS f32x4*)(rp + cc * 1024) = acc[1][cc];
    }
  }
  wait_lgkm0();
  bar();
  const int row = m0 + 32 * wm + 16 * kh + (l & 15);
  const LDS_AS char* rp = smem + (wm * 2 + (1 - kh)) * NC * 1024 + l * 16;
  if constexpr (EPI == 2) {
    float* op = (float*)outv + ((size_t)ks * 2048 + row) * 768 + n0 + 4 * g;
#pragma unroll
    for (int cc = 0; cc < NC; ++cc) *(f32x4*)(op + 16 * cc) = acc[0][cc] + lds_read16f(rp + cc * 1024);
  } else {
    u16* out = (u16*)outv;
    unsigned pk[NC][2];
#pragma unroll
    for (int cc = 0; cc < NC; ++cc) {
      f32x4 v = acc[0][cc] + lds_read16f(rp + cc * 1024);
      if constexpr (EPI == 1) {
        const float c1 = -2.302208198144325f;
        const float c2 = -0.1029432397500235f;
        unsigned y01 = pack2(v[0], v[1]), y23 = pack2(v[2], v[3]);
        float y[4] = {lo_f(y01), hi_f(y01), lo_f(y23), hi_f(y23)};
#pragma unroll
        for (int j = 0; j < 4; ++j) {
          float e = __builtin_amdgcn_exp2f(y[j] * (c1 + c2 * y[j] * y[j]));
          v[j] = y[j] * __builtin_amdgcn_rcpf(1.f + e);
        }
      }
      pk[cc][0] = pack2(v[0], v[1]);
      pk[cc][1] = pack2(v[2], v[3]);
    }
    auto dst_ptr = [&](int n) -> u16* {
      if constexpr (EPI == 1) {
        return out + (size_t)row * 3072 + n;
      } else {
        if (n < 1536) return out + (size_t)row * 1536 + n;
        int nv = n - 1536;
        int hd = nv / 96;
        int dd = nv - hd * 96;
        return out2 + ((size_t)(((row >> 10) * 8 + hd) * 1024 + (row & 1023))) * 96 + dd;
      }
    };
#pragma unroll
    for (int a = 0; a < NC / 2; ++a) {
      u32x4 o = pair_pack(pk[2 * a][0], pk[2 * a][1], pk[2 * a + 1][0], pk[2 * a + 1][1]);
      *(u32x4*)dst_ptr(n0 + 32 * a + (g & 1) * 16 + (g & 2) * 4) = o;
    }
    if constexpr (NC & 1) {
      u32x2 o;
      o[0] = pk[NC - 1][0];
      o[1] = pk[NC - 1][1];
      *(u32x2*)dst_ptr(n0 + 16 * (NC - 1) + 4 * g) = o;
    }
  }
  STAMP(KID, 15);
}

__global__ __launch_bounds__(256) void k_final(const float* __restrict__ part, const float* __restrict__ modp,
                                               const u16* __restrict__ x1, u16* __restrict__ out) {
  const int t = blockIdx.x * 256 + threadIdx.x;
  const int row = t / 96, c8 = (t - row * 96) * 8;
  const int b = row >> 10;
  const size_t e = (size_t)row * 768 + c8;
  f32x4 p[4][2];
#pragma unroll
  for (int k = 0; k < 4; ++k) {
    p[k][0] = *(const f32x4*)(part + (size_t)k * 2048 * 768 + e);
    p[k][1] = *(const f32x4*)(part + (size_t)k * 2048 * 768 + e + 4);
  }
  u32x4 xr = *(const u32x4*)(x1 + e);
  const float* mp0 = modp + (0 * 2 + b) * 4608 + 5 * 768 + c8;
  const float* mp1 = modp + (1 * 2 + b) * 4608 + 5 * 768 + c8;
  f32x4 ga[2] = {*(const f32x4*)mp0, *(const f32x4*)(mp0 + 4)};
  f32x4 gb[2] = {*(const f32x4*)mp1, *(const f32x4*)(mp1 + 4)};
  float v[8];
#pragma unroll
  for (int hh = 0; hh < 2; ++hh)
#pragma unroll
    for (int j = 0; j < 4; ++j) {
      float m = (p[0][hh][j] + p[1][hh][j]) + (p[2][hh][j] + p[3][hh][j]);
      float gate = rbf(ga[hh][j] + gb[hh][j]);
      unsigned xw = xr[hh * 2 + (j >> 1)];
      float xv = (j & 1) ? hi_f(xw) : lo_f(xw);
      v[hh * 4 + j] = xv + rbf(gate * rbf(m));
    }
  u32x4 o;
#pragma unroll
  for (int j = 0; j < 4; ++j) o[j] = pack2(v[2 * j], v[2 * j + 1]);
  *(u32x4*)(out + e) = o;
}

__global__ __launch_bounds__(768, 3) void k_scores(const u16* __restrict__ qk, u16* __restrict__ P,
                                                   f32x2* __restrict__ ml) {
  constexpr int NS = 4;
  constexpr int ASZ = 128 * 128;
  constexpr int STG = 2 * ASZ;
  constexpr int NK = 384 / 64;
  constexpr int NLD = 8;
  constexpr int LDQ = 1536;
  __shared__ __attribute__((aligned(16))) char smem_raw[NS * STG];
  LDS_AS char* smem = (LDS_AS char*)smem_raw;
  const unsigned lds0 = (unsigned)(uintptr_t)smem;
  STAMP(2, 0);
  const int tid = threadIdx.x, l = tid & 63;
  const int w = uni(tid >> 6);
  const int bid = blockIdx.x;
  const int xcd = bid & 7, idx = bid >> 3;
  const int bh = xcd >> 1;
  const int tq = (xcd & 1) * 4 + (idx & 3);
  const int tk = idx >> 2;
  const int b = bh >> 1, h = bh & 1;

  if (w >= 8) {
    const int lw = w - 8;
    const unsigned voff = (unsigned)((l >> 3) * LDQ * 2 + (((l & 7) ^ (l >> 3)) << 4));
    const char* qbase = (const char*)(qk + (size_t)(b * 1024 + tq * 128 + 32 * lw) * LDQ + h * 384);
    const char* kbase = (const char*)(qk + (size_t)(b * 1024 + tk * 128 + 32 * lw) * LDQ + 768 + h * 384);
    auto issue = [&](int kt, int st) {
      const unsigned sa = lds0 + st * STG;
#pragma unroll
      for (int i = 0; i < 4; ++i)
        glds16(qbase + (size_t)i * 8 * LDQ * 2 + kt * 128, voff, sa + (32 * lw + 8 * i) * 128);
#pragma unroll
      for (int i = 0; i < 4; ++i)
        glds16(kbase + (size_t)i * 8 * LDQ * 2 + kt * 128, voff, sa + ASZ + (32 * lw + 8 * i) * 128);
    };
#pragma unroll
    for (int s = 0; s < NS - 1; ++s) issue(s, s);
    int stn = NS - 1;
#pragma unroll 1
    for (int kt = 0; kt < NK; ++kt) {
      if (kt < NK - (NS - 2)) wait_vm<(NS - 2) * NLD>();
      else wait_vm<0>();
      bar();
      if (kt + NS - 1 < NK) issue(kt + NS - 1, stn);
      stn = (stn + 1 == NS) ? 0 : stn + 1;
    }
    bar();
    bar();
    return;
  }

  const int wm = w & 3, kh = w >> 2;
  const int g = l >> 4;
  const unsigned qoff = (unsigned)(((32 * wm + (l & 15)) * 128 + ((g ^ (l & 7)) << 4)) ^ (kh * 64));
  const unsigned koff = (unsigned)(ASZ + (((l & 15) * 128 + ((g ^ (l & 7)) << 4)) ^ (kh * 64)));
  f32x4 acc[2][8];
#pragma unroll
  for (int i = 0; i < 2; ++i)
#pragma unroll
    for (int j = 0; j < 8; ++j) acc[i][j] = (f32x4){0.f, 0.f, 0.f, 0.f};

  int st = 0;
#pragma unroll 1
  for (int kt = 0; kt < NK; ++kt) {
    wait_lgkm0();
    bar();
    const LDS_AS char* sp = smem + st * STG;
    bf16x8 qf[2];
#pragma unroll
    for (int mf = 0; mf < 2; ++mf) qf[mf] = lds_read16(sp + qoff + mf * 2048);
#pragma unroll
    for (int kf = 0; kf < 8; ++kf) {
      bf16x8 kfr = lds_read16(sp + koff + kf * 2048);
      acc[0][kf] = mfma16(kfr, qf[0], acc[0][kf]);
      acc[1][kf] = mfma16(kfr, qf[1], acc[1][kf]);
    }
    st = (st + 1 == NS) ? 0 : st + 1;
  }
  STAMP(2, 14);
  wait_lgkm0();
  bar();
  {
    LDS_AS char* rp = smem + (wm * 2 + kh) * 8 * 1024 + l * 16;
    if (kh) {
#pragma unroll
      for (int kf = 0; kf < 8; ++kf) {
        *(LDS_AS f32x4*)(rp + kf * 1024) = acc[0][kf];
        acc[0][kf] = acc[1][kf];
      }
    } else {
#pragma unroll
      for (int kf = 0; kf < 8; ++kf) *(LDS_AS f32x4*)(rp + kf * 1024) = acc[1][kf];
    }
  }
  wait_lgkm0();
  bar();
  f32x4 s[8];
  {
    const LDS_AS char* rp = smem + (wm * 2 + (1 - kh)) * 8 * 1024 + l * 16;
#pragma unroll
    for (int kf = 0; kf < 8; ++kf) {
      f32x4 v = acc[0][kf] + lds_read16f(rp + kf * 1024);
      unsigned a01 = pack2(v[0], v[1]), a23 = pack2(v[2], v[3]);
      a01 = pack2(lo_f(a01) * 0.051031036307982884f, hi_f(a01) * 0.051031036307982884f);
      a23 = pack2(lo_f(a23) * 0.051031036307982884f, hi_f(a23) * 0.051031036307982884f);
      s[kf] = (f32x4){lo_f(a01), hi_f(a01), lo_f(a23), hi_f(a23)};
    }
  }
  const float l2e = 1.4426950408889634f;
  float mx = -INFINITY;
#pragma unroll
  for (int kf = 0; kf < 8; ++kf)
#pragma unroll
    for (int j = 0; j < 4; ++j) mx = fmaxf(mx, s[kf][j]);
  mx = fmaxf(mx, __shfl_xor(mx, 16));
  mx = fmaxf(mx, __shfl_xor(mx, 32));
  const float mbl = mx * l2e;
  float sum = 0.f;
  unsigned pk[8][2];
#pragma unroll
  for (int kf = 0; kf < 8; ++kf) {
    float e[4];
#pragma unroll
    for (int j = 0; j < 4; ++j) {
      e[j] = __builtin_amdgcn_exp2f(s[kf][j] * l2e - mbl);
      sum += e[j];
    }
    pk[kf][0] = pack2(e[0], e[1]);
    pk[kf][1] = pack2(e[2], e[3]);
  }
  sum += __shfl_xor(sum, 16);
  sum += __shfl_xor(sum, 32);
  const int qrow = tq * 128 + 32 * wm + 16 * kh + (l & 15);
  u16* op = P + ((size_t)bh * 1024 + qrow) * 1024 + tk * 128;
#pragma unroll
  for (int a = 0; a < 4; ++a) {
    u32x4 o = pair_pack(pk[2 * a][0], pk[2 * a][1], pk[2 * a + 1][0], pk[2 * a + 1][1]);
    *(u32x4*)(op + 32 * a + (g & 1) * 16 + (g & 2) * 4) = o;
  }
  if (g == 0) {
    f32x2 s2;
    s2[0] = mbl;
    s2[1] = sum;
    ml[((size_t)bh * 1024 + qrow) * 8 + tk] = s2;
  }
  STAMP(2, 15);
}

template <int MODE>
__global__ __launch_bounds__(512, 2) void k_gemm_small(const u16* __restrict__ Ain, const u16* __restrict__ Bin,
                                                       const float* __restrict__ modp,
                                                       const u16* __restrict__ resid,
                                                       const f32x2* __restrict__ ml, f32x2* __restrict__ stats,
                                                       u16* __restrict__ out) {
  constexpr int KTOT = (MODE == 0) ? 1024 : (MODE == 1 ? 768 : 3072);
  constexpr int KH = KTOT / 2;
  constexpr int NK = KH / 64;
  constexpr int LDA = KTOT;
  constexpr int LDB = (MODE == 0) ? 96 : 768;
  constexpr int NS = 4;
  constexpr int STG = 40960;
  constexpr int NLD = 10;
  __shared__ __attribute__((aligned(16))) char smem_raw[NS * STG];
  LDS_AS char* smem = (LDS_AS char*)smem_raw;
  const unsigned lds0 = (unsigned)(uintptr_t)smem;
  STAMP(3 + MODE, 0);
  const int tid = threadIdx.x, l = tid & 63;
  const int w = uni(tid >> 6);
  const int bid = blockIdx.x;
  const int xcd = bid & 7, idx = bid >> 3;
  const int g = l >> 4;

  int m0, n0, bh = 0;
  const u16* Ap;
  const u16* Bp;
  if constexpr (MODE == 0) {
    bh = xcd >> 1;
    const int tq = (xcd & 1) * 8 + (idx & 7);
    const int td = idx >> 3;
    m0 = tq * 64;
    n0 = td * 96;
    Ap = Ain + ((size_t)bh * 1024 + m0) * 1024;
    Bp = Bin + ((size_t)(bh * 4 + td) * 1024) * 96;
  } else {
    const int tm = xcd * 4 + (idx & 3);
    const int tn = idx >> 2;
    m0 = tm * 64;
    n0 = tn * 96;
    Ap = Ain + (size_t)m0 * LDA;
    Bp = Bin + n0;
  }

  if (w >= 4) {
    const int lw = w - 4;
    const int kq = lw & 1, part = lw >> 1;
    const unsigned voffA = (unsigned)((l >> 3) * LDA * 2 + (((l & 7) ^ (l >> 3)) << 4));
    unsigned voffB[6];
#pragma unroll
    for (int i = 0; i < 6; ++i) {
      int sub = 24 * part + 4 * i + (l >> 4);
      int kb = sub / 6, cc = sub % 6;
      int within = l & 15;
      int r = (within >> 1) ^ ((kb & 1) << 2);
      voffB[i] = (unsigned)(((kb * 8 + r) * LDB + cc * 16 + (within & 1) * 8) * 2);
    }
    const char* abase = (const char*)(Ap + (size_t)(32 * part) * LDA + kq * KH);
    const char* bbase = (const char*)(Bp + (size_t)(kq * KH) * LDB);
    auto issue = [&](int kt, int st) {
      const unsigned sa = lds0 + st * STG;
#pragma unroll
      for (int i = 0; i < 4; ++i)
        glds16(abase + (size_t)i * 8 * LDA * 2 + kt * 128, voffA, sa + kq * 8192 + (32 * part + 8 * i) * 128);
      const char* sb = bbase + (size_t)kt * 64 * LDB * 2;
#pragma unroll
      for (int i = 0; i < 6; ++i) glds16(sb, voffB[i], sa + 16384 + kq * 12288 + (24 * part + 4 * i) * 256);
    };
#pragma unroll
    for (int s = 0; s < NS - 1; ++s) issue(s, s);
    int stn = NS - 1;
#pragma unroll 1
    for (int kt = 0; kt < NK; ++kt) {
      if (kt < NK - (NS - 2)) wait_vm<(NS - 2) * NLD>();
      else wait_vm<0>();
      bar();
      if (kt + NS - 1 < NK) issue(kt + NS - 1, stn);
      stn = (stn + 1 == NS) ? 0 : stn + 1;
    }
    bar();
    bar();
    return;
  }

  const int kq = w & 1, nh = w >> 1;
  float cf[4][4];
  if constexpr (MODE == 0) {
#pragma unroll
    for (int mf = 0; mf < 4; ++mf) {
      const f32x4* sp = (const f32x4*)(ml + ((size_t)bh * 1024 + m0 + 16 * mf + (l & 15)) * 8);
      f32x4 sv[4];
#pragma unroll
      for (int j = 0; j < 4; ++j) sv[j] = sp[j];
      float mx = fmaxf(sv[0][0], sv[0][2]);
#pragma unroll
      for (int j = 1; j < 4; ++j) mx = fmaxf(mx, fmaxf(sv[j][0], sv[j][2]));
      float e[8];
      float lsum = 0.f;
#pragma unroll
      for (int j = 0; j < 4; ++j) {
        e[2 * j] = __builtin_amdgcn_exp2f(sv[j][0] - mx);
        e[2 * j + 1] = __builtin_amdgcn_exp2f(sv[j][2] - mx);
        lsum += e[2 * j] * sv[j][1] + e[2 * j + 1] * sv[j][3];
      }
      float inv = 1.f / lsum;
      if (kq) {
#pragma unroll
        for (int j = 0; j < 4; ++j) cf[mf][j] = e[4 + j] * inv;
      } else {
#pragma unroll
        for (int j = 0; j < 4; ++j) cf[mf][j] = e[j] * inv;
      }
    }
  }
  f32x4 gq[2][3][2];
  u32x2 xr[2][3];
  if constexpr (MODE != 0) {
    const int goff = (MODE == 1) ? 2 * 768 : 5 * 768;
    const int b = m0 >> 10;
    const float* mp0 = modp + (0 * 2 + b) * 4608 + goff + n0 + 48 * nh + 4 * g;
    const float* mp1 = modp + (1 * 2 + b) * 4608 + goff + n0 + 48 * nh + 4 * g;
#pragma unroll
    for (int cc = 0; cc < 3; ++cc) {
      gq[0][cc][0] = *(const f32x4*)(mp0 + 16 * cc);
      gq[0][cc][1] = *(const f32x4*)(mp1 + 16 * cc);
    }
#pragma unroll
    for (int mf = 0; mf < 2; ++mf) {
      const u16* rp = resid + (size_t)(m0 + 32 * kq + 16 * mf + (l & 15)) * 768 + n0 + 48 * nh + 4 * g;
#pragma unroll
      for (int cc = 0; cc < 3; ++cc) xr[mf][cc] = *(const u32x2*)(rp + 16 * cc);
    }
  }

  const unsigned aoff0 = (unsigned)(kq * 8192 + (l & 15) * 128 + ((g ^ (l & 7)) << 4));
  const int q = (l & 15) >> 2, p = l & 3;
  const unsigned boff_t0 = (unsigned)(16384 + kq * 12288 + (g * 6 + 3 * nh) * 256 + ((4 * (g & 1) + q) * 32) + p * 8);
  const unsigned boff_t1 =
      (unsigned)(16384 + kq * 12288 + (g * 6 + 3 * nh) * 256 + ((4 * (1 - (g & 1)) + q) * 32) + p * 8);

  f32x4 acc[4][3];
  f32x4 tot[4][3];
#pragma unroll
  for (int i = 0; i < 4; ++i)
#pragma unroll
    for (int j = 0; j < 3; ++j) {
      acc[i][j] = (f32x4){0.f, 0.f, 0.f, 0.f};
      tot[i][j] = (f32x4){0.f, 0.f, 0.f, 0.f};
    }
  STAMP(3 + MODE, 1);

  int st = 0;
#pragma unroll 1
  for (int kt = 0; kt < NK; ++kt) {
    wait_lgkm0();
    bar();
    const LDS_AS char* sp = smem + st * STG;
#pragma unroll
    for (int ks = 0; ks < 2; ++ks) {
      bf16x8 af[4];
#pragma unroll
      for (int mf = 0; mf < 4; ++mf) af[mf] = lds_read16(sp + ((aoff0 ^ (ks * 64)) + mf * 2048));
#pragma unroll
      for (int cc = 0; cc < 3; ++cc) {
        bf16x4 w0 = lds_tr(sp + boff_t0 + ks * 6144 + cc * 256);
        bf16x4 w1 = lds_tr(sp + boff_t1 + ks * 6144 + cc * 256);
        bf16x8 wf = cat4(w0, w1);
#pragma unroll
        for (int mf = 0; mf < 4; ++mf) acc[mf][cc] = mfma16(wf, af[mf], acc[mf][cc]);
      }
    }
    if constexpr (MODE == 0) {
      if (kt & 1) {
#pragma unroll
        for (int mf = 0; mf < 4; ++mf) {
          const float c = cf[mf][0];
          cf[mf][0] = cf[mf][1];
          cf[mf][1] = cf[mf][2];
          cf[mf][2] = cf[mf][3];
#pragma unroll
          for (int cc = 0; cc < 3; ++cc) {
            tot[mf][cc] += acc[mf][cc] * c;
            acc[mf][cc] = (f32x4){0.f, 0.f, 0.f, 0.f};
          }
        }
      }
    }
    st = (st + 1 == NS) ? 0 : st + 1;
  }
  if constexpr (MODE == 0) {
#pragma unroll
    for (int mf = 0; mf < 4; ++mf)
#pragma unroll
      for (int cc = 0; cc < 3; ++cc) acc[mf][cc] = tot[mf][cc];
  }
  STAMP(3 + MODE, 12);
  wait_lgkm0();
  bar();
  {
    LDS_AS char* rp = smem + (nh * 2 + kq) * 6144 + l * 16;
    if (kq) {
#pragma unroll
      for (int mf = 0; mf < 2; ++mf)
#pragma unroll
        for (int cc = 0; cc < 3; ++cc) {
          *(LDS_AS f32x4*)(rp + (mf * 3 + cc) * 1024) = acc[mf][cc];
          acc[mf][cc] = acc[2 + mf][cc];
        }
    } else {
#pragma unroll
      for (int mf = 0; mf < 2; ++mf)
#pragma unroll
        for (int cc = 0; cc < 3; ++cc) *(LDS_AS f32x4*)(rp + (mf * 3 + cc) * 1024) = acc[2 + mf][cc];
    }
  }
  wait_lgkm0();
  bar();
  f32x4 r[2][3];
  {
    const LDS_AS char* rp = smem + (nh * 2 + (1 - kq)) * 6144 + l * 16;
#pragma unroll
    for (int mf = 0; mf < 2; ++mf)
#pragma unroll
      for (int cc = 0; cc < 3; ++cc)
        r[mf][cc] = acc[mf][cc] + lds_read16f(rp + (mf * 3 + cc) * 1024);
  }
  STAMP(3 + MODE, 13);
#pragma unroll
  for (int mf = 0; mf < 2; ++mf) {
    const int mrow = m0 + 32 * kq + 16 * mf + (l & 15);
    float v[3][4];
    u16* op;
    if constexpr (MODE == 0) {
      op = out + (size_t)((bh >> 1) * 1024 + mrow) * 768 + (bh & 1) * 384 + n0 + 48 * nh;
#pragma unroll
      for (int cc = 0; cc < 3; ++cc)
#pragma unroll
        for (int j = 0; j < 4; ++j) v[cc][j] = r[mf][cc][j];
    } else {
      op = out + (size_t)mrow * 768 + n0 + 48 * nh;
#pragma unroll
      for (int cc = 0; cc < 3; ++cc) {
        float xv[4] = {lo_f(xr[mf][cc][0]), hi_f(xr[mf][cc][0]), lo_f(xr[mf][cc][1]), hi_f(xr[mf][cc][1])};
#pragma unroll
        for (int j = 0; j < 4; ++j) {
          float gate = rbf(gq[0][cc][0][j] + gq[0][cc][1][j]);
          v[cc][j] = rbf(xv[j] + rbf(gate * rbf(r[mf][cc][j])));
        }
      }
    }
    {
      u32x4 o = pair_pack(pack2(v[0][0], v[0][1]), pack2(v[0][2], v[0][3]), pack2(v[1][0], v[1][1]),
                          pack2(v[1][2], v[1][3]));
      *(u32x4*)(op + (g & 1) * 16 + (g & 2) * 4) = o;
      u32x2 o2;
      o2[0] = pack2(v[2][0], v[2][1]);
      o2[1] = pack2(v[2][2], v[2][3]);
      *(u32x2*)(op + 32 + 4 * g) = o2;
    }
    if constexpr (MODE == 1) {
      float s = 0.f;
#pragma unroll
      for (int cc = 0; cc < 3; ++cc)
#pragma unroll
        for (int j = 0; j < 4; ++j) s += v[cc][j];
      s += __shfl_xor(s, 16);
      s += __shfl_xor(s, 32);
      float mean = s * (1.f / 48.f);
      float m2 = 0.f;
#pragma unroll
      for (int cc = 0; cc < 3; ++cc)
#pragma unroll
        for (int j = 0; j < 4; ++j) {
          float d = v[cc][j] - mean;
          m2 += d * d;
        }
      m2 += __shfl_xor(m2, 16);
      m2 += __shfl_xor(m2, 32);
      if (g == 0) {
        f32x2 s2;
        s2[0] = s;
        s2[1] = m2;
        stats[(size_t)mrow * 16 + (n0 / 48) + nh] = s2;
      }
    }
  }
  STAMP(3 + MODE, 15);
}

extern "C" void kernel_launch(void* const* d_in, const int* in_sizes, int n_in, void* d_out, int out_size,
                              void* d_ws, size_t ws_size, hipStream_t stream) {
  const u16* x = (const u16*)d_in[0];
  const u16* c = (const u16*)d_in[1];
  const u16* wqkv = (const u16*)d_in[2];
  const u16* wo = (const u16*)d_in[3];
  const u16* wmod = (const u16*)d_in[4];
  const u16* wfc1 = (const u16*)d_in[5];
  const u16* wfc2 = (const u16*)d_in[6];
  u16* out = (u16*)d_out;

  char* ws = (char*)d_ws;
  size_t off = 0;
  auto alloc = [&](size_t bytes) {
    char* p = ws + off;
    off += (bytes + 255) & ~(size_t)255;
    return p;
  };
  float* modp = (float*)alloc(2 * 2 * 4608 * 4);
  u16* h = (u16*)alloc((size_t)Mrows * 768 * 2);
  u16* qk = (u16*)alloc((size_t)Mrows * 1536 * 2);
  u16* Vt = (u16*)alloc((size_t)Mrows * 768 * 2);
  u16* P = (u16*)alloc((size_t)4 * 1024 * 1024 * 2);
  f32x2* ml = (f32x2*)alloc((size_t)4 * 1024 * 8 * 8);
  u16* O = (u16*)alloc((size_t)Mrows * 768 * 2);
  u16* x1 = (u16*)alloc((size_t)Mrows * 768 * 2);
  f32x2* st2 = (f32x2*)alloc((size_t)Mrows * 16 * 8);
  u16* h2 = (u16*)alloc((size_t)Mrows * 768 * 2);
  u16* u = (u16*)alloc((size_t)Mrows * 3072 * 2);

  k0_mod<<<144 + 96, 256, 0, stream>>>(c, wmod, modp, wqkv, wo);
  k_lnmod<<<256 + 192, 256, 0, stream>>>(x, modp, 0, h, wfc1, wfc2);
  k_gemm_big<144, 4, 0><<<256, 768, 0, stream>>>(h, wqkv, qk, Vt);
  k_scores<<<256, 768, 0, stream>>>(qk, P, ml);
  k_gemm_small<0><<<256, 512, 0, stream>>>(P, Vt, modp, nullptr, ml, nullptr, O);
  k_gemm_small<1><<<256, 512, 0, stream>>>(O, wo, modp, x, nullptr, st2, x1);
  k_lnmod<<<256, 256, 0, stream>>>(x1, modp, 3 * 768, h2, nullptr, nullptr);
  k_gemm_big<192, 4, 1><<<256, 768, 0, stream>>>(h2, wfc1, u, nullptr);
  k_gemm_small<2><<<256, 512, 0, stream>>>(u, wfc2, modp, x1, nullptr, nullptr, out);
}
```

```cpp
#include <hip/hip_runtime.h>
#include <hip/hip_bf16.h>
#include <stdint.h>
#include <type_traits>

typedef __attribute__((ext_vector_type(8))) __bf16 bf16x8;
typedef __attribute__((ext_vector_type(4))) __bf16 bf16x4;
typedef __attribute__((ext_vector_type(2))) __bf16 bf16x2;
typedef __attribute__((ext_vector_type(4))) float f32x4;
typedef __attribute__((ext_vector_type(2))) float f32x2;
typedef __attribute__((ext_vector_type(4))) unsigned u32x4;
typedef __attribute__((ext_vector_type(2))) unsigned u32x2;
typedef unsigned short u16;
#define LDS_AS __attribute__((address_space(3)))

#define DEVI __device__ __forceinline__
#define STAMP(K, I)

static constexpr int Bsz = 2, Nseq = 1024, Dm = 768, Ff = 3072, Dh = 384;
static constexpr int Mrows = Bsz * Nseq;

template <int I, int N, class F>
DEVI void static_for(F&& f) {
  if constexpr (I < N) {
    f(std::integral_constant<int, I>{});
    static_for<I + 1, N>(f);
  }
}

DEVI float lo_f(unsigned u) { return __uint_as_float(u << 16); }
DEVI float hi_f(unsigned u) { return __uint_as_float(u & 0xffff0000u); }
DEVI float bf2f(u16 h) { return __uint_as_float(((unsigned)h) << 16); }
DEVI unsigned pack2(float a, float b) {
  bf16x2 v;
  v[0] = (__bf16)a;
  v[1] = (__bf16)b;
  return __builtin_bit_cast(unsigned, v);
}
DEVI float rbf(float a) { return lo_f(pack2(a, 0.f) ); }

template <int N>
DEVI void wait_vm() { asm volatile("s_waitcnt vmcnt(%0)" ::"i"(N) : "memory"); }
DEVI void wait_lgkm0() { asm volatile("s_waitcnt lgkmcnt(0)" ::: "memory"); }
DEVI void bar() { __builtin_amdgcn_s_barrier(); }

DEVI void glds16(const void* sbase, unsigned voff, unsigned lds_dst) {
  unsigned keep;
  asm volatile(
      "s_mov_b32 %0, m0\n\t"
      "s_mov_b32 m0, %3\n\t"
      "s_nop 0\n\t"
      "global_load_lds_dwordx4 %1, %2\n\t"
      "s_mov_b32 m0, %0"
      : "=&s"(keep)
      : "v"(voff), "s"(sbase), "s"(lds_dst)
      : "memory");
}

DEVI void gld16(u32x4& d, const void* p) { asm volatile("global_load_dwordx4 %0, %1, off" : "=v"(d) : "v"(p) : "memory"); }
DEVI void gld8(u32x2& d, const void* p) { asm volatile("global_load_dwordx2 %0, %1, off" : "=v"(d) : "v"(p) : "memory"); }
template <class T>
DEVI void pin(T& v) { asm volatile("" : "+v"(v)); }
DEVI f32x4 asf4(u32x4 v) { return __builtin_bit_cast(f32x4, v); }

DEVI void st16(void* p, u32x4 v) { asm volatile("global_store_dwordx4 %0, %1, off sc1\n\ts_nop 1" ::"v"(p), "v"(v) : "memory"); }
DEVI void st8(void* p, u32x2 v) { asm volatile("global_store_dwordx2 %0, %1, off sc1\n\ts_nop 1" ::"v"(p), "v"(v) : "memory"); }

DEVI bf16x8 lds_read16(const LDS_AS char* p) { return *(const LDS_AS bf16x8*)p; }
DEVI u32x4 lds_read16u(const LDS_AS char* p) { return *(const LDS_AS u32x4*)p; }
DEVI f32x4 lds_read16f(const LDS_AS char* p) { return *(const LDS_AS f32x4*)p; }
DEVI bf16x4 lds_tr(const LDS_AS char* p) {
  return __builtin_amdgcn_ds_read_tr16_b64_v4bf16((LDS_AS bf16x4*)p);
}
DEVI bf16x8 cat4(bf16x4 a, bf16x4 b) {
  return __builtin_shufflevector(a, b, 0, 1, 2, 3, 4, 5, 6, 7);
}
DEVI f32x4 mfma16(bf16x8 a, bf16x8 b, f32x4 c) {
  return __builtin_amdgcn_mfma_f32_16x16x32_bf16(a, b, c, 0, 0, 0);
}
DEVI int uni(int v) { return __builtin_amdgcn_readfirstlane(v); }

DEVI u32x4 pair_pack(unsigned ax, unsigned ay, unsigned bx, unsigned by) {
  auto r0 = __builtin_amdgcn_permlane16_swap(ax, bx, false, false);
  auto r1 = __builtin_amdgcn_permlane16_swap(ay, by, false, false);
  u32x4 o;
  o[0] = r0[0];
  o[1] = r1[0];
  o[2] = r0[1];
  o[3] = r1[1];
  return o;
}

__global__ __launch_bounds__(256) void k0_mod(const u16* __restrict__ c, const u16* __restrict__ wmod,
                                              float* __restrict__ modp, const u16* __restrict__ w0,
                                              const u16* __restrict__ w1) {
  __shared__ float sc[2][384];
  __shared__ float red[32][2][64];
  const int tid = threadIdx.x;
  if (blockIdx.x >= 144) {
    const int t = (blockIdx.x - 144) * 256 + tid;
    u32x4 a = {0u, 0u, 0u, 0u};
    u32x4 v[12];
#pragma unroll
    for (int i = 0; i < 12; ++i) {
      int ci = t + i * 24576;
      const u16* p = (ci < 221184) ? (w0 + (size_t)ci * 8) : (w1 + (size_t)(ci - 221184) * 8);
      v[i] = *(const u32x4*)p;
    }
#pragma unroll
    for (int i = 0; i < 12; ++i) a ^= v[i];
    asm volatile("" ::"v"(a));
    return;
  }
  const int cb = blockIdx.x % 72, kp = blockIdx.x / 72;
  const int tc = tid & 7, tr = tid >> 3;
  u32x4 w[12];
  const u16* wp = wmod + (size_t)(kp * 384 + tr) * 4608 + cb * 64 + tc * 8;
#pragma unroll
  for (int i = 0; i < 12; ++i) w[i] = *(const u32x4*)(wp + (size_t)i * 32 * 4608);
  for (int i = tid; i < 768; i += 256) {
    int b = i / 384, k = i % 384;
    float v = bf2f(c[b * 768 + kp * 384 + k]);
    float s = v / (1.f + __expf(-v));
    sc[b][k] = rbf(s);
  }
  __syncthreads();
  float acc[2][8];
#pragma unroll
  for (int b = 0; b < 2; ++b)
#pragma unroll
    for (int j = 0; j < 8; ++j) acc[b][j] = 0.f;
#pragma unroll
  for (int i = 0; i < 12; ++i) {
    float s0 = sc[0][tr + 32 * i], s1 = sc[1][tr + 32 * i];
#pragma unroll
    for (int j = 0; j < 4; ++j) {
      float a = lo_f(w[i][j]), bb = hi_f(w[i][j]);
      acc[0][2 * j] += s0 * a;
      acc[0][2 * j + 1] += s0 * bb;
      acc[1][2 * j] += s1 * a;
      acc[1][2 * j + 1] += s1 * bb;
    }
  }
#pragma unroll
  for (int b = 0; b < 2; ++b)
#pragma unroll
    for (int j = 0; j < 8; ++j) red[tr][b][tc * 8 + j] = acc[b][j];
  __syncthreads();
  if (tid < 128) {
    int b = tid >> 6, col = tid & 63;
    float s = 0.f;
#pragma unroll
    for (int r = 0; r < 32; ++r) s += red[r][b][col];
    modp[(kp * 2 + b) * 4608 + cb * 64 + col] = s;
  }
}

__global__ __launch_bounds__(256) void k_lnmod(const u16* __restrict__ x, const float* __restrict__ modp, int offS,
                                               u16* __restrict__ h, const u16* __restrict__ w0,
                                               const u16* __restrict__ w1) {
  const int tid = threadIdx.x;
  if (blockIdx.x >= 256) {
    const int t = (blockIdx.x - 256) * 256 + tid;
    u32x4 a = {0u, 0u, 0u, 0u};
    u32x4 v[12];
#pragma unroll
    for (int i = 0; i < 12; ++i) {
      int ci = t + i * 49152;
      const u16* p = (ci < 294912) ? (w0 + (size_t)ci * 8) : (w1 + (size_t)(ci - 294912) * 8);
      v[i] = *(const u32x4*)p;
    }
#pragma unroll
    for (int i = 0; i < 12; ++i) a ^= v[i];
    asm volatile("" ::"v"(a));
    return;
  }
  const int w = tid >> 6, l = tid & 63;
  const int row0 = blockIdx.x * 8 + w * 2;
  const int b = row0 >> 10;
  u32x2 v[2][3];
#pragma unroll
  for (int r = 0; r < 2; ++r) {
    const u16* xp = x + (size_t)(row0 + r) * 768 + l * 4;
#pragma unroll
    for (int i = 0; i < 3; ++i) v[r][i] = *(const u32x2*)(xp + i * 256);
  }
  f32x4 gg[3], tt[3];
#pragma unroll
  for (int i = 0; i < 3; ++i) {
    const float* m0p = modp + (0 * 2 + b) * 4608 + offS + l * 4 + i * 256;
    const float* m1p = modp + (1 * 2 + b) * 4608 + offS + l * 4 + i * 256;
    f32x4 s0 = *(const f32x4*)m0p, s1 = *(const f32x4*)m1p;
    f32x4 c0 = *(const f32x4*)(m0p + 768), c1 = *(const f32x4*)(m1p + 768);
#pragma unroll
    for (int j = 0; j < 4; ++j) {
      tt[i][j] = rbf(s0[j] + s1[j]);
      gg[i][j] = rbf(1.f + rbf(c0[j] + c1[j]));
    }
  }
#pragma unroll
  for (int r = 0; r < 2; ++r) {
    float f[12];
#pragma unroll
    for (int i = 0; i < 3; ++i) {
      f[4 * i] = lo_f(v[r][i][0]);
      f[4 * i + 1] = hi_f(v[r][i][0]);
      f[4 * i + 2] = lo_f(v[r][i][1]);
      f[4 * i + 3] = hi_f(v[r][i][1]);
    }
    float s = 0.f;
#pragma unroll
    for (int i = 0; i < 12; ++i) s += f[i];
#pragma unroll
    for (int o = 32; o > 0; o >>= 1) s += __shfl_xor(s, o);
    float mean = s * (1.f / 768.f);
    float q = 0.f;
#pragma unroll
    for (int i = 0; i < 12; ++i) {
      float d = f[i] - mean;
      q += d * d;
    }
#pragma unroll
    for (int o = 32; o > 0; o >>= 1) q += __shfl_xor(q, o);
    const float mb = rbf(mean);
    const float den = rbf(sqrtf(rbf(rbf(q * (1.f / 768.f)) + 1e-5f)));
    u16* op = h + (size_t)(row0 + r) * 768 + l * 4;
#pragma unroll
    for (int i = 0; i < 3; ++i) {
      float y[4];
#pragma unroll
      for (int j = 0; j < 4; ++j) y[j] = rbf(rbf(rbf(f[4 * i + j] - mb) / den) * gg[i][j]) + tt[i][j];
      u32x2 o;
      o[0] = pack2(y[0], y[1]);
      o[1] = pack2(y[2], y[3]);
      st8(op + i * 256, o);
    }
  }
}

template <int BN, int NS, int EPI>
__global__ __launch_bounds__(768, 3) void k_gemm_big(const u16* __restrict__ A, const u16* __restrict__ W,
                                                     void* __restrict__ outv, u16* __restrict__ out2) {
  constexpr int NC = BN / 16;
  constexpr int LDW = (EPI == 0) ? 2304 : (EPI == 1 ? 3072 : 768);
  constexpr int LDA = (EPI == 2) ? 3072 : 768;
  constexpr int ASZ = 128 * 128;
  constexpr int BSZ = 8 * NC * 256;
  constexpr int STG = ASZ + BSZ;
  constexpr int SUBW = 2 * NC;
  constexpr int NBI = (SUBW + 3) / 4;
  constexpr int NLD = 4 + NBI;
  constexpr int NK = 768 / 64;
  __shared__ __attribute__((aligned(16))) char smem_raw[NS * STG];
  LDS_AS char* smem = (LDS_AS char*)smem_raw;
  const unsigned lds0 = (unsigned)(uintptr_t)smem;
  constexpr int KID = (EPI == 0) ? 1 : (EPI == 1 ? 6 : 5);
  STAMP(KID, 0);
  const int tid = threadIdx.x;
  const int l = tid & 63;
  const int w = uni(tid >> 6);
  const int bid = blockIdx.x;
  const int xcd = bid & 7, idx = bid >> 3;
  int tm, tn, ks = 0;
  if constexpr (EPI == 2) {
    tm = (xcd >> 1) * 4 + (idx & 3);
    tn = (idx >> 2) & 3;
    ks = (xcd & 1) * 2 + (idx >> 4);
  } else {
    tm = (xcd >> 1) * 4 + (idx & 3);
    tn = (xcd & 1) * 8 + (idx >> 2);
  }
  const int m0 = tm * 128, n0 = tn * BN;

  if (w >= 8) {
    const int lw = w - 8;
    const unsigned voffA = (unsigned)((l >> 3) * LDA * 2 + (((l & 7) ^ (l >> 3)) << 4));
    unsigned voffB[NBI];
#pragma unroll
    for (int i = 0; i < NBI; ++i) {
      int sub = SUBW * lw + 4 * i + (l >> 4);
      if (sub >= SUBW * (lw + 1)) sub = SUBW * (lw + 1) - 1;
      int kb = sub / NC, cc = sub % NC;
      int within = l & 15;
      int r = (within >> 1) ^ ((kb & 1) << 2);
      voffB[i] = (unsigned)(((kb * 8 + r) * LDW + cc * 16 + (within & 1) * 8) * 2);
    }
    const char* abase = (const char*)(A + (size_t)(m0 + 32 * lw) * LDA + ks * 768);
    const char* bbase = (const char*)(W + (size_t)ks * 768 * LDW + n0);
    auto issue = [&](int kt, int st) {
      const unsigned sa = lds0 + st * STG;
#pragma unroll
      for (int i = 0; i < 4; ++i)
        glds16(abase + (size_t)i * 8 * LDA * 2 + kt * 128, voffA, sa + (32 * lw + 8 * i) * 128);
      const char* sb = bbase + (size_t)kt * 64 * LDW * 2;
#pragma unroll
      for (int i = 0; i < NBI; ++i) {
        unsigned dst = sa + ASZ + (SUBW * lw + 4 * i) * 256;
        if (4 * i + 4 <= SUBW) {
          glds16(sb, voffB[i], dst);
        } else {
          if (l < (SUBW - 4 * i) * 16) glds16(sb, voffB[i], dst);
        }
      }
    };
#pragma unroll
    for (int s = 0; s < NS - 1; ++s) issue(s, s);
    int stn = NS - 1;
#pragma unroll 1
    for (int kt = 0; kt < NK; ++kt) {
      if (kt < NK - (NS - 2)) wait_vm<(NS - 2) * NLD>();
      else wait_vm<0>();
      bar();
      if (kt + NS - 1 < NK) issue(kt + NS - 1, stn);
      stn = (stn + 1 == NS) ? 0 : stn + 1;
    }
    bar();
    bar();
    return;
  }

  const int wm = w & 3, kh = w >> 2;
  const int g = l >> 4;
  const unsigned aoff = (unsigned)(((32 * wm + (l & 15)) * 128 + ((g ^ (l & 7)) << 4)) ^ (kh * 64));
  const int q = (l & 15) >> 2, p = l & 3;
  const unsigned boff_t0 = (unsigned)(ASZ + (4 * kh + g) * NC * 256 + ((4 * (g & 1) + q) * 32) + p * 8);
  const unsigned boff_t1 = (unsigned)(ASZ + (4 * kh + g) * NC * 256 + ((4 * (1 - (g & 1)) + q) * 32) + p * 8);

  f32x4 acc[2][NC];
#pragma unroll
  for (int i = 0; i < 2; ++i)
#pragma unroll
    for (int j = 0; j < NC; ++j) acc[i][j] = (f32x4){0.f, 0.f, 0.f, 0.f};
  STAMP(KID, 1);

  int st = 0;
#pragma unroll 1
  for (int kt = 0; kt < NK; ++kt) {
    wait_lgkm0();
    bar();
    const LDS_AS char* sp = smem + st * STG;
    bf16x8 xb[2];
#pragma unroll
    for (int mf = 0; mf < 2; ++mf) xb[mf] = lds_read16(sp + aoff + mf * 2048);
#pragma unroll
    for (int cc = 0; cc < NC; ++cc) {
      bf16x4 w0 = lds_tr(sp + boff_t0 + cc * 256);
      bf16x4 w1 = lds_tr(sp + boff_t1 + cc * 256);
      bf16x8 wf = cat4(w0, w1);
      acc[0][cc] = mfma16(wf, xb[0], acc[0][cc]);
      acc[1][cc] = mfma16(wf, xb[1], acc[1][cc]);
    }
    st = (st + 1 == NS) ? 0 : st + 1;
  }
  STAMP(KID, 14);

  wait_lgkm0();
  bar();
  {
    LDS_AS char* rp = smem + (wm * 2 + kh) * NC * 1024 + l * 16;
    if (kh) {
#pragma unroll
      for (int cc = 0; cc < NC; ++cc) {
        *(LDS_AS f32x4*)(rp + cc * 1024) = acc[0][cc];
        acc[0][cc] = acc[1][cc];
      }
    } else {
#pragma unroll
      for (int cc = 0; cc < NC; ++cc) *(LDS_AS f32x4*)(rp + cc * 1024) = acc[1][cc];
    }
  }
  wait_lgkm0();
  bar();
  const int row = m0 + 32 * wm + 16 * kh + (l & 15);
  const LDS_AS char* rp = smem + (wm * 2 + (1 - kh)) * NC * 1024 + l * 16;
  if constexpr (EPI == 2) {
    float* op = (float*)outv + ((size_t)ks * 2048 + row) * 768 + n0 + 4 * g;
#pragma unroll
    for (int cc = 0; cc < NC; ++cc) *(f32x4*)(op + 16 * cc) = acc[0][cc] + lds_read16f(rp + cc * 1024);
  } else {
    u16* out = (u16*)outv;
    unsigned pk[NC][2];
#pragma unroll
    for (int cc = 0; cc < NC; ++cc) {
      f32x4 v = acc[0][cc] + lds_read16f(rp + cc * 1024);
      if constexpr (EPI == 1) {
        const float c1 = -2.302208198144325f;
        const float c2 = -0.1029432397500235f;
        unsigned y01 = pack2(v[0], v[1]), y23 = pack2(v[2], v[3]);
        float y[4] = {lo_f(y01), hi_f(y01), lo_f(y23), hi_f(y23)};
#pragma unroll
        for (int j = 0; j < 4; ++j) {
          float e = __builtin_amdgcn_exp2f(y[j] * (c1 + c2 * y[j] * y[j]));
          v[j] = y[j] * __builtin_amdgcn_rcpf(1.f + e);
        }
      }
      pk[cc][0] = pack2(v[0], v[1]);
      pk[cc][1] = pack2(v[2], v[3]);
    }
    auto dst_ptr = [&](int n) -> u16* {
      if constexpr (EPI == 1) {
        return out + (size_t)row * 3072 + n;
      } else {
        if (n < 1536) return out + (size_t)row * 1536 + n;
        int nv = n - 1536;
        int hd = nv / 96;
        int dd = nv - hd * 96;
        return out2 + ((size_t)(((row >> 10) * 8 + hd) * 1024 + (row & 1023))) * 96 + dd;
      }
    };
#pragma unroll
    for (int a = 0; a < NC / 2; ++a) {
      u32x4 o = pair_pack(pk[2 * a][0], pk[2 * a][1], pk[2 * a + 1][0], pk[2 * a + 1][1]);
      st16(dst_ptr(n0 + 32 * a + (g & 1) * 16 + (g & 2) * 4), o);
    }
    if constexpr (NC & 1) {
      u32x2 o;
      o[0] = pk[NC - 1][0];
      o[1] = pk[NC - 1][1];
      st8(dst_ptr(n0 + 16 * (NC - 1) + 4 * g), o);
    }
  }
  STAMP(KID, 15);
}

__global__ __launch_bounds__(256) void k_final(const float* __restrict__ part, const float* __restrict__ modp,
                                               const u16* __restrict__ x1, u16* __restrict__ out) {
  const int t = blockIdx.x * 256 + threadIdx.x;
  const int row = t / 96, c8 = (t - row * 96) * 8;
  const int b = row >> 10;
  const size_t e = (size_t)row * 768 + c8;
  f32x4 p[4][2];
#pragma unroll
  for (int k = 0; k < 4; ++k) {
    p[k][0] = *(const f32x4*)(part + (size_t)k * 2048 * 768 + e);
    p[k][1] = *(const f32x4*)(part + (size_t)k * 2048 * 768 + e + 4);
  }
  u32x4 xr = *(const u32x4*)(x1 + e);
  const float* mp0 = modp + (0 * 2 + b) * 4608 + 5 * 768 + c8;
  const float* mp1 = modp + (1 * 2 + b) * 4608 + 5 * 768 + c8;
  f32x4 ga[2] = {*(const f32x4*)mp0, *(const f32x4*)(mp0 + 4)};
  f32x4 gb[2] = {*(const f32x4*)mp1, *(const f32x4*)(mp1 + 4)};
  float v[8];
#pragma unroll
  for (int hh = 0; hh < 2; ++hh)
#pragma unroll
    for (int j = 0; j < 4; ++j) {
      float m = (p[0][hh][j] + p[1][hh][j]) + (p[2][hh][j] + p[3][hh][j]);
      float gate = rbf(ga[hh][j] + gb[hh][j]);
      unsigned xw = xr[hh * 2 + (j >> 1)];
      float xv = (j & 1) ? hi_f(xw) : lo_f(xw);
      v[hh * 4 + j] = xv + rbf(gate * rbf(m));
    }
  u32x4 o;
#pragma unroll
  for (int j = 0; j < 4; ++j) o[j] = pack2(v[2 * j], v[2 * j + 1]);
  *(u32x4*)(out + e) = o;
}

__global__ __launch_bounds__(768, 3) void k_scores(const u16* __restrict__ qk, u16* __restrict__ P,
                                                   f32x2* __restrict__ ml) {
  constexpr int NS = 4;
  constexpr int ASZ = 128 * 128;
  constexpr int STG = 2 * ASZ;
  constexpr int NK = 384 / 64;
  constexpr int NLD = 8;
  constexpr int LDQ = 1536;
  __shared__ __attribute__((aligned(16))) char smem_raw[NS * STG];
  LDS_AS char* smem = (LDS_AS char*)smem_raw;
  const unsigned lds0 = (unsigned)(uintptr_t)smem;
  STAMP(2, 0);
  const int tid = threadIdx.x, l = tid & 63;
  const int w = uni(tid >> 6);
  const int bid = blockIdx.x;
  const int xcd = bid & 7, idx = bid >> 3;
  const int bh = xcd >> 1;
  const int tq = (xcd & 1) * 4 + (idx & 3);
  const int tk = idx >> 2;
  const int b = bh >> 1, h = bh & 1;

  if (w >= 8) {
    const int lw = w - 8;
    const unsigned voff = (unsigned)((l >> 3) * LDQ * 2 + (((l & 7) ^ (l >> 3)) << 4));
    const char* qbase = (const char*)(qk + (size_t)(b * 1024 + tq * 128 + 32 * lw) * LDQ + h * 384);
    const char* kbase = (const char*)(qk + (size_t)(b * 1024 + tk * 128 + 32 * lw) * LDQ + 768 + h * 384);
    auto issue = [&](int kt, int st) {
      const unsigned sa = lds0 + st * STG;
#pragma unroll
      for (int i = 0; i < 4; ++i)
        glds16(qbase + (size_t)i * 8 * LDQ * 2 + kt * 128, voff, sa + (32 * lw + 8 * i) * 128);
#pragma unroll
      for (int i = 0; i < 4; ++i)
        glds16(kbase + (size_t)i * 8 * LDQ * 2 + kt * 128, voff, sa + ASZ + (32 * lw + 8 * i) * 128);
    };
#pragma unroll
    for (int s = 0; s < NS - 1; ++s) issue(s, s);
    int stn = NS - 1;
#pragma unroll 1
    for (int kt = 0; kt < NK; ++kt) {
      if (kt < NK - (NS - 2)) wait_vm<(NS - 2) * NLD>();
      else wait_vm<0>();
      bar();
      if (kt + NS - 1 < NK) issue(kt + NS - 1, stn);
      stn = (stn + 1 == NS) ? 0 : stn + 1;
    }
    bar();
    bar();
    return;
  }

  const int wm = w & 3, kh = w >> 2;
  const int g = l >> 4;
  const unsigned qoff = (unsigned)(((32 * wm + (l & 15)) * 128 + ((g ^ (l & 7)) << 4)) ^ (kh * 64));
  const unsigned koff = (unsigned)(ASZ + (((l & 15) * 128 + ((g ^ (l & 7)) << 4)) ^ (kh * 64)));
  f32x4 acc[2][8];
#pragma unroll
  for (int i = 0; i < 2; ++i)
#pragma unroll
    for (int j = 0; j < 8; ++j) acc[i][j] = (f32x4){0.f, 0.f, 0.f, 0.f};

  int st = 0;
#pragma unroll 1
  for (int kt = 0; kt < NK; ++kt) {
    wait_lgkm0();
    bar();
    const LDS_AS char* sp = smem + st * STG;
    bf16x8 qf[2];
#pragma unroll
    for (int mf = 0; mf < 2; ++mf) qf[mf] = lds_read16(sp + qoff + mf * 2048);
#pragma unroll
    for (int kf = 0; kf < 8; ++kf) {
      bf16x8 kfr = lds_read16(sp + koff + kf * 2048);
      acc[0][kf] = mfma16(kfr, qf[0], acc[0][kf]);
      acc[1][kf] = mfma16(kfr, qf[1], acc[1][kf]);
    }
    st = (st + 1 == NS) ? 0 : st + 1;
  }
  STAMP(2, 14);
  wait_lgkm0();
  bar();
  {
    LDS_AS char* rp = smem + (wm * 2 + kh) * 8 * 1024 + l * 16;
    if (kh) {
#pragma unroll
      for (int kf = 0; kf < 8; ++kf) {
        *(LDS_AS f32x4*)(rp + kf * 1024) = acc[0][kf];
        acc[0][kf] = acc[1][kf];
      }
    } else {
#pragma unroll
      for (int kf = 0; kf < 8; ++kf) *(LDS_AS f32x4*)(rp + kf * 1024) = acc[1][kf];
    }
  }
  wait_lgkm0();
  bar();
  f32x4 s[8];
  {
    const LDS_AS char* rp = smem + (wm * 2 + (1 - kh)) * 8 * 1024 + l * 16;
#pragma unroll
    for (int kf = 0; kf < 8; ++kf) {
      f32x4 v = acc[0][kf] + lds_read16f(rp + kf * 1024);
      unsigned a01 = pack2(v[0], v[1]), a23 = pack2(v[2], v[3]);
      a01 = pack2(lo_f(a01) * 0.051031036307982884f, hi_f(a01) * 0.051031036307982884f);
      a23 = pack2(lo_f(a23) * 0.051031036307982884f, hi_f(a23) * 0.051031036307982884f);
      s[kf] = (f32x4){lo_f(a01), hi_f(a01), lo_f(a23), hi_f(a23)};
    }
  }
  const float l2e = 1.4426950408889634f;
  float mx = -INFINITY;
#pragma unroll
  for (int kf = 0; kf < 8; ++kf)
#pragma unroll
    for (int j = 0; j < 4; ++j) mx = fmaxf(mx, s[kf][j]);
  mx = fmaxf(mx, __shfl_xor(mx, 16));
  mx = fmaxf(mx, __shfl_xor(mx, 32));
  const float mbl = mx * l2e;
  float sum = 0.f;
  unsigned pk[8][2];
#pragma unroll
  for (int kf = 0; kf < 8; ++kf) {
    float e[4];
#pragma unroll
    for (int j = 0; j < 4; ++j) {
      e[j] = __builtin_amdgcn_exp2f(s[kf][j] * l2e - mbl);
      sum += e[j];
    }
    pk[kf][0] = pack2(e[0], e[1]);
    pk[kf][1] = pack2(e[2], e[3]);
  }
  sum += __shfl_xor(sum, 16);
  sum += __shfl_xor(sum, 32);
  const int qrow = tq * 128 + 32 * wm + 16 * kh + (l & 15);
  u16* op = P + ((size_t)bh * 1024 + qrow) * 1024 + tk * 128;
#pragma unroll
  for (int a = 0; a < 4; ++a) {
    u32x4 o = pair_pack(pk[2 * a][0], pk[2 * a][1], pk[2 * a + 1][0], pk[2 * a + 1][1]);
    st16(op + 32 * a + (g & 1) * 16 + (g & 2) * 4, o);
  }
  if (g == 0) {
    f32x2 s2;
    s2[0] = mbl;
    s2[1] = sum;
    ml[((size_t)bh * 1024 + qrow) * 8 + tk] = s2;
  }
  STAMP(2, 15);
}

template <int MODE>
__global__ __launch_bounds__(512, 2) void k_gemm_small(const u16* __restrict__ Ain, const u16* __restrict__ Bin,
                                                       const float* __restrict__ modp,
                                                       const u16* __restrict__ resid,
                                                       const f32x2* __restrict__ ml, f32x2* __restrict__ stats,
                                                       u16* __restrict__ out) {
  constexpr int KTOT = (MODE == 0) ? 1024 : (MODE == 1 ? 768 : 3072);
  constexpr int KH = KTOT / 2;
  constexpr int NK = KH / 64;
  constexpr int LDA = KTOT;
  constexpr int LDB = (MODE == 0) ? 96 : 768;
  constexpr int NS = 4;
  constexpr int STG = 40960;
  constexpr int NLD = 10;
  __shared__ __attribute__((aligned(16))) char smem_raw[NS * STG];
  LDS_AS char* smem = (LDS_AS char*)smem_raw;
  const unsigned lds0 = (unsigned)(uintptr_t)smem;
  STAMP(3 + MODE, 0);
  const int tid = threadIdx.x, l = tid & 63;
  const int w = uni(tid >> 6);
  const int bid = blockIdx.x;
  const int xcd = bid & 7, idx = bid >> 3;
  const int g = l >> 4;

  int m0, n0, bh = 0;
  const u16* Ap;
  const u16* Bp;
  if constexpr (MODE == 0) {
    bh = xcd >> 1;
    const int tq = (xcd & 1) * 8 + (idx & 7);
    const int td = idx >> 3;
    m0 = tq * 64;
    n0 = td * 96;
    Ap = Ain + ((size_t)bh * 1024 + m0) * 1024;
    Bp = Bin + ((size_t)(bh * 4 + td) * 1024) * 96;
  } else {
    const int tm = xcd * 4 + (idx & 3);
    const int tn = idx >> 2;
    m0 = tm * 64;
    n0 = tn * 96;
    Ap = Ain + (size_t)m0 * LDA;
    Bp = Bin + n0;
  }

  if (w >= 4) {
    const int lw = w - 4;
    const int kq = lw & 1, part = lw >> 1;
    const unsigned voffA = (unsigned)((l >> 3) * LDA * 2 + (((l & 7) ^ (l >> 3)) << 4));
    unsigned voffB[6];
#pragma unroll
    for (int i = 0; i < 6; ++i) {
      int sub = 24 * part + 4 * i + (l >> 4);
      int kb = sub / 6, cc = sub % 6;
      int within = l & 15;
      int r = (within >> 1) ^ ((kb & 1) << 2);
      voffB[i] = (unsigned)(((kb * 8 + r) * LDB + cc * 16 + (within & 1) * 8) * 2);
    }
    const char* abase = (const char*)(Ap + (size_t)(32 * part) * LDA + kq * KH);
    const char* bbase = (const char*)(Bp + (size_t)(kq * KH) * LDB);
    auto issue = [&](int kt, int st) {
      const unsigned sa = lds0 + st * STG;
#pragma unroll
      for (int i = 0; i < 4; ++i)
        glds16(abase + (size_t)i * 8 * LDA * 2 + kt * 128, voffA, sa + kq * 8192 + (32 * part + 8 * i) * 128);
      const char* sb = bbase + (size_t)kt * 64 * LDB * 2;
#pragma unroll
      for (int i = 0; i < 6; ++i) glds16(sb, voffB[i], sa + 16384 + kq * 12288 + (24 * part + 4 * i) * 256);
    };
#pragma unroll
    for (int s = 0; s < NS - 1; ++s) issue(s, s);
    int stn = NS - 1;
#pragma unroll 1
    for (int kt = 0; kt < NK; ++kt) {
      if (kt < NK - (NS - 2)) wait_vm<(NS - 2) * NLD>();
      else wait_vm<0>();
      bar();
      if (kt + NS - 1 < NK) issue(kt + NS - 1, stn);
      stn = (stn + 1 == NS) ? 0 : stn + 1;
    }
    bar();
    bar();
    return;
  }

  const int kq = w & 1, nh = w >> 1;
  float cf[4][4];
  if constexpr (MODE == 0) {
#pragma unroll
    for (int mf = 0; mf < 4; ++mf) {
      const f32x4* sp = (const f32x4*)(ml + ((size_t)bh * 1024 + m0 + 16 * mf + (l & 15)) * 8);
      f32x4 sv[4];
#pragma unroll
      for (int j = 0; j < 4; ++j) sv[j] = sp[j];
      float mx = fmaxf(sv[0][0], sv[0][2]);
#pragma unroll
      for (int j = 1; j < 4; ++j) mx = fmaxf(mx, fmaxf(sv[j][0], sv[j][2]));
      float e[8];
      float lsum = 0.f;
#pragma unroll
      for (int j = 0; j < 4; ++j) {
        e[2 * j] = __builtin_amdgcn_exp2f(sv[j][0] - mx);
        e[2 * j + 1] = __builtin_amdgcn_exp2f(sv[j][2] - mx);
        lsum += e[2 * j] * sv[j][1] + e[2 * j + 1] * sv[j][3];
      }
      float inv = 1.f / lsum;
      if (kq) {
#pragma unroll
        for (int j = 0; j < 4; ++j) cf[mf][j] = e[4 + j] * inv;
      } else {
#pragma unroll
        for (int j = 0; j < 4; ++j) cf[mf][j] = e[j] * inv;
      }
    }
  }
  f32x4 gq[2][3][2];
  u32x2 xr[2][3];
  if constexpr (MODE != 0) {
    const int goff = (MODE == 1) ? 2 * 768 : 5 * 768;
    const int b = m0 >> 10;
    const float* mp0 = modp + (0 * 2 + b) * 4608 + goff + n0 + 48 * nh + 4 * g;
    const float* mp1 = modp + (1 * 2 + b) * 4608 + goff + n0 + 48 * nh + 4 * g;
#pragma unroll
    for (int cc = 0; cc < 3; ++cc) {
      gq[0][cc][0] = *(const f32x4*)(mp0 + 16 * cc);
      gq[0][cc][1] = *(const f32x4*)(mp1 + 16 * cc);
    }
#pragma unroll
    for (int mf = 0; mf < 2; ++mf) {
      const u16* rp = resid + (size_t)(m0 + 32 * kq + 16 * mf + (l & 15)) * 768 + n0 + 48 * nh + 4 * g;
#pragma unroll
      for (int cc = 0; cc < 3; ++cc) xr[mf][cc] = *(const u32x2*)(rp + 16 * cc);
    }
  }

  const unsigned aoff0 = (unsigned)(kq * 8192 + (l & 15) * 128 + ((g ^ (l & 7)) << 4));
  const int q = (l & 15) >> 2, p = l & 3;
  const unsigned boff_t0 = (unsigned)(16384 + kq * 12288 + (g * 6 + 3 * nh) * 256 + ((4 * (g & 1) + q) * 32) + p * 8);
  const unsigned boff_t1 =
      (unsigned)(16384 + kq * 12288 + (g * 6 + 3 * nh) * 256 + ((4 * (1 - (g & 1)) + q) * 32) + p * 8);

  f32x4 acc[4][3];
  f32x4 tot[4][3];
#pragma unroll
  for (int i = 0; i < 4; ++i)
#pragma unroll
    for (int j = 0; j < 3; ++j) {
      acc[i][j] = (f32x4){0.f, 0.f, 0.f, 0.f};
      tot[i][j] = (f32x4){0.f, 0.f, 0.f, 0.f};
    }
  STAMP(3 + MODE, 1);

  int st = 0;
#pragma unroll 1
  for (int kt = 0; kt < NK; ++kt) {
    wait_lgkm0();
    bar();
    const LDS_AS char* sp = smem + st * STG;
#pragma unroll
    for (int ks = 0; ks < 2; ++ks) {
      bf16x8 af[4];
#pragma unroll
      for (int mf = 0; mf < 4; ++mf) af[mf] = lds_read16(sp + ((aoff0 ^ (ks * 64)) + mf * 2048));
#pragma unroll
      for (int cc = 0; cc < 3; ++cc) {
        bf16x4 w0 = lds_tr(sp + boff_t0 + ks * 6144 + cc * 256);
        bf16x4 w1 = lds_tr(sp + boff_t1 + ks * 6144 + cc * 256);
        bf16x8 wf = cat4(w0, w1);
#pragma unroll
        for (int mf = 0; mf < 4; ++mf) acc[mf][cc] = mfma16(wf, af[mf], acc[mf][cc]);
      }
    }
    if constexpr (MODE == 0) {
      if (kt & 1) {
#pragma unroll
        for (int mf = 0; mf < 4; ++mf) {
          const float c = cf[mf][0];
          cf[mf][0] = cf[mf][1];
          cf[mf][1] = cf[mf][2];
          cf[mf][2] = cf[mf][3];
#pragma unroll
          for (int cc = 0; cc < 3; ++cc) {
            tot[mf][cc] += acc[mf][cc] * c;
            acc[mf][cc] = (f32x4){0.f, 0.f, 0.f, 0.f};
          }
        }
      }
    }
    st = (st + 1 == NS) ? 0 : st + 1;
  }
  if constexpr (MODE == 0) {
#pragma unroll
    for (int mf = 0; mf < 4; ++mf)
#pragma unroll
      for (int cc = 0; cc < 3; ++cc) acc[mf][cc] = tot[mf][cc];
  }
  STAMP(3 + MODE, 12);
  wait_lgkm0();
  bar();
  {
    LDS_AS char* rp = smem + (nh * 2 + kq) * 6144 + l * 16;
    if (kq) {
#pragma unroll
      for (int mf = 0; mf < 2; ++mf)
#pragma unroll
        for (int cc = 0; cc < 3; ++cc) {
          *(LDS_AS f32x4*)(rp + (mf * 3 + cc) * 1024) = acc[mf][cc];
          acc[mf][cc] = acc[2 + mf][cc];
        }
    } else {
#pragma unroll
      for (int mf = 0; mf < 2; ++mf)
#pragma unroll
        for (int cc = 0; cc < 3; ++cc) *(LDS_AS f32x4*)(rp + (mf * 3 + cc) * 1024) = acc[2 + mf][cc];
    }
  }
  wait_lgkm0();
  bar();
  f32x4 r[2][3];
  {
    const LDS_AS char* rp = smem + (nh * 2 + (1 - kq)) * 6144 + l * 16;
#pragma unroll
    for (int mf = 0; mf < 2; ++mf)
#pragma unroll
      for (int cc = 0; cc < 3; ++cc)
        r[mf][cc] = acc[mf][cc] + lds_read16f(rp + (mf * 3 + cc) * 1024);
  }
  STAMP(3 + MODE, 13);
#pragma unroll
  for (int mf = 0; mf < 2; ++mf) {
    const int mrow = m0 + 32 * kq + 16 * mf + (l & 15);
    float v[3][4];
    u16* op;
    if constexpr (MODE == 0) {
      op = out + (size_t)((bh >> 1) * 1024 + mrow) * 768 + (bh & 1) * 384 + n0 + 48 * nh;
#pragma unroll
      for (int cc = 0; cc < 3; ++cc)
#pragma unroll
        for (int j = 0; j < 4; ++j) v[cc][j] = r[mf][cc][j];
    } else {
      op = out + (size_t)mrow * 768 + n0 + 48 * nh;
#pragma unroll
      for (int cc = 0; cc < 3; ++cc) {
        float xv[4] = {lo_f(xr[mf][cc][0]), hi_f(xr[mf][cc][0]), lo_f(xr[mf][cc][1]), hi_f(xr[mf][cc][1])};
#pragma unroll
        for (int j = 0; j < 4; ++j) {
          float gate = rbf(gq[0][cc][0][j] + gq[0][cc][1][j]);
          v[cc][j] = rbf(xv[j] + rbf(gate * rbf(r[mf][cc][j])));
        }
      }
    }
    {
      u32x4 o = pair_pack(pack2(v[0][0], v[0][1]), pack2(v[0][2], v[0][3]), pack2(v[1][0], v[1][1]),
                          pack2(v[1][2], v[1][3]));
      st16(op + (g & 1) * 16 + (g & 2) * 4, o);
      u32x2 o2;
      o2[0] = pack2(v[2][0], v[2][1]);
      o2[1] = pack2(v[2][2], v[2][3]);
      st8(op + 32 + 4 * g, o2);
    }
    if constexpr (MODE == 1) {
      float s = 0.f;
#pragma unroll
      for (int cc = 0; cc < 3; ++cc)
#pragma unroll
        for (int j = 0; j < 4; ++j) s += v[cc][j];
      s += __shfl_xor(s, 16);
      s += __shfl_xor(s, 32);
      float mean = s * (1.f / 48.f);
      float m2 = 0.f;
#pragma unroll
      for (int cc = 0; cc < 3; ++cc)
#pragma unroll
        for (int j = 0; j < 4; ++j) {
          float d = v[cc][j] - mean;
          m2 += d * d;
        }
      m2 += __shfl_xor(m2, 16);
      m2 += __shfl_xor(m2, 32);
      if (g == 0) {
        f32x2 s2;
        s2[0] = s;
        s2[1] = m2;
        stats[(size_t)mrow * 16 + (n0 / 48) + nh] = s2;
      }
    }
  }
  STAMP(3 + MODE, 15);
}

extern "C" void kernel_launch(void* const* d_in, const int* in_sizes, int n_in, void* d_out, int out_size,
                              void* d_ws, size_t ws_size, hipStream_t stream) {
  const u16* x = (const u16*)d_in[0];
  const u16* c = (const u16*)d_in[1];
  const u16* wqkv = (const u16*)d_in[2];
  const u16* wo = (const u16*)d_in[3];
  const u16* wmod = (const u16*)d_in[4];
  const u16* wfc1 = (const u16*)d_in[5];
  const u16* wfc2 = (const u16*)d_in[6];
  u16* out = (u16*)d_out;

  char* ws = (char*)d_ws;
  size_t off = 0;
  auto alloc = [&](size_t bytes) {
    char* p = ws + off;
    off += (bytes + 255) & ~(size_t)255;
    return p;
  };
  float* modp = (float*)alloc(2 * 2 * 4608 * 4);
  u16* h = (u16*)alloc((size_t)Mrows * 768 * 2);
  u16* qk = (u16*)alloc((size_t)Mrows * 1536 * 2);
  u16* Vt = (u16*)alloc((size_t)Mrows * 768 * 2);
  u16* P = (u16*)alloc((size_t)4 * 1024 * 1024 * 2);
  f32x2* ml = (f32x2*)alloc((size_t)4 * 1024 * 8 * 8);
  u16* O = (u16*)alloc((size_t)Mrows * 768 * 2);
  u16* x1 = (u16*)alloc((size_t)Mrows * 768 * 2);
  f32x2* st2 = (f32x2*)alloc((size_t)Mrows * 16 * 8);
  u16* h2 = (u16*)alloc((size_t)Mrows * 768 * 2);
  u16* u = (u16*)alloc((size_t)Mrows * 3072 * 2);

  k0_mod<<<144 + 96, 256, 0, stream>>>(c, wmod, modp, wqkv, wo);
  k_lnmod<<<256 + 192, 256, 0, stream>>>(x, modp, 0, h, wfc1, wfc2);
  k_gemm_big<144, 4, 0><<<256, 768, 0, stream>>>(h, wqkv, qk, Vt);
  k_scores<<<256, 768, 0, stream>>>(qk, P, ml);
  k_gemm_small<0><<<256, 512, 0, stream>>>(P, Vt, modp, nullptr, ml, nullptr, O);
  k_gemm_small<1><<<256, 512, 0, stream>>>(O, wo, modp, x, nullptr, st2, x1);
  k_lnmod<<<256, 256, 0, stream>>>(x1, modp, 3 * 768, h2, nullptr, nullptr);
  k_gemm_big<192, 4, 1><<<256, 768, 0, stream>>>(h2, wfc1, u, nullptr);
  k_gemm_small<2><<<256, 512, 0, stream>>>(u, wfc2, modp, x1, nullptr, nullptr, out);
}
```

```cpp
#include <hip/hip_runtime.h>
#include <hip/hip_bf16.h>
#include <stdint.h>
#include <type_traits>

typedef __attribute__((ext_vector_type(8))) __bf16 bf16x8;
typedef __attribute__((ext_vector_type(4))) __bf16 bf16x4;
typedef __attribute__((ext_vector_type(2))) __bf16 bf16x2;
typedef __attribute__((ext_vector_type(4))) float f32x4;
typedef __attribute__((ext_vector_type(2))) float f32x2;
typedef __attribute__((ext_vector_type(4))) unsigned u32x4;
typedef __attribute__((ext_vector_type(2))) unsigned u32x2;
typedef unsigned short u16;
#define LDS_AS __attribute__((address_space(3)))

#define DEVI __device__ __forceinline__
#define STAMP(K, I)

static constexpr int Bsz = 2, Nseq = 1024, Dm = 768, Ff = 3072, Dh = 384;
static constexpr int Mrows = Bsz * Nseq;

template <int I, int N, class F>
DEVI void static_for(F&& f) {
  if constexpr (I < N) {
    f(std::integral_constant<int, I>{});
    static_for<I + 1, N>(f);
  }
}

DEVI float lo_f(unsigned u) { return __uint_as_float(u << 16); }
DEVI float hi_f(unsigned u) { return __uint_as_float(u & 0xffff0000u); }
DEVI float bf2f(u16 h) { return __uint_as_float(((unsigned)h) << 16); }
DEVI unsigned pack2(float a, float b) {
  bf16x2 v;
  v[0] = (__bf16)a;
  v[1] = (__bf16)b;
  return __builtin_bit_cast(unsigned, v);
}
DEVI float rbf(float a) { return lo_f(pack2(a, 0.f) ); }

template <int N>
DEVI void wait_vm() { asm volatile("s_waitcnt vmcnt(%0)" ::"i"(N) : "memory"); }
DEVI void wait_lgkm0() { asm volatile("s_waitcnt lgkmcnt(0)" ::: "memory"); }
DEVI void bar() { __builtin_amdgcn_s_barrier(); }

DEVI void glds16(const void* sbase, unsigned voff, unsigned lds_dst) {
  unsigned keep;
  asm volatile(
      "s_mov_b32 %0, m0\n\t"
      "s_mov_b32 m0, %3\n\t"
      "s_nop 0\n\t"
      "global_load_lds_dwordx4 %1, %2\n\t"
      "s_mov_b32 m0, %0"
      : "=&s"(keep)
      : "v"(voff), "s"(sbase), "s"(lds_dst)
      : "memory");
}

DEVI void gld16(u32x4& d, const void* p) { asm volatile("global_load_dwordx4 %0, %1, off" : "=v"(d) : "v"(p) : "memory"); }
DEVI void gld8(u32x2& d, const void* p) { asm volatile("global_load_dwordx2 %0, %1, off" : "=v"(d) : "v"(p) : "memory"); }
template <class T>
DEVI void pin(T& v) { asm volatile("" : "+v"(v)); }
DEVI f32x4 asf4(u32x4 v) { return __builtin_bit_cast(f32x4, v); }

DEVI void st8_dev(void* p, u32x2 v) { asm volatile("global_store_dwordx2 %0, %1, off sc1\n\ts_nop 1" ::"v"(p), "v"(v) : "memory"); }

DEVI bf16x8 lds_read16(const LDS_AS char* p) { return *(const LDS_AS bf16x8*)p; }
DEVI u32x4 lds_read16u(const LDS_AS char* p) { return *(const LDS_AS u32x4*)p; }
DEVI f32x4 lds_read16f(const LDS_AS char* p) { return *(const LDS_AS f32x4*)p; }
DEVI bf16x4 lds_tr(const LDS_AS char* p) {
  return __builtin_amdgcn_ds_read_tr16_b64_v4bf16((LDS_AS bf16x4*)p);
}
DEVI bf16x8 cat4(bf16x4 a, bf16x4 b) {
  return __builtin_shufflevector(a, b, 0, 1, 2, 3, 4, 5, 6, 7);
}
DEVI f32x4 mfma16(bf16x8 a, bf16x8 b, f32x4 c) {
  return __builtin_amdgcn_mfma_f32_16x16x32_bf16(a, b, c, 0, 0, 0);
}
DEVI int uni(int v) { return __builtin_amdgcn_readfirstlane(v); }

DEVI u32x4 pair_pack(unsigned ax, unsigned ay, unsigned bx, unsigned by) {
  auto r0 = __builtin_amdgcn_permlane16_swap(ax, bx, false, false);
  auto r1 = __builtin_amdgcn_permlane16_swap(ay, by, false, false);
  u32x4 o;
  o[0] = r0[0];
  o[1] = r1[0];
  o[2] = r0[1];
  o[3] = r1[1];
  return o;
}

__global__ __launch_bounds__(256) void k0_mod(const u16* __restrict__ c, const u16* __restrict__ wmod,
                                              float* __restrict__ modp, const u16* __restrict__ w0,
                                              const u16* __restrict__ w1, const u16* __restrict__ w2,
                                              const u16* __restrict__ w3, unsigned* __restrict__ cnt) {
  __shared__ float sc[2][384];
  __shared__ float red[32][2][64];
  const int tid = threadIdx.x;
  if (blockIdx.x >= 144) {
    const int t = (blockIdx.x - 144) * 256 + tid;
    if (t < 64) cnt[t * 32] = 0u;
    u32x4 a = {0u, 0u, 0u, 0u};
    u32x4 v[12];
#pragma unroll
    for (int i = 0; i < 12; ++i) {
      int ci = t + i * 73728;
      const u16* p;
      if (ci < 221184) p = w0 + (size_t)ci * 8;
      else if (ci < 294912) p = w1 + (size_t)(ci - 221184) * 8;
      else if (ci < 589824) p = w2 + (size_t)(ci - 294912) * 8;
      else p = w3 + (size_t)(ci - 589824) * 8;
      v[i] = *(const u32x4*)p;
    }
#pragma unroll
    for (int i = 0; i < 12; ++i) a ^= v[i];
    asm volatile("" ::"v"(a));
    return;
  }
  const int cb = blockIdx.x % 72, kp = blockIdx.x / 72;
  const int tc = tid & 7, tr = tid >> 3;
  u32x4 w[12];
  const u16* wp = wmod + (size_t)(kp * 384 + tr) * 4608 + cb * 64 + tc * 8;
#pragma unroll
  for (int i = 0; i < 12; ++i) w[i] = *(const u32x4*)(wp + (size_t)i * 32 * 4608);
  for (int i = tid; i < 768; i += 256) {
    int b = i / 384, k = i % 384;
    float v = bf2f(c[b * 768 + kp * 384 + k]);
    float s = v / (1.f + __expf(-v));
    sc[b][k] = rbf(s);
  }
  __syncthreads();
  float acc[2][8];
#pragma unroll
  for (int b = 0; b < 2; ++b)
#pragma unroll
    for (int j = 0; j < 8; ++j) acc[b][j] = 0.f;
#pragma unroll
  for (int i = 0; i < 12; ++i) {
    float s0 = sc[0][tr + 32 * i], s1 = sc[1][tr + 32 * i];
#pragma unroll
    for (int j = 0; j < 4; ++j) {
      float a = lo_f(w[i][j]), bb = hi_f(w[i][j]);
      acc[0][2 * j] += s0 * a;
      acc[0][2 * j + 1] += s0 * bb;
      acc[1][2 * j] += s1 * a;
      acc[1][2 * j + 1] += s1 * bb;
    }
  }
#pragma unroll
  for (int b = 0; b < 2; ++b)
#pragma unroll
    for (int j = 0; j < 8; ++j) red[tr][b][tc * 8 + j] = acc[b][j];
  __syncthreads();
  if (tid < 128) {
    int b = tid >> 6, col = tid & 63;
    float s = 0.f;
#pragma unroll
    for (int r = 0; r < 32; ++r) s += red[r][b][col];
    modp[(kp * 2 + b) * 4608 + cb * 64 + col] = s;
  }
}

__global__ __launch_bounds__(256) void k_lnmod(const u16* __restrict__ x, const float* __restrict__ modp, int offS,
                                               u16* __restrict__ h, const u16* __restrict__ w0,
                                               const u16* __restrict__ w1) {
  const int tid = threadIdx.x;
  if (blockIdx.x >= 256) {
    const int t = (blockIdx.x - 256) * 256 + tid;
    u32x4 a = {0u, 0u, 0u, 0u};
    u32x4 v[12];
#pragma unroll
    for (int i = 0; i < 12; ++i) {
      int ci = t + i * 49152;
      const u16* p = (ci < 294912) ? (w0 + (size_t)ci * 8) : (w1 + (size_t)(ci - 294912) * 8);
      v[i] = *(const u32x4*)p;
    }
#pragma unroll
    for (int i = 0; i < 12; ++i) a ^= v[i];
    asm volatile("" ::"v"(a));
    return;
  }
  const int w = tid >> 6, l = tid & 63;
  const int row0 = blockIdx.x * 8 + w * 2;
  const int b = row0 >> 10;
  u32x2 v[2][3];
#pragma unroll
  for (int r = 0; r < 2; ++r) {
    const u16* xp = x + (size_t)(row0 + r) * 768 + l * 4;
#pragma unroll
    for (int i = 0; i < 3; ++i) v[r][i] = *(const u32x2*)(xp + i * 256);
  }
  f32x4 gg[3], tt[3];
#pragma unroll
  for (int i = 0; i < 3; ++i) {
    const float* m0p = modp + (0 * 2 + b) * 4608 + offS + l * 4 + i * 256;
    const float* m1p = modp + (1 * 2 + b) * 4608 + offS + l * 4 + i * 256;
    f32x4 s0 = *(const f32x4*)m0p, s1 = *(const f32x4*)m1p;
    f32x4 c0 = *(const f32x4*)(m0p + 768), c1 = *(const f32x4*)(m1p + 768);
#pragma unroll
    for (int j = 0; j < 4; ++j) {
      tt[i][j] = rbf(s0[j] + s1[j]);
      gg[i][j] = rbf(1.f + rbf(c0[j] + c1[j]));
    }
  }
#pragma unroll
  for (int r = 0; r < 2; ++r) {
    float f[12];
#pragma unroll
    for (int i = 0; i < 3; ++i) {
      f[4 * i] = lo_f(v[r][i][0]);
      f[4 * i + 1] = hi_f(v[r][i][0]);
      f[4 * i + 2] = lo_f(v[r][i][1]);
      f[4 * i + 3] = hi_f(v[r][i][1]);
    }
    float s = 0.f;
#pragma unroll
    for (int i = 0; i < 12; ++i) s += f[i];
#pragma unroll
    for (int o = 32; o > 0; o >>= 1) s += __shfl_xor(s, o);
    float mean = s * (1.f / 768.f);
    float q = 0.f;
#pragma unroll
    for (int i = 0; i < 12; ++i) {
      float d = f[i] - mean;
      q += d * d;
    }
#pragma unroll
    for (int o = 32; o > 0; o >>= 1) q += __shfl_xor(q, o);
    const float mb = rbf(mean);
    const float den = rbf(sqrtf(rbf(rbf(q * (1.f / 768.f)) + 1e-5f)));
    u16* op = h + (size_t)(row0 + r) * 768 + l * 4;
#pragma unroll
    for (int i = 0; i < 3; ++i) {
      float y[4];
#pragma unroll
      for (int j = 0; j < 4; ++j) y[j] = rbf(rbf(rbf(f[4 * i + j] - mb) / den) * gg[i][j]) + tt[i][j];
      u32x2 o;
      o[0] = pack2(y[0], y[1]);
      o[1] = pack2(y[2], y[3]);
      *(u32x2*)(op + i * 256) = o;
    }
  }
}

template <int BN, int NS, int EPI>
__global__ __launch_bounds__(768, 3) void k_gemm_big(const u16* __restrict__ X, u16* __restrict__ A,
                                                     const float* __restrict__ modp, unsigned* __restrict__ cnt,
                                                     const u16* __restrict__ W, void* __restrict__ outv,
                                                     u16* __restrict__ out2) {
  constexpr int NC = BN / 16;
  constexpr int LDW = (EPI == 0) ? 2304 : (EPI == 1 ? 3072 : 768);
  constexpr int LDA = (EPI == 2) ? 3072 : 768;
  constexpr int ASZ = 128 * 128;
  constexpr int BSZ = 8 * NC * 256;
  constexpr int STG = ASZ + BSZ;
  constexpr int SUBW = 2 * NC;
  constexpr int NBI = (SUBW + 3) / 4;
  constexpr int NLD = 4 + NBI;
  constexpr int NK = 768 / 64;
  __shared__ __attribute__((aligned(16))) char smem_raw[NS * STG];
  LDS_AS char* smem = (LDS_AS char*)smem_raw;
  const unsigned lds0 = (unsigned)(uintptr_t)smem;
  constexpr int KID = (EPI == 0) ? 1 : (EPI == 1 ? 6 : 5);
  STAMP(KID, 0);
  const int tid = threadIdx.x;
  const int l = tid & 63;
  const int w = uni(tid >> 6);
  const int bid = blockIdx.x;
  const int xcd = bid & 7, idx = bid >> 3;
  int tm, tn, ks = 0;
  if constexpr (EPI == 2) {
    tm = (xcd >> 1) * 4 + (idx & 3);
    tn = (idx >> 2) & 3;
    ks = (xcd & 1) * 2 + (idx >> 4);
  } else {
    tm = (xcd >> 1) * 4 + (idx & 3);
    tn = (xcd & 1) * 8 + (idx >> 2);
  }
  const int m0 = tm * 128, n0 = tn * BN;

  if (w >= 8) {
    const int lw = w - 8;
    const unsigned voffA = (unsigned)((l >> 3) * LDA * 2 + (((l & 7) ^ (l >> 3)) << 4));
    unsigned voffB[NBI];
#pragma unroll
    for (int i = 0; i < NBI; ++i) {
      int sub = SUBW * lw + 4 * i + (l >> 4);
      if (sub >= SUBW * (lw + 1)) sub = SUBW * (lw + 1) - 1;
      int kb = sub / NC, cc = sub % NC;
      int within = l & 15;
      int r = (within >> 1) ^ ((kb & 1) << 2);
      voffB[i] = (unsigned)(((kb * 8 + r) * LDW + cc * 16 + (within & 1) * 8) * 2);
    }
    const char* abase = (const char*)(A + (size_t)(m0 + 32 * lw) * LDA + ks * 768);
    const char* bbase = (const char*)(W + (size_t)ks * 768 * LDW + n0);
    auto issueA = [&](int kt, int st) {
      const unsigned sa = lds0 + st * STG;
#pragma unroll
      for (int i = 0; i < 4; ++i)
        glds16(abase + (size_t)i * 8 * LDA * 2 + kt * 128, voffA, sa + (32 * lw + 8 * i) * 128);
    };
    auto issueB = [&](int kt, int st) {
      const unsigned sa = lds0 + st * STG;
      const char* sb = bbase + (size_t)kt * 64 * LDW * 2;
#pragma unroll
      for (int i = 0; i < NBI; ++i) {
        unsigned dst = sa + ASZ + (SUBW * lw + 4 * i) * 256;
        if (4 * i + 4 <= SUBW) {
          glds16(sb, voffB[i], dst);
        } else {
          if (l < (SUBW - 4 * i) * 16) glds16(sb, voffB[i], dst);
        }
      }
    };
#pragma unroll
    for (int s = 0; s < NS - 1; ++s) issueB(s, s);
    {
      const unsigned* cp = cnt + tm * 32;
      for (int spin = 0; spin < (1 << 22); ++spin) {
        unsigned v = __hip_atomic_load(cp, __ATOMIC_RELAXED, __HIP_MEMORY_SCOPE_AGENT);
        if (uni((int)v) >= 128) break;
        __builtin_amdgcn_s_sleep(2);
      }
      __builtin_amdgcn_fence(__ATOMIC_ACQUIRE, "agent");
    }
#pragma unroll
    for (int s = 0; s < NS - 1; ++s) issueA(s, s);
    int stn = NS - 1;
    static_for<0, NS - 1>([&](auto KT) {
      constexpr int kt = KT.value;
      wait_vm<4 * (NS - 2 - kt) + kt * NLD>();
      bar();
      issueA(kt + NS - 1, stn);
      issueB(kt + NS - 1, stn);
      stn = (stn + 1 == NS) ? 0 : stn + 1;
    });
#pragma unroll 1
    for (int kt = NS - 1; kt < NK; ++kt) {
      if (kt < NK - (NS - 2)) wait_vm<(NS - 2) * NLD>();
      else wait_vm<0>();
      bar();
      if (kt + NS - 1 < NK) {
        issueA(kt + NS - 1, stn);
        issueB(kt + NS - 1, stn);
      }
      stn = (stn + 1 == NS) ? 0 : stn + 1;
    }
    bar();
    bar();
    return;
  }

  {
    const int row = m0 + 8 * tn + w;
    const int b = m0 >> 10;
    constexpr int offS = (EPI == 0) ? 0 : 3 * 768;
    const u16* xp = X + (size_t)row * 768 + l * 4;
    u32x2 v[3];
#pragma unroll
    for (int i = 0; i < 3; ++i) v[i] = *(const u32x2*)(xp + i * 256);
    f32x4 gg[3], tt[3];
#pragma unroll
    for (int i = 0; i < 3; ++i) {
      const float* m0p = modp + (0 * 2 + b) * 4608 + offS + l * 4 + i * 256;
      const float* m1p = modp + (1 * 2 + b) * 4608 + offS + l * 4 + i * 256;
      f32x4 s0 = *(const f32x4*)m0p, s1 = *(const f32x4*)m1p;
      f32x4 c0 = *(const f32x4*)(m0p + 768), c1 = *(const f32x4*)(m1p + 768);
#pragma unroll
      for (int j = 0; j < 4; ++j) {
        tt[i][j] = rbf(s0[j] + s1[j]);
        gg[i][j] = rbf(1.f + rbf(c0[j] + c1[j]));
      }
    }
    float f[12];
#pragma unroll
    for (int i = 0; i < 3; ++i) {
      f[4 * i] = lo_f(v[i][0]);
      f[4 * i + 1] = hi_f(v[i][0]);
      f[4 * i + 2] = lo_f(v[i][1]);
      f[4 * i + 3] = hi_f(v[i][1]);
    }
    float s = 0.f;
#pragma unroll
    for (int i = 0; i < 12; ++i) s += f[i];
#pragma unroll
    for (int o = 32; o > 0; o >>= 1) s += __shfl_xor(s, o);
    float mean = s * (1.f / 768.f);
    float qq = 0.f;
#pragma unroll
    for (int i = 0; i < 12; ++i) {
      float d = f[i] - mean;
      qq += d * d;
    }
#pragma unroll
    for (int o = 32; o > 0; o >>= 1) qq += __shfl_xor(qq, o);
    const float mb = rbf(mean);
    const float den = rbf(sqrtf(rbf(rbf(qq * (1.f / 768.f)) + 1e-5f)));
    u16* op = A + (size_t)row * 768 + l * 4;
#pragma unroll
    for (int i = 0; i < 3; ++i) {
      float y[4];
#pragma unroll
      for (int j = 0; j < 4; ++j) y[j] = rbf(rbf(rbf(f[4 * i + j] - mb) / den) * gg[i][j]) + tt[i][j];
      u32x2 o;
      o[0] = pack2(y[0], y[1]);
      o[1] = pack2(y[2], y[3]);
      st8_dev(op + i * 256, o);
    }
    wait_vm<0>();
    if (l == 0) __hip_atomic_fetch_add(cnt + tm * 32, 1u, __ATOMIC_RELAXED, __HIP_MEMORY_SCOPE_AGENT);
  }
  const int wm = w & 3, kh = w >> 2;
  const int g = l >> 4;
  const unsigned aoff = (unsigned)(((32 * wm + (l & 15)) * 128 + ((g ^ (l & 7)) << 4)) ^ (kh * 64));
  const int q = (l & 15) >> 2, p = l & 3;
  const unsigned boff_t0 = (unsigned)(ASZ + (4 * kh + g) * NC * 256 + ((4 * (g & 1) + q) * 32) + p * 8);
  const unsigned boff_t1 = (unsigned)(ASZ + (4 * kh + g) * NC * 256 + ((4 * (1 - (g & 1)) + q) * 32) + p * 8);

  f32x4 acc[2][NC];
#pragma unroll
  for (int i = 0; i < 2; ++i)
#pragma unroll
    for (int j = 0; j < NC; ++j) acc[i][j] = (f32x4){0.f, 0.f, 0.f, 0.f};
  STAMP(KID, 1);

  int st = 0;
#pragma unroll 1
  for (int kt = 0; kt < NK; ++kt) {
    wait_lgkm0();
    bar();
    const LDS_AS char* sp = smem + st * STG;
    bf16x8 xb[2];
#pragma unroll
    for (int mf = 0; mf < 2; ++mf) xb[mf] = lds_read16(sp + aoff + mf * 2048);
#pragma unroll
    for (int cc = 0; cc < NC; ++cc) {
      bf16x4 w0 = lds_tr(sp + boff_t0 + cc * 256);
      bf16x4 w1 = lds_tr(sp + boff_t1 + cc * 256);
      bf16x8 wf = cat4(w0, w1);
      acc[0][cc] = mfma16(wf, xb[0], acc[0][cc]);
      acc[1][cc] = mfma16(wf, xb[1], acc[1][cc]);
    }
    st = (st + 1 == NS) ? 0 : st + 1;
  }
  STAMP(KID, 14);

  wait_lgkm0();
  bar();
  {
    LDS_AS char* rp = smem + (wm * 2 + kh) * NC * 1024 + l * 16;
    if (kh) {
#pragma unroll
      for (int cc = 0; cc < NC; ++cc) {
        *(LDS_AS f32x4*)(rp + cc * 1024) = acc[0][cc];
        acc[0][cc] = acc[1][cc];
      }
    } else {
#pragma unroll
      for (int cc = 0; cc < NC; ++cc) *(LDS_AS f32x4*)(rp + cc * 1024) = acc[1][cc];
    }
  }
  wait_lgkm0();
  bar();
  const int row = m0 + 32 * wm + 16 * kh + (l & 15);
  const LDS_AS char* rp = smem + (wm * 2 + (1 - kh)) * NC * 1024 + l * 16;
  if constexpr (EPI == 2) {
    float* op = (float*)outv + ((size_t)ks * 2048 + row) * 768 + n0 + 4 * g;
#pragma unroll
    for (int cc = 0; cc < NC; ++cc) *(f32x4*)(op + 16 * cc) = acc[0][cc] + lds_read16f(rp + cc * 1024);
  } else {
    u16* out = (u16*)outv;
    unsigned pk[NC][2];
#pragma unroll
    for (int cc = 0; cc < NC; ++cc) {
      f32x4 v = acc[0][cc] + lds_read16f(rp + cc * 1024);
      if constexpr (EPI == 1) {
        const float c1 = -2.302208198144325f;
        const float c2 = -0.1029432397500235f;
        unsigned y01 = pack2(v[0], v[1]), y23 = pack2(v[2], v[3]);
        float y[4] = {lo_f(y01), hi_f(y01), lo_f(y23), hi_f(y23)};
#pragma unroll
        for (int j = 0; j < 4; ++j) {
          float e = __builtin_amdgcn_exp2f(y[j] * (c1 + c2 * y[j] * y[j]));
          v[j] = y[j] * __builtin_amdgcn_rcpf(1.f + e);
        }
      }
      pk[cc][0] = pack2(v[0], v[1]);
      pk[cc][1] = pack2(v[2], v[3]);
    }
    auto dst_ptr = [&](int n) -> u16* {
      if constexpr (EPI == 1) {
        return out + (size_t)row * 3072 + n;
      } else {
        if (n < 1536) return out + (size_t)row * 1536 + n;
        int nv = n - 1536;
        int hd = nv / 96;
        int dd = nv - hd * 96;
        return out2 + ((size_t)(((row >> 10) * 8 + hd) * 1024 + (row & 1023))) * 96 + dd;
      }
    };
#pragma unroll
    for (int a = 0; a < NC / 2; ++a) {
      u32x4 o = pair_pack(pk[2 * a][0], pk[2 * a][1], pk[2 * a + 1][0], pk[2 * a + 1][1]);
      *(u32x4*)dst_ptr(n0 + 32 * a + (g & 1) * 16 + (g & 2) * 4) = o;
    }
    if constexpr (NC & 1) {
      u32x2 o;
      o[0] = pk[NC - 1][0];
      o[1] = pk[NC - 1][1];
      *(u32x2*)dst_ptr(n0 + 16 * (NC - 1) + 4 * g) = o;
    }
  }
  STAMP(KID, 15);
}

__global__ __launch_bounds__(256) void k_final(const float* __restrict__ part, const float* __restrict__ modp,
                                               const u16* __restrict__ x1, u16* __restrict__ out) {
  const int t = blockIdx.x * 256 + threadIdx.x;
  const int row = t / 96, c8 = (t - row * 96) * 8;
  const int b = row >> 10;
  const size_t e = (size_t)row * 768 + c8;
  f32x4 p[4][2];
#pragma unroll
  for (int k = 0; k < 4; ++k) {
    p[k][0] = *(const f32x4*)(part + (size_t)k * 2048 * 768 + e);
    p[k][1] = *(const f32x4*)(part + (size_t)k * 2048 * 768 + e + 4);
  }
  u32x4 xr = *(const u32x4*)(x1 + e);
  const float* mp0 = modp + (0 * 2 + b) * 4608 + 5 * 768 + c8;
  const float* mp1 = modp + (1 * 2 + b) * 4608 + 5 * 768 + c8;
  f32x4 ga[2] = {*(const f32x4*)mp0, *(const f32x4*)(mp0 + 4)};
  f32x4 gb[2] = {*(const f32x4*)mp1, *(const f32x4*)(mp1 + 4)};
  float v[8];
#pragma unroll
  for (int hh = 0; hh < 2; ++hh)
#pragma unroll
    for (int j = 0; j < 4; ++j) {
      float m = (p[0][hh][j] + p[1][hh][j]) + (p[2][hh][j] + p[3][hh][j]);
      float gate = rbf(ga[hh][j] + gb[hh][j]);
      unsigned xw = xr[hh * 2 + (j >> 1)];
      float xv = (j & 1) ? hi_f(xw) : lo_f(xw);
      v[hh * 4 + j] = xv + rbf(gate * rbf(m));
    }
  u32x4 o;
#pragma unroll
  for (int j = 0; j < 4; ++j) o[j] = pack2(v[2 * j], v[2 * j + 1]);
  *(u32x4*)(out + e) = o;
}

__global__ __launch_bounds__(768, 3) void k_scores(const u16* __restrict__ qk, u16* __restrict__ P,
                                                   f32x2* __restrict__ ml) {
  constexpr int NS = 4;
  constexpr int ASZ = 128 * 128;
  constexpr int STG = 2 * ASZ;
  constexpr int NK = 384 / 64;
  constexpr int NLD = 8;
  constexpr int LDQ = 1536;
  __shared__ __attribute__((aligned(16))) char smem_raw[NS * STG];
  LDS_AS char* smem = (LDS_AS char*)smem_raw;
  const unsigned lds0 = (unsigned)(uintptr_t)smem;
  STAMP(2, 0);
  const int tid = threadIdx.x, l = tid & 63;
  const int w = uni(tid >> 6);
  const int bid = blockIdx.x;
  const int xcd = bid & 7, idx = bid >> 3;
  const int bh = xcd >> 1;
  const int tq = (xcd & 1) * 4 + (idx & 3);
  const int tk = idx >> 2;
  const int b = bh >> 1, h = bh & 1;

  if (w >= 8) {
    const int lw = w - 8;
    const unsigned voff = (unsigned)((l >> 3) * LDQ * 2 + (((l & 7) ^ (l >> 3)) << 4));
    const char* qbase = (const char*)(qk + (size_t)(b * 1024 + tq * 128 + 32 * lw) * LDQ + h * 384);
    const char* kbase = (const char*)(qk + (size_t)(b * 1024 + tk * 128 + 32 * lw) * LDQ + 768 + h * 384);
    auto issue = [&](int kt, int st) {
      const unsigned sa = lds0 + st * STG;
#pragma unroll
      for (int i = 0; i < 4; ++i)
        glds16(qbase + (size_t)i * 8 * LDQ * 2 + kt * 128, voff, sa + (32 * lw + 8 * i) * 128);
#pragma unroll
      for (int i = 0; i < 4; ++i)
        glds16(kbase + (size_t)i * 8 * LDQ * 2 + kt * 128, voff, sa + ASZ + (32 * lw + 8 * i) * 128);
    };
#pragma unroll
    for (int s = 0; s < NS - 1; ++s) issue(s, s);
    int stn = NS - 1;
#pragma unroll 1
    for (int kt = 0; kt < NK; ++kt) {
      if (kt < NK - (NS - 2)) wait_vm<(NS - 2) * NLD>();
      else wait_vm<0>();
      bar();
      if (kt + NS - 1 < NK) issue(kt + NS - 1, stn);
      stn = (stn + 1 == NS) ? 0 : stn + 1;
    }
    bar();
    bar();
    return;
  }

  const int wm = w & 3, kh = w >> 2;
  const int g = l >> 4;
  const unsigned qoff = (unsigned)(((32 * wm + (l & 15)) * 128 + ((g ^ (l & 7)) << 4)) ^ (kh * 64));
  const unsigned koff = (unsigned)(ASZ + (((l & 15) * 128 + ((g ^ (l & 7)) << 4)) ^ (kh * 64)));
  f32x4 acc[2][8];
#pragma unroll
  for (int i = 0; i < 2; ++i)
#pragma unroll
    for (int j = 0; j < 8; ++j) acc[i][j] = (f32x4){0.f, 0.f, 0.f, 0.f};

  int st = 0;
#pragma unroll 1
  for (int kt = 0; kt < NK; ++kt) {
    wait_lgkm0();
    bar();
    const LDS_AS char* sp = smem + st * STG;
    bf16x8 qf[2];
#pragma unroll
    for (int mf = 0; mf < 2; ++mf) qf[mf] = lds_read16(sp + qoff + mf * 2048);
#pragma unroll
    for (int kf = 0; kf < 8; ++kf) {
      bf16x8 kfr = lds_read16(sp + koff + kf * 2048);
      acc[0][kf] = mfma16(kfr, qf[0], acc[0][kf]);
      acc[1][kf] = mfma16(kfr, qf[1], acc[1][kf]);
    }
    st = (st + 1 == NS) ? 0 : st + 1;
  }
  STAMP(2, 14);
  wait_lgkm0();
  bar();
  {
    LDS_AS char* rp = smem + (wm * 2 + kh) * 8 * 1024 + l * 16;
    if (kh) {
#pragma unroll
      for (int kf = 0; kf < 8; ++kf) {
        *(LDS_AS f32x4*)(rp + kf * 1024) = acc[0][kf];
        acc[0][kf] = acc[1][kf];
      }
    } else {
#pragma unroll
      for (int kf = 0; kf < 8; ++kf) *(LDS_AS f32x4*)(rp + kf * 1024) = acc[1][kf];
    }
  }
  wait_lgkm0();
  bar();
  f32x4 s[8];
  {
    const LDS_AS char* rp = smem + (wm * 2 + (1 - kh)) * 8 * 1024 + l * 16;
#pragma unroll
    for (int kf = 0; kf < 8; ++kf) {
      f32x4 v = acc[0][kf] + lds_read16f(rp + kf * 1024);
      unsigned a01 = pack2(v[0], v[1]), a23 = pack2(v[2], v[3]);
      a01 = pack2(lo_f(a01) * 0.051031036307982884f, hi_f(a01) * 0.051031036307982884f);
      a23 = pack2(lo_f(a23) * 0.051031036307982884f, hi_f(a23) * 0.051031036307982884f);
      s[kf] = (f32x4){lo_f(a01), hi_f(a01), lo_f(a23), hi_f(a23)};
    }
  }
  const float l2e = 1.4426950408889634f;
  float mx = -INFINITY;
#pragma unroll
  for (int kf = 0; kf < 8; ++kf)
#pragma unroll
    for (int j = 0; j < 4; ++j) mx = fmaxf(mx, s[kf][j]);
  mx = fmaxf(mx, __shfl_xor(mx, 16));
  mx = fmaxf(mx, __shfl_xor(mx, 32));
  const float mbl = mx * l2e;
  float sum = 0.f;
  unsigned pk[8][2];
#pragma unroll
  for (int kf = 0; kf < 8; ++kf) {
    float e[4];
#pragma unroll
    for (int j = 0; j < 4; ++j) {
      e[j] = __builtin_amdgcn_exp2f(s[kf][j] * l2e - mbl);
      sum += e[j];
    }
    pk[kf][0] = pack2(e[0], e[1]);
    pk[kf][1] = pack2(e[2], e[3]);
  }
  sum += __shfl_xor(sum, 16);
  sum += __shfl_xor(sum, 32);
  const int qrow = tq * 128 + 32 * wm + 16 * kh + (l & 15);
  u16* op = P + ((size_t)bh * 1024 + qrow) * 1024 + tk * 128;
#pragma unroll
  for (int a = 0; a < 4; ++a) {
    u32x4 o = pair_pack(pk[2 * a][0], pk[2 * a][1], pk[2 * a + 1][0], pk[2 * a + 1][1]);
    *(u32x4*)(op + 32 * a + (g & 1) * 16 + (g & 2) * 4) = o;
  }
  if (g == 0) {
    f32x2 s2;
    s2[0] = mbl;
    s2[1] = sum;
    ml[((size_t)bh * 1024 + qrow) * 8 + tk] = s2;
  }
  STAMP(2, 15);
}

template <int MODE>
__global__ __launch_bounds__(512, 2) void k_gemm_small(const u16* __restrict__ Ain, const u16* __restrict__ Bin,
                                                       const float* __restrict__ modp,
                                                       const u16* __restrict__ resid,
                                                       const f32x2* __restrict__ ml, f32x2* __restrict__ stats,
                                                       u16* __restrict__ out) {
  constexpr int KTOT = (MODE == 0) ? 1024 : (MODE == 1 ? 768 : 3072);
  constexpr int KH = KTOT / 2;
  constexpr int NK = KH / 64;
  constexpr int LDA = KTOT;
  constexpr int LDB = (MODE == 0) ? 96 : 768;
  constexpr int NS = 4;
  constexpr int STG = 40960;
  constexpr int NLD = 10;
  __shared__ __attribute__((aligned(16))) char smem_raw[NS * STG];
  LDS_AS char* smem = (LDS_AS char*)smem_raw;
  const unsigned lds0 = (unsigned)(uintptr_t)smem;
  STAMP(3 + MODE, 0);
  const int tid = threadIdx.x, l = tid & 63;
  const int w = uni(tid >> 6);
  const int bid = blockIdx.x;
  const int xcd = bid & 7, idx = bid >> 3;
  const int g = l >> 4;

  int m0, n0, bh = 0;
  const u16* Ap;
  const u16* Bp;
  if constexpr (MODE == 0) {
    bh = xcd >> 1;
    const int tq = (xcd & 1) * 8 + (idx & 7);
    const int td = idx >> 3;
    m0 = tq * 64;
    n0 = td * 96;
    Ap = Ain + ((size_t)bh * 1024 + m0) * 1024;
    Bp = Bin + ((size_t)(bh * 4 + td) * 1024) * 96;
  } else {
    const int tm = xcd * 4 + (idx & 3);
    const int tn = idx >> 2;
    m0 = tm * 64;
    n0 = tn * 96;
    Ap = Ain + (size_t)m0 * LDA;
    Bp = Bin + n0;
  }

  if (w >= 4) {
    const int lw = w - 4;
    const int kq = lw & 1, part = lw >> 1;
    const unsigned voffA = (unsigned)((l >> 3) * LDA * 2 + (((l & 7) ^ (l >> 3)) << 4));
    unsigned voffB[6];
#pragma unroll
    for (int i = 0; i < 6; ++i) {
      int sub = 24 * part + 4 * i + (l >> 4);
      int kb = sub / 6, cc = sub % 6;
      int within = l & 15;
      int r = (within >> 1) ^ ((kb & 1) << 2);
      voffB[i] = (unsigned)(((kb * 8 + r) * LDB + cc * 16 + (within & 1) * 8) * 2);
    }
    const char* abase = (const char*)(Ap + (size_t)(32 * part) * LDA + kq * KH);
    const char* bbase = (const char*)(Bp + (size_t)(kq * KH) * LDB);
    auto issue = [&](int kt, int st) {
      const unsigned sa = lds0 + st * STG;
#pragma unroll
      for (int i = 0; i < 4; ++i)
        glds16(abase + (size_t)i * 8 * LDA * 2 + kt * 128, voffA, sa + kq * 8192 + (32 * part + 8 * i) * 128);
      const char* sb = bbase + (size_t)kt * 64 * LDB * 2;
#pragma unroll
      for (int i = 0; i < 6; ++i) glds16(sb, voffB[i], sa + 16384 + kq * 12288 + (24 * part + 4 * i) * 256);
    };
#pragma unroll
    for (int s = 0; s < NS - 1; ++s) issue(s, s);
    int stn = NS - 1;
#pragma unroll 1
    for (int kt = 0; kt < NK; ++kt) {
      if (kt < NK - (NS - 2)) wait_vm<(NS - 2) * NLD>();
      else wait_vm<0>();
      bar();
      if (kt + NS - 1 < NK) issue(kt + NS - 1, stn);
      stn = (stn + 1 == NS) ? 0 : stn + 1;
    }
    bar();
    bar();
    return;
  }

  const int kq = w & 1, nh = w >> 1;
  float cf[4][4];
  if constexpr (MODE == 0) {
#pragma unroll
    for (int mf = 0; mf < 4; ++mf) {
      const f32x4* sp = (const f32x4*)(ml + ((size_t)bh * 1024 + m0 + 16 * mf + (l & 15)) * 8);
      f32x4 sv[4];
#pragma unroll
      for (int j = 0; j < 4; ++j) sv[j] = sp[j];
      float mx = fmaxf(sv[0][0], sv[0][2]);
#pragma unroll
      for (int j = 1; j < 4; ++j) mx = fmaxf(mx, fmaxf(sv[j][0], sv[j][2]));
      float e[8];
      float lsum = 0.f;
#pragma unroll
      for (int j = 0; j < 4; ++j) {
        e[2 * j] = __builtin_amdgcn_exp2f(sv[j][0] - mx);
        e[2 * j + 1] = __builtin_amdgcn_exp2f(sv[j][2] - mx);
        lsum += e[2 * j] * sv[j][1] + e[2 * j + 1] * sv[j][3];
      }
      float inv = 1.f / lsum;
      if (kq) {
#pragma unroll
        for (int j = 0; j < 4; ++j) cf[mf][j] = e[4 + j] * inv;
      } else {
#pragma unroll
        for (int j = 0; j < 4; ++j) cf[mf][j] = e[j] * inv;
      }
    }
  }
  f32x4 gq[2][3][2];
  u32x2 xr[2][3];
  if constexpr (MODE != 0) {
    const int goff = (MODE == 1) ? 2 * 768 : 5 * 768;
    const int b = m0 >> 10;
    const float* mp0 = modp + (0 * 2 + b) * 4608 + goff + n0 + 48 * nh + 4 * g;
    const float* mp1 = modp + (1 * 2 + b) * 4608 + goff + n0 + 48 * nh + 4 * g;
#pragma unroll
    for (int cc = 0; cc < 3; ++cc) {
      gq[0][cc][0] = *(const f32x4*)(mp0 + 16 * cc);
      gq[0][cc][1] = *(const f32x4*)(mp1 + 16 * cc);
    }
#pragma unroll
    for (int mf = 0; mf < 2; ++mf) {
      const u16* rp = resid + (size_t)(m0 + 32 * kq + 16 * mf + (l & 15)) * 768 + n0 + 48 * nh + 4 * g;
#pragma unroll
      for (int cc = 0; cc < 3; ++cc) xr[mf][cc] = *(const u32x2*)(rp + 16 * cc);
    }
  }

  const unsigned aoff0 = (unsigned)(kq * 8192 + (l & 15) * 128 + ((g ^ (l & 7)) << 4));
  const int q = (l & 15) >> 2, p = l & 3;
  const unsigned boff_t0 = (unsigned)(16384 + kq * 12288 + (g * 6 + 3 * nh) * 256 + ((4 * (g & 1) + q) * 32) + p * 8);
  const unsigned boff_t1 =
      (unsigned)(16384 + kq * 12288 + (g * 6 + 3 * nh) * 256 + ((4 * (1 - (g & 1)) + q) * 32) + p * 8);

  f32x4 acc[4][3];
  f32x4 tot[4][3];
#pragma unroll
  for (int i = 0; i < 4; ++i)
#pragma unroll
    for (int j = 0; j < 3; ++j) {
      acc[i][j] = (f32x4){0.f, 0.f, 0.f, 0.f};
      tot[i][j] = (f32x4){0.f, 0.f, 0.f, 0.f};
    }
  STAMP(3 + MODE, 1);

  int st = 0;
#pragma unroll 1
  for (int kt = 0; kt < NK; ++kt) {
    wait_lgkm0();
    bar();
    const LDS_AS char* sp = smem + st * STG;
#pragma unroll
    for (int ks = 0; ks < 2; ++ks) {
      bf16x8 af[4];
#pragma unroll
      for (int mf = 0; mf < 4; ++mf) af[mf] = lds_read16(sp + ((aoff0 ^ (ks * 64)) + mf * 2048));
#pragma unroll
      for (int cc = 0; cc < 3; ++cc) {
        bf16x4 w0 = lds_tr(sp + boff_t0 + ks * 6144 + cc * 256);
        bf16x4 w1 = lds_tr(sp + boff_t1 + ks * 6144 + cc * 256);
        bf16x8 wf = cat4(w0, w1);
#pragma unroll
        for (int mf = 0; mf < 4; ++mf) acc[mf][cc] = mfma16(wf, af[mf], acc[mf][cc]);
      }
    }
    if constexpr (MODE == 0) {
      if (kt & 1) {
#pragma unroll
        for (int mf = 0; mf < 4; ++mf) {
          const float c = cf[mf][0];
          cf[mf][0] = cf[mf][1];
          cf[mf][1] = cf[mf][2];
          cf[mf][2] = cf[mf][3];
#pragma unroll
          for (int cc = 0; cc < 3; ++cc) {
            tot[mf][cc] += acc[mf][cc] * c;
            acc[mf][cc] = (f32x4){0.f, 0.f, 0.f, 0.f};
          }
        }
      }
    }
    st = (st + 1 == NS) ? 0 : st + 1;
  }
  if constexpr (MODE == 0) {
#pragma unroll
    for (int mf = 0; mf < 4; ++mf)
#pragma unroll
      for (int cc = 0; cc < 3; ++cc) acc[mf][cc] = tot[mf][cc];
  }
  STAMP(3 + MODE, 12);
  wait_lgkm0();
  bar();
  {
    LDS_AS char* rp = smem + (nh * 2 + kq) * 6144 + l * 16;
    if (kq) {
#pragma unroll
      for (int mf = 0; mf < 2; ++mf)
#pragma unroll
        for (int cc = 0; cc < 3; ++cc) {
          *(LDS_AS f32x4*)(rp + (mf * 3 + cc) * 1024) = acc[mf][cc];
          acc[mf][cc] = acc[2 + mf][cc];
        }
    } else {
#pragma unroll
      for (int mf = 0; mf < 2; ++mf)
#pragma unroll
        for (int cc = 0; cc < 3; ++cc) *(LDS_AS f32x4*)(rp + (mf * 3 + cc) * 1024) = acc[2 + mf][cc];
    }
  }
  wait_lgkm0();
  bar();
  f32x4 r[2][3];
  {
    const LDS_AS char* rp = smem + (nh * 2 + (1 - kq)) * 6144 + l * 16;
#pragma unroll
    for (int mf = 0; mf < 2; ++mf)
#pragma unroll
      for (int cc = 0; cc < 3; ++cc)
        r[mf][cc] = acc[mf][cc] + lds_read16f(rp + (mf * 3 + cc) * 1024);
  }
  STAMP(3 + MODE, 13);
#pragma unroll
  for (int mf = 0; mf < 2; ++mf) {
    const int mrow = m0 + 32 * kq + 16 * mf + (l & 15);
    float v[3][4];
    u16* op;
    if constexpr (MODE == 0) {
      op = out + (size_t)((bh >> 1) * 1024 + mrow) * 768 + (bh & 1) * 384 + n0 + 48 * nh;
#pragma unroll
      for (int cc = 0; cc < 3; ++cc)
#pragma unroll
        for (int j = 0; j < 4; ++j) v[cc][j] = r[mf][cc][j];
    } else {
      op = out + (size_t)mrow * 768 + n0 + 48 * nh;
#pragma unroll
      for (int cc = 0; cc < 3; ++cc) {
        float xv[4] = {lo_f(xr[mf][cc][0]), hi_f(xr[mf][cc][0]), lo_f(xr[mf][cc][1]), hi_f(xr[mf][cc][1])};
#pragma unroll
        for (int j = 0; j < 4; ++j) {
          float gate = rbf(gq[0][cc][0][j] + gq[0][cc][1][j]);
          v[cc][j] = rbf(xv[j] + rbf(gate * rbf(r[mf][cc][j])));
        }
      }
    }
    {
      u32x4 o = pair_pack(pack2(v[0][0], v[0][1]), pack2(v[0][2], v[0][3]), pack2(v[1][0], v[1][1]),
                          pack2(v[1][2], v[1][3]));
      *(u32x4*)(op + (g & 1) * 16 + (g & 2) * 4) = o;
      u32x2 o2;
      o2[0] = pack2(v[2][0], v[2][1]);
      o2[1] = pack2(v[2][2], v[2][3]);
      *(u32x2*)(op + 32 + 4 * g) = o2;
    }
    if constexpr (MODE == 1) {
      float s = 0.f;
#pragma unroll
      for (int cc = 0; cc < 3; ++cc)
#pragma unroll
        for (int j = 0; j < 4; ++j) s += v[cc][j];
      s += __shfl_xor(s, 16);
      s += __shfl_xor(s, 32);
      float mean = s * (1.f / 48.f);
      float m2 = 0.f;
#pragma unroll
      for (int cc = 0; cc < 3; ++cc)
#pragma unroll
        for (int j = 0; j < 4; ++j) {
          float d = v[cc][j] - mean;
          m2 += d * d;
        }
      m2 += __shfl_xor(m2, 16);
      m2 += __shfl_xor(m2, 32);
      if (g == 0) {
        f32x2 s2;
        s2[0] = s;
        s2[1] = m2;
        stats[(size_t)mrow * 16 + (n0 / 48) + nh] = s2;
      }
    }
  }
  STAMP(3 + MODE, 15);
}

extern "C" void kernel_launch(void* const* d_in, const int* in_sizes, int n_in, void* d_out, int out_size,
                              void* d_ws, size_t ws_size, hipStream_t stream) {
  const u16* x = (const u16*)d_in[0];
  const u16* c = (const u16*)d_in[1];
  const u16* wqkv = (const u16*)d_in[2];
  const u16* wo = (const u16*)d_in[3];
  const u16* wmod = (const u16*)d_in[4];
  const u16* wfc1 = (const u16*)d_in[5];
  const u16* wfc2 = (const u16*)d_in[6];
  u16* out = (u16*)d_out;

  char* ws = (char*)d_ws;
  size_t off = 0;
  auto alloc = [&](size_t bytes) {
    char* p = ws + off;
    off += (bytes + 255) & ~(size_t)255;
    return p;
  };
  float* modp = (float*)alloc(2 * 2 * 4608 * 4);
  unsigned* cnt = (unsigned*)alloc(64 * 128);
  u16* h = (u16*)alloc((size_t)Mrows * 768 * 2);
  u16* qk = (u16*)alloc((size_t)Mrows * 1536 * 2);
  u16* Vt = (u16*)alloc((size_t)Mrows * 768 * 2);
  u16* P = (u16*)alloc((size_t)4 * 1024 * 1024 * 2);
  f32x2* ml = (f32x2*)alloc((size_t)4 * 1024 * 8 * 8);
  u16* O = (u16*)alloc((size_t)Mrows * 768 * 2);
  u16* x1 = (u16*)alloc((size_t)Mrows * 768 * 2);
  f32x2* st2 = (f32x2*)alloc((size_t)Mrows * 16 * 8);
  u16* h2 = (u16*)alloc((size_t)Mrows * 768 * 2);
  u16* u = (u16*)alloc((size_t)Mrows * 3072 * 2);

  k0_mod<<<144 + 288, 256, 0, stream>>>(c, wmod, modp, wqkv, wo, wfc1, wfc2, cnt);
  k_gemm_big<144, 4, 0><<<256, 768, 0, stream>>>(x, h, modp, cnt, wqkv, qk, Vt);
  k_scores<<<256, 768, 0, stream>>>(qk, P, ml);
  k_gemm_small<0><<<256, 512, 0, stream>>>(P, Vt, modp, nullptr, ml, nullptr, O);
  k_gemm_small<1><<<256, 512, 0, stream>>>(O, wo, modp, x, nullptr, st2, x1);
  k_gemm_big<192, 4, 1><<<256, 768, 0, stream>>>(x1, h2, modp, cnt + 16 * 32, wfc1, u, nullptr);
  k_gemm_small<2><<<256, 512, 0, stream>>>(u, wfc2, modp, x1, nullptr, nullptr, out);
}
```

```cpp
#include <hip/hip_runtime.h>
#include <hip/hip_bf16.h>
#include <stdint.h>
#include <type_traits>

typedef __attribute__((ext_vector_type(8))) __bf16 bf16x8;
typedef __attribute__((ext_vector_type(4))) __bf16 bf16x4;
typedef __attribute__((ext_vector_type(2))) __bf16 bf16x2;
typedef __attribute__((ext_vector_type(4))) float f32x4;
typedef __attribute__((ext_vector_type(2))) float f32x2;
typedef __attribute__((ext_vector_type(4))) unsigned u32x4;
typedef __attribute__((ext_vector_type(2))) unsigned u32x2;
typedef unsigned short u16;
#define LDS_AS __attribute__((address_space(3)))

#define DEVI __device__ __forceinline__
#define STAMP(K, I)

static constexpr int Bsz = 2, Nseq = 1024, Dm = 768, Ff = 3072, Dh = 384;
static constexpr int Mrows = Bsz * Nseq;

template <int I, int N, class F>
DEVI void static_for(F&& f) {
  if constexpr (I < N) {
    f(std::integral_constant<int, I>{});
    static_for<I + 1, N>(f);
  }
}

DEVI float lo_f(unsigned u) { return __uint_as_float(u << 16); }
DEVI float hi_f(unsigned u) { return __uint_as_float(u & 0xffff0000u); }
DEVI float bf2f(u16 h) { return __uint_as_float(((unsigned)h) << 16); }
DEVI unsigned pack2(float a, float b) {
  bf16x2 v;
  v[0] = (__bf16)a;
  v[1] = (__bf16)b;
  return __builtin_bit_cast(unsigned, v);
}
DEVI float rbf(float a) { return lo_f(pack2(a, 0.f) ); }

template <int N>
DEVI void wait_vm() { asm volatile("s_waitcnt vmcnt(%0)" ::"i"(N) : "memory"); }
DEVI void wait_lgkm0() { asm volatile("s_waitcnt lgkmcnt(0)" ::: "memory"); }
DEVI void bar() { __builtin_amdgcn_s_barrier(); }

DEVI void glds16(const void* sbase, unsigned voff, unsigned lds_dst) {
  unsigned keep;
  asm volatile(
      "s_mov_b32 %0, m0\n\t"
      "s_mov_b32 m0, %3\n\t"
      "s_nop 0\n\t"
      "global_load_lds_dwordx4 %1, %2\n\t"
      "s_mov_b32 m0, %0"
      : "=&s"(keep)
      : "v"(voff), "s"(sbase), "s"(lds_dst)
      : "memory");
}

DEVI void gld16(u32x4& d, const void* p) { asm volatile("global_load_dwordx4 %0, %1, off" : "=v"(d) : "v"(p) : "memory"); }
DEVI void gld8(u32x2& d, const void* p) { asm volatile("global_load_dwordx2 %0, %1, off" : "=v"(d) : "v"(p) : "memory"); }
template <class T>
DEVI void pin(T& v) { asm volatile("" : "+v"(v)); }
DEVI f32x4 asf4(u32x4 v) { return __builtin_bit_cast(f32x4, v); }

DEVI bf16x8 lds_read16(const LDS_AS char* p) { return *(const LDS_AS bf16x8*)p; }
DEVI u32x4 lds_read16u(const LDS_AS char* p) { return *(const LDS_AS u32x4*)p; }
DEVI f32x4 lds_read16f(const LDS_AS char* p) { return *(const LDS_AS f32x4*)p; }
DEVI bf16x4 lds_tr(const LDS_AS char* p) {
  return __builtin_amdgcn_ds_read_tr16_b64_v4bf16((LDS_AS bf16x4*)p);
}
DEVI bf16x8 cat4(bf16x4 a, bf16x4 b) {
  return __builtin_shufflevector(a, b, 0, 1, 2, 3, 4, 5, 6, 7);
}
DEVI f32x4 mfma16(bf16x8 a, bf16x8 b, f32x4 c) {
  return __builtin_amdgcn_mfma_f32_16x16x32_bf16(a, b, c, 0, 0, 0);
}
DEVI int uni(int v) { return __builtin_amdgcn_readfirstlane(v); }

DEVI u32x4 pair_pack(unsigned ax, unsigned ay, unsigned bx, unsigned by) {
  auto r0 = __builtin_amdgcn_permlane16_swap(ax, bx, false, false);
  auto r1 = __builtin_amdgcn_permlane16_swap(ay, by, false, false);
  u32x4 o;
  o[0] = r0[0];
  o[1] = r1[0];
  o[2] = r0[1];
  o[3] = r1[1];
  return o;
}

__global__ __launch_bounds__(256) void k0_mod(const u16* __restrict__ c, const u16* __restrict__ wmod,
                                              float* __restrict__ modp, const u16* __restrict__ w0,
                                              const u16* __restrict__ w1) {
  __shared__ float sc[2][384];
  __shared__ float red[32][2][64];
  const int tid = threadIdx.x;
  if (blockIdx.x >= 144) {
    const int t = (blockIdx.x - 144) * 256 + tid;
    u32x4 a = {0u, 0u, 0u, 0u};
    u32x4 v[12];
#pragma unroll
    for (int i = 0; i < 12; ++i) {
      int ci = t + i * 24576;
      const u16* p = (ci < 221184) ? (w0 + (size_t)ci * 8) : (w1 + (size_t)(ci - 221184) * 8);
      v[i] = *(const u32x4*)p;
    }
#pragma unroll
    for (int i = 0; i < 12; ++i) a ^= v[i];
    asm volatile("" ::"v"(a));
    return;
  }
  const int cb = blockIdx.x % 72, kp = blockIdx.x / 72;
  const int tc = tid & 7, tr = tid >> 3;
  u32x4 w[12];
  const u16* wp = wmod + (size_t)(kp * 384 + tr) * 4608 + cb * 64 + tc * 8;
#pragma unroll
  for (int i = 0; i < 12; ++i) w[i] = *(const u32x4*)(wp + (size_t)i * 32 * 4608);
  for (int i = tid; i < 768; i += 256) {
    int b = i / 384, k = i % 384;
    float v = bf2f(c[b * 768 + kp * 384 + k]);
    float s = v / (1.f + __expf(-v));
    sc[b][k] = rbf(s);
  }
  __syncthreads();
  float acc[2][8];
#pragma unroll
  for (int b = 0; b < 2; ++b)
#pragma unroll
    for (int j = 0; j < 8; ++j) acc[b][j] = 0.f;
#pragma unroll
  for (int i = 0; i < 12; ++i) {
    float s0 = sc[0][tr + 32 * i], s1 = sc[1][tr + 32 * i];
#pragma unroll
    for (int j = 0; j < 4; ++j) {
      float a = lo_f(w[i][j]), bb = hi_f(w[i][j]);
      acc[0][2 * j] += s0 * a;
      acc[0][2 * j + 1] += s0 * bb;
      acc[1][2 * j] += s1 * a;
      acc[1][2 * j + 1] += s1 * bb;
    }
  }
#pragma unroll
  for (int b = 0; b < 2; ++b)
#pragma unroll
    for (int j = 0; j < 8; ++j) red[tr][b][tc * 8 + j] = acc[b][j];
  __syncthreads();
  if (tid < 128) {
    int b = tid >> 6, col = tid & 63;
    float s = 0.f;
#pragma unroll
    for (int r = 0; r < 32; ++r) s += red[r][b][col];
    modp[(kp * 2 + b) * 4608 + cb * 64 + col] = s;
  }
}

__global__ __launch_bounds__(256) void k_lnmod(const u16* __restrict__ x, const float* __restrict__ modp, int offS,
                                               u16* __restrict__ h, const u16* __restrict__ w0,
                                               const u16* __restrict__ w1) {
  const int tid = threadIdx.x;
  if (blockIdx.x >= 256) {
    const int t = (blockIdx.x - 256) * 256 + tid;
    u32x4 a = {0u, 0u, 0u, 0u};
    u32x4 v[12];
#pragma unroll
    for (int i = 0; i < 12; ++i) {
      int ci = t + i * 49152;
      const u16* p = (ci < 294912) ? (w0 + (size_t)ci * 8) : (w1 + (size_t)(ci - 294912) * 8);
      v[i] = *(const u32x4*)p;
    }
#pragma unroll
    for (int i = 0; i < 12; ++i) a ^= v[i];
    asm volatile("" ::"v"(a));
    return;
  }
  const int w = tid >> 6, l = tid & 63;
  const int row0 = blockIdx.x * 8 + w * 2;
  const int b = row0 >> 10;
  u32x2 v[2][3];
#pragma unroll
  for (int r = 0; r < 2; ++r) {
    const u16* xp = x + (size_t)(row0 + r) * 768 + l * 4;
#pragma unroll
    for (int i = 0; i < 3; ++i) v[r][i] = *(const u32x2*)(xp + i * 256);
  }
  f32x4 gg[3], tt[3];
#pragma unroll
  for (int i = 0; i < 3; ++i) {
    const float* m0p = modp + (0 * 2 + b) * 4608 + offS + l * 4 + i * 256;
    const float* m1p = modp + (1 * 2 + b) * 4608 + offS + l * 4 + i * 256;
    f32x4 s0 = *(const f32x4*)m0p, s1 = *(const f32x4*)m1p;
    f32x4 c0 = *(const f32x4*)(m0p + 768), c1 = *(const f32x4*)(m1p + 768);
#pragma unroll
    for (int j = 0; j < 4; ++j) {
      tt[i][j] = rbf(s0[j] + s1[j]);
      gg[i][j] = rbf(1.f + rbf(c0[j] + c1[j]));
    }
  }
#pragma unroll
  for (int r = 0; r < 2; ++r) {
    float f[12];
#pragma unroll
    for (int i = 0; i < 3; ++i) {
      f[4 * i] = lo_f(v[r][i][0]);
      f[4 * i + 1] = hi_f(v[r][i][0]);
      f[4 * i + 2] = lo_f(v[r][i][1]);
      f[4 * i + 3] = hi_f(v[r][i][1]);
    }
    float s = 0.f;
#pragma unroll
    for (int i = 0; i < 12; ++i) s += f[i];
#pragma unroll
    for (int o = 32; o > 0; o >>= 1) s += __shfl_xor(s, o);
    float mean = s * (1.f / 768.f);
    float q = 0.f;
#pragma unroll
    for (int i = 0; i < 12; ++i) {
      float d = f[i] - mean;
      q += d * d;
    }
#pragma unroll
    for (int o = 32; o > 0; o >>= 1) q += __shfl_xor(q, o);
    const float mb = rbf(mean);
    const float den = rbf(sqrtf(rbf(rbf(q * (1.f / 768.f)) + 1e-5f)));
    u16* op = h + (size_t)(row0 + r) * 768 + l * 4;
#pragma unroll
    for (int i = 0; i < 3; ++i) {
      float y[4];
#pragma unroll
      for (int j = 0; j < 4; ++j) y[j] = rbf(rbf(rbf(f[4 * i + j] - mb) / den) * gg[i][j]) + tt[i][j];
      u32x2 o;
      o[0] = pack2(y[0], y[1]);
      o[1] = pack2(y[2], y[3]);
      *(u32x2*)(op + i * 256) = o;
    }
  }
}

template <int BN, int NS, int EPI>
__global__ __launch_bounds__(768, 3) void k_gemm_big(const u16* __restrict__ A, const u16* __restrict__ W,
                                                     void* __restrict__ outv, u16* __restrict__ out2) {
  constexpr int NC = BN / 16;
  constexpr int LDW = (EPI == 0) ? 2304 : (EPI == 1 ? 3072 : 768);
  constexpr int LDA = (EPI == 2) ? 3072 : 768;
  constexpr int ASZ = 128 * 128;
  constexpr int BSZ = 8 * NC * 256;
  constexpr int STG = ASZ + BSZ;
  constexpr int SUBW = 2 * NC;
  constexpr int NBI = (SUBW + 3) / 4;
  constexpr int NLD = 4 + NBI;
  constexpr int NK = 768 / 64;
  __shared__ __attribute__((aligned(16))) char smem_raw[NS * STG];
  LDS_AS char* smem = (LDS_AS char*)smem_raw;
  const unsigned lds0 = (unsigned)(uintptr_t)smem;
  constexpr int KID = (EPI == 0) ? 1 : (EPI == 1 ? 6 : 5);
  STAMP(KID, 0);
  const int tid = threadIdx.x;
  const int l = tid & 63;
  const int w = uni(tid >> 6);
  const int bid = blockIdx.x;
  const int xcd = bid & 7, idx = bid >> 3;
  int tm, tn, ks = 0;
  if constexpr (EPI == 2) {
    tm = (xcd >> 1) * 4 + (idx & 3);
    tn = (idx >> 2) & 3;
    ks = (xcd & 1) * 2 + (idx >> 4);
  } else {
    tm = (xcd >> 1) * 4 + (idx & 3);
    tn = (xcd & 1) * 8 + (idx >> 2);
  }
  const int m0 = tm * 128, n0 = tn * BN;

  if (w >= 8) {
    const int lw = w - 8;
    const unsigned voffA = (unsigned)((l >> 3) * LDA * 2 + (((l & 7) ^ (l >> 3)) << 4));
    unsigned voffB[NBI];
#pragma unroll
    for (int i = 0; i < NBI; ++i) {
      int sub = SUBW * lw + 4 * i + (l >> 4);
      if (sub >= SUBW * (lw + 1)) sub = SUBW * (lw + 1) - 1;
      int kb = sub / NC, cc = sub % NC;
      int within = l & 15;
      int r = (within >> 1) ^ ((kb & 1) << 2);
      voffB[i] = (unsigned)(((kb * 8 + r) * LDW + cc * 16 + (within & 1) * 8) * 2);
    }
    const char* abase = (const char*)(A + (size_t)(m0 + 32 * lw) * LDA + ks * 768);
    const char* bbase = (const char*)(W + (size_t)ks * 768 * LDW + n0);
    auto issue = [&](int kt, int st) {
      const unsigned sa = lds0 + st * STG;
#pragma unroll
      for (int i = 0; i < 4; ++i)
        glds16(abase + (size_t)i * 8 * LDA * 2 + kt * 128, voffA, sa + (32 * lw + 8 * i) * 128);
      const char* sb = bbase + (size_t)kt * 64 * LDW * 2;
#pragma unroll
      for (int i = 0; i < NBI; ++i) {
        unsigned dst = sa + ASZ + (SUBW * lw + 4 * i) * 256;
        if (4 * i + 4 <= SUBW) {
          glds16(sb, voffB[i], dst);
        } else {
          if (l < (SUBW - 4 * i) * 16) glds16(sb, voffB[i], dst);
        }
      }
    };
#pragma unroll
    for (int s = 0; s < NS - 1; ++s) issue(s, s);
    int stn = NS - 1;
#pragma unroll 1
    for (int kt = 0; kt < NK; ++kt) {
      if (kt < NK - (NS - 2)) wait_vm<(NS - 2) * NLD>();
      else wait_vm<0>();
      bar();
      if (kt + NS - 1 < NK) issue(kt + NS - 1, stn);
      stn = (stn + 1 == NS) ? 0 : stn + 1;
    }
    bar();
    bar();
    return;
  }

  const int wm = w & 3, kh = w >> 2;
  const int g = l >> 4;
  const unsigned aoff = (unsigned)(((32 * wm + (l & 15)) * 128 + ((g ^ (l & 7)) << 4)) ^ (kh * 64));
  const int q = (l & 15) >> 2, p = l & 3;
  const unsigned boff_t0 = (unsigned)(ASZ + (4 * kh + g) * NC * 256 + ((4 * (g & 1) + q) * 32) + p * 8);
  const unsigned boff_t1 = (unsigned)(ASZ + (4 * kh + g) * NC * 256 + ((4 * (1 - (g & 1)) + q) * 32) + p * 8);

  f32x4 acc[2][NC];
#pragma unroll
  for (int i = 0; i < 2; ++i)
#pragma unroll
    for (int j = 0; j < NC; ++j) acc[i][j] = (f32x4){0.f, 0.f, 0.f, 0.f};
  STAMP(KID, 1);

  int st = 0;
#pragma unroll 1
  for (int kt = 0; kt < NK; ++kt) {
    wait_lgkm0();
    bar();
    const LDS_AS char* sp = smem + st * STG;
    bf16x8 xb[2];
#pragma unroll
    for (int mf = 0; mf < 2; ++mf) xb[mf] = lds_read16(sp + aoff + mf * 2048);
#pragma unroll
    for (int cc = 0; cc < NC; ++cc) {
      bf16x4 w0 = lds_tr(sp + boff_t0 + cc * 256);
      bf16x4 w1 = lds_tr(sp + boff_t1 + cc * 256);
      bf16x8 wf = cat4(w0, w1);
      acc[0][cc] = mfma16(wf, xb[0], acc[0][cc]);
      acc[1][cc] = mfma16(wf, xb[1], acc[1][cc]);
    }
    st = (st + 1 == NS) ? 0 : st + 1;
  }
  STAMP(KID, 14);

  wait_lgkm0();
  bar();
  {
    LDS_AS char* rp = smem + (wm * 2 + kh) * NC * 1024 + l * 16;
    if (kh) {
#pragma unroll
      for (int cc = 0; cc < NC; ++cc) {
        *(LDS_AS f32x4*)(rp + cc * 1024) = acc[0][cc];
        acc[0][cc] = acc[1][cc];
      }
    } else {
#pragma unroll
      for (int cc = 0; cc < NC; ++cc) *(LDS_AS f32x4*)(rp + cc * 1024) = acc[1][cc];
    }
  }
  wait_lgkm0();
  bar();
  const int row = m0 + 32 * wm + 16 * kh + (l & 15);
  const LDS_AS char* rp = smem + (wm * 2 + (1 - kh)) * NC * 1024 + l * 16;
  if constexpr (EPI == 2) {
    float* op = (float*)outv + ((size_t)ks * 2048 + row) * 768 + n0 + 4 * g;
#pragma unroll
    for (int cc = 0; cc < NC; ++cc) *(f32x4*)(op + 16 * cc) = acc[0][cc] + lds_read16f(rp + cc * 1024);
  } else {
    u16* out = (u16*)outv;
    unsigned pk[NC][2];
#pragma unroll
    for (int cc = 0; cc < NC; ++cc) {
      f32x4 v = acc[0][cc] + lds_read16f(rp + cc * 1024);
      if constexpr (EPI == 1) {
        const float c1 = -2.302208198144325f;
        const float c2 = -0.1029432397500235f;
        unsigned y01 = pack2(v[0], v[1]), y23 = pack2(v[2], v[3]);
        float y[4] = {lo_f(y01), hi_f(y01), lo_f(y23), hi_f(y23)};
#pragma unroll
        for (int j = 0; j < 4; ++j) {
          float e = __builtin_amdgcn_exp2f(y[j] * (c1 + c2 * y[j] * y[j]));
          v[j] = y[j] * __builtin_amdgcn_rcpf(1.f + e);
        }
      }
      pk[cc][0] = pack2(v[0], v[1]);
      pk[cc][1] = pack2(v[2], v[3]);
    }
    auto dst_ptr = [&](int n) -> u16* {
      if constexpr (EPI == 1) {
        return out + (size_t)row * 3072 + n;
      } else {
        if (n < 1536) return out + (size_t)row * 1536 + n;
        int nv = n - 1536;
        int hd = nv / 96;
        int dd = nv - hd * 96;
        return out2 + ((size_t)(((row >> 10) * 8 + hd) * 1024 + (row & 1023))) * 96 + dd;
      }
    };
#pragma unroll
    for (int a = 0; a < NC / 2; ++a) {
      u32x4 o = pair_pack(pk[2 * a][0], pk[2 * a][1], pk[2 * a + 1][0], pk[2 * a + 1][1]);
      *(u32x4*)dst_ptr(n0 + 32 * a + (g & 1) * 16 + (g & 2) * 4) = o;
    }
    if constexpr (NC & 1) {
      u32x2 o;
      o[0] = pk[NC - 1][0];
      o[1] = pk[NC - 1][1];
      *(u32x2*)dst_ptr(n0 + 16 * (NC - 1) + 4 * g) = o;
    }
  }
  STAMP(KID, 15);
}

__global__ __launch_bounds__(768, 3) void k_scores(const u16* __restrict__ qk, u16* __restrict__ P,
                                                   f32x2* __restrict__ ml) {
  constexpr int NS = 4;
  constexpr int ASZ = 128 * 128;
  constexpr int STG = 2 * ASZ;
  constexpr int NK = 384 / 64;
  constexpr int NLD = 8;
  constexpr int LDQ = 1536;
  __shared__ __attribute__((aligned(16))) char smem_raw[NS * STG];
  LDS_AS char* smem = (LDS_AS char*)smem_raw;
  const unsigned lds0 = (unsigned)(uintptr_t)smem;
  STAMP(2, 0);
  const int tid = threadIdx.x, l = tid & 63;
  const int w = uni(tid >> 6);
  const int bid = blockIdx.x;
  const int xcd = bid & 7, idx = bid >> 3;
  const int bh = xcd >> 1;
  const int tq = (xcd & 1) * 4 + (idx & 3);
  const int tk = idx >> 2;
  const int b = bh >> 1, h = bh & 1;

  if (w >= 8) {
    const int lw = w - 8;
    const unsigned voff = (unsigned)((l >> 3) * LDQ * 2 + (((l & 7) ^ (l >> 3)) << 4));
    const char* qbase = (const char*)(qk + (size_t)(b * 1024 + tq * 128 + 32 * lw) * LDQ + h * 384);
    const char* kbase = (const char*)(qk + (size_t)(b * 1024 + tk * 128 + 32 * lw) * LDQ + 768 + h * 384);
    auto issue = [&](int kt, int st) {
      const unsigned sa = lds0 + st * STG;
#pragma unroll
      for (int i = 0; i < 4; ++i)
        glds16(qbase + (size_t)i * 8 * LDQ * 2 + kt * 128, voff, sa + (32 * lw + 8 * i) * 128);
#pragma unroll
      for (int i = 0; i < 4; ++i)
        glds16(kbase + (size_t)i * 8 * LDQ * 2 + kt * 128, voff, sa + ASZ + (32 * lw + 8 * i) * 128);
    };
#pragma unroll
    for (int s = 0; s < NS - 1; ++s) issue(s, s);
    int stn = NS - 1;
#pragma unroll 1
    for (int kt = 0; kt < NK; ++kt) {
      if (kt < NK - (NS - 2)) wait_vm<(NS - 2) * NLD>();
      else wait_vm<0>();
      bar();
      if (kt + NS - 1 < NK) issue(kt + NS - 1, stn);
      stn = (stn + 1 == NS) ? 0 : stn + 1;
    }
    bar();
    bar();
    return;
  }

  const int wm = w & 3, kh = w >> 2;
  const int g = l >> 4;
  const unsigned qoff = (unsigned)(((32 * wm + (l & 15)) * 128 + ((g ^ (l & 7)) << 4)) ^ (kh * 64));
  const unsigned koff = (unsigned)(ASZ + (((l & 15) * 128 + ((g ^ (l & 7)) << 4)) ^ (kh * 64)));
  f32x4 acc[2][8];
#pragma unroll
  for (int i = 0; i < 2; ++i)
#pragma unroll
    for (int j = 0; j < 8; ++j) acc[i][j] = (f32x4){0.f, 0.f, 0.f, 0.f};

  int st = 0;
#pragma unroll 1
  for (int kt = 0; kt < NK; ++kt) {
    wait_lgkm0();
    bar();
    const LDS_AS char* sp = smem + st * STG;
    bf16x8 qf[2];
#pragma unroll
    for (int mf = 0; mf < 2; ++mf) qf[mf] = lds_read16(sp + qoff + mf * 2048);
#pragma unroll
    for (int kf = 0; kf < 8; ++kf) {
      bf16x8 kfr = lds_read16(sp + koff + kf * 2048);
      acc[0][kf] = mfma16(kfr, qf[0], acc[0][kf]);
      acc[1][kf] = mfma16(kfr, qf[1], acc[1][kf]);
    }
    st = (st + 1 == NS) ? 0 : st + 1;
  }
  STAMP(2, 14);
  wait_lgkm0();
  bar();
  {
    LDS_AS char* rp = smem + (wm * 2 + kh) * 8 * 1024 + l * 16;
    if (kh) {
#pragma unroll
      for (int kf = 0; kf < 8; ++kf) {
        *(LDS_AS f32x4*)(rp + kf * 1024) = acc[0][kf];
        acc[0][kf] = acc[1][kf];
      }
    } else {
#pragma unroll
      for (int kf = 0; kf < 8; ++kf) *(LDS_AS f32x4*)(rp + kf * 1024) = acc[1][kf];
    }
  }
  wait_lgkm0();
  bar();
  f32x4 s[8];
  {
    const LDS_AS char* rp = smem + (wm * 2 + (1 - kh)) * 8 * 1024 + l * 16;
#pragma unroll
    for (int kf = 0; kf < 8; ++kf) {
      f32x4 v = acc[0][kf] + lds_read16f(rp + kf * 1024);
      unsigned a01 = pack2(v[0], v[1]), a23 = pack2(v[2], v[3]);
      a01 = pack2(lo_f(a01) * 0.051031036307982884f, hi_f(a01) * 0.051031036307982884f);
      a23 = pack2(lo_f(a23) * 0.051031036307982884f, hi_f(a23) * 0.051031036307982884f);
      s[kf] = (f32x4){lo_f(a01), hi_f(a01), lo_f(a23), hi_f(a23)};
    }
  }
  const float l2e = 1.4426950408889634f;
  float mx = -INFINITY;
#pragma unroll
  for (int kf = 0; kf < 8; ++kf)
#pragma unroll
    for (int j = 0; j < 4; ++j) mx = fmaxf(mx, s[kf][j]);
  mx = fmaxf(mx, __shfl_xor(mx, 16));
  mx = fmaxf(mx, __shfl_xor(mx, 32));
  const float mbl = mx * l2e;
  float sum = 0.f;
  unsigned pk[8][2];
#pragma unroll
  for (int kf = 0; kf < 8; ++kf) {
    float e[4];
#pragma unroll
    for (int j = 0; j < 4; ++j) {
      e[j] = __builtin_amdgcn_exp2f(s[kf][j] * l2e - mbl);
      sum += e[j];
    }
    pk[kf][0] = pack2(e[0], e[1]);
    pk[kf][1] = pack2(e[2], e[3]);
  }
  sum += __shfl_xor(sum, 16);
  sum += __shfl_xor(sum, 32);
  const int qrow = tq * 128 + 32 * wm + 16 * kh + (l & 15);
  u16* op = P + ((size_t)bh * 1024 + qrow) * 1024 + tk * 128;
#pragma unroll
  for (int a = 0; a < 4; ++a) {
    u32x4 o = pair_pack(pk[2 * a][0], pk[2 * a][1], pk[2 * a + 1][0], pk[2 * a + 1][1]);
    *(u32x4*)(op + 32 * a + (g & 1) * 16 + (g & 2) * 4) = o;
  }
  if (g == 0) {
    f32x2 s2;
    s2[0] = mbl;
    s2[1] = sum;
    ml[((size_t)bh * 1024 + qrow) * 8 + tk] = s2;
  }
  STAMP(2, 15);
}

template <int MODE, int NL>
__global__ __launch_bounds__(64 * (4 + NL), NL / 4 + 1) void k_gemm_small(const u16* __restrict__ Ain, const u16* __restrict__ Bin,
                                                       const float* __restrict__ modp,
                                                       const u16* __restrict__ resid,
                                                       const f32x2* __restrict__ ml, f32x2* __restrict__ stats,
                                                       u16* __restrict__ out) {
  constexpr int KTOT = (MODE == 0) ? 1024 : (MODE == 1 ? 768 : 3072);
  constexpr int KH = KTOT / 2;
  constexpr int NK = KH / 64;
  constexpr int LDA = KTOT;
  constexpr int LDB = (MODE == 0) ? 96 : 768;
  constexpr int NS = 4;
  constexpr int STG = 40960;
  constexpr int NLD = 40 / NL;
  constexpr int AI = 16 / NL, BI = 24 / NL;
  __shared__ __attribute__((aligned(16))) char smem_raw[NS * STG];
  LDS_AS char* smem = (LDS_AS char*)smem_raw;
  const unsigned lds0 = (unsigned)(uintptr_t)smem;
  STAMP(3 + MODE, 0);
  const int tid = threadIdx.x, l = tid & 63;
  const int w = uni(tid >> 6);
  const int bid = blockIdx.x;
  const int xcd = bid & 7, idx = bid >> 3;
  const int g = l >> 4;

  int m0, n0, bh = 0;
  const u16* Ap;
  const u16* Bp;
  if constexpr (MODE == 0) {
    bh = xcd >> 1;
    const int tq = (xcd & 1) * 8 + (idx & 7);
    const int td = idx >> 3;
    m0 = tq * 64;
    n0 = td * 96;
    Ap = Ain + ((size_t)bh * 1024 + m0) * 1024;
    Bp = Bin + ((size_t)(bh * 4 + td) * 1024) * 96;
  } else {
    const int tm = (xcd >> 1) * 8 + (idx & 7);
    const int tn = (xcd & 1) * 4 + (idx >> 3);
    m0 = tm * 64;
    n0 = tn * 96;
    Ap = Ain + (size_t)m0 * LDA;
    Bp = Bin + n0;
  }

  if (w >= 4) {
    const int lw = w - 4;
    const int kq = lw & 1, part = lw >> 1;
    const unsigned voffA = (unsigned)((l >> 3) * LDA * 2 + (((l & 7) ^ (l >> 3)) << 4));
    unsigned voffB[BI];
#pragma unroll
    for (int i = 0; i < BI; ++i) {
      int sub = 4 * BI * part + 4 * i + (l >> 4);
      int kb = sub / 6, cc = sub % 6;
      int within = l & 15;
      int r = (within >> 1) ^ ((kb & 1) << 2);
      voffB[i] = (unsigned)(((kb * 8 + r) * LDB + cc * 16 + (within & 1) * 8) * 2);
    }
    const char* abase = (const char*)(Ap + (size_t)(8 * AI * part) * LDA + kq * KH);
    const char* bbase = (const char*)(Bp + (size_t)(kq * KH) * LDB);
    auto issue = [&](int kt, int st) {
      const unsigned sa = lds0 + st * STG;
#pragma unroll
      for (int i = 0; i < AI; ++i)
        glds16(abase + (size_t)i * 8 * LDA * 2 + kt * 128, voffA, sa + kq * 8192 + (8 * AI * part + 8 * i) * 128);
      const char* sb = bbase + (size_t)kt * 64 * LDB * 2;
#pragma unroll
      for (int i = 0; i < BI; ++i) glds16(sb, voffB[i], sa + 16384 + kq * 12288 + (4 * BI * part + 4 * i) * 256);
    };
#pragma unroll
    for (int s = 0; s < NS - 1; ++s) issue(s, s);
    int stn = NS - 1;
#pragma unroll 1
    for (int kt = 0; kt < NK; ++kt) {
      if (kt < NK - (NS - 2)) wait_vm<(NS - 2) * NLD>();
      else wait_vm<0>();
      bar();
      if (kt + NS - 1 < NK) issue(kt + NS - 1, stn);
      stn = (stn + 1 == NS) ? 0 : stn + 1;
    }
    bar();
    bar();
    return;
  }

  const int kq = w & 1, nh = w >> 1;
  float cf[4][4];
  if constexpr (MODE == 0) {
#pragma unroll
    for (int mf = 0; mf < 4; ++mf) {
      const f32x4* sp = (const f32x4*)(ml + ((size_t)bh * 1024 + m0 + 16 * mf + (l & 15)) * 8);
      f32x4 sv[4];
#pragma unroll
      for (int j = 0; j < 4; ++j) sv[j] = sp[j];
      float mx = fmaxf(sv[0][0], sv[0][2]);
#pragma unroll
      for (int j = 1; j < 4; ++j) mx = fmaxf(mx, fmaxf(sv[j][0], sv[j][2]));
      float e[8];
      float lsum = 0.f;
#pragma unroll
      for (int j = 0; j < 4; ++j) {
        e[2 * j] = __builtin_amdgcn_exp2f(sv[j][0] - mx);
        e[2 * j + 1] = __builtin_amdgcn_exp2f(sv[j][2] - mx);
        lsum += e[2 * j] * sv[j][1] + e[2 * j + 1] * sv[j][3];
      }
      float inv = 1.f / lsum;
      if (kq) {
#pragma unroll
        for (int j = 0; j < 4; ++j) cf[mf][j] = e[4 + j] * inv;
      } else {
#pragma unroll
        for (int j = 0; j < 4; ++j) cf[mf][j] = e[j] * inv;
      }
    }
  }
  f32x4 gq[2][3][2];
  u32x2 xr[2][3];
  if constexpr (MODE != 0) {
    const int goff = (MODE == 1) ? 2 * 768 : 5 * 768;
    const int b = m0 >> 10;
    const float* mp0 = modp + (0 * 2 + b) * 4608 + goff + n0 + 48 * nh + 4 * g;
    const float* mp1 = modp + (1 * 2 + b) * 4608 + goff + n0 + 48 * nh + 4 * g;
#pragma unroll
    for (int cc = 0; cc < 3; ++cc) {
      gq[0][cc][0] = *(const f32x4*)(mp0 + 16 * cc);
      gq[0][cc][1] = *(const f32x4*)(mp1 + 16 * cc);
    }
#pragma unroll
    for (int mf = 0; mf < 2; ++mf) {
      const u16* rp = resid + (size_t)(m0 + 32 * kq + 16 * mf + (l & 15)) * 768 + n0 + 48 * nh + 4 * g;
#pragma unroll
      for (int cc = 0; cc < 3; ++cc) xr[mf][cc] = *(const u32x2*)(rp + 16 * cc);
    }
  }

  const unsigned aoff0 = (unsigned)(kq * 8192 + (l & 15) * 128 + ((g ^ (l & 7)) << 4));
  const int q = (l & 15) >> 2, p = l & 3;
  const unsigned boff_t0 = (unsigned)(16384 + kq * 12288 + (g * 6 + 3 * nh) * 256 + ((4 * (g & 1) + q) * 32) + p * 8);
  const unsigned boff_t1 =
      (unsigned)(16384 + kq * 12288 + (g * 6 + 3 * nh) * 256 + ((4 * (1 - (g & 1)) + q) * 32) + p * 8);

  f32x4 acc[4][3];
  f32x4 tot[4][3];
#pragma unroll
  for (int i = 0; i < 4; ++i)
#pragma unroll
    for (int j = 0; j < 3; ++j) {
      acc[i][j] = (f32x4){0.f, 0.f, 0.f, 0.f};
      tot[i][j] = (f32x4){0.f, 0.f, 0.f, 0.f};
    }
  STAMP(3 + MODE, 1);

  int st = 0;
#pragma unroll 1
  for (int kt = 0; kt < NK; ++kt) {
    wait_lgkm0();
    bar();
    const LDS_AS char* sp = smem + st * STG;
#pragma unroll
    for (int ks = 0; ks < 2; ++ks) {
      bf16x8 af[4];
#pragma unroll
      for (int mf = 0; mf < 4; ++mf) af[mf] = lds_read16(sp + ((aoff0 ^ (ks * 64)) + mf * 2048));
#pragma unroll
      for (int cc = 0; cc < 3; ++cc) {
        bf16x4 w0 = lds_tr(sp + boff_t0 + ks * 6144 + cc * 256);
        bf16x4 w1 = lds_tr(sp + boff_t1 + ks * 6144 + cc * 256);
        bf16x8 wf = cat4(w0, w1);
#pragma unroll
        for (int mf = 0; mf < 4; ++mf) acc[mf][cc] = mfma16(wf, af[mf], acc[mf][cc]);
      }
    }
    if constexpr (MODE == 0) {
      if (kt & 1) {
#pragma unroll
        for (int mf = 0; mf < 4; ++mf) {
          const float c = cf[mf][0];
          cf[mf][0] = cf[mf][1];
          cf[mf][1] = cf[mf][2];
          cf[mf][2] = cf[mf][3];
#pragma unroll
          for (int cc = 0; cc < 3; ++cc) {
            tot[mf][cc] += acc[mf][cc] * c;
            acc[mf][cc] = (f32x4){0.f, 0.f, 0.f, 0.f};
          }
        }
      }
    }
    st = (st + 1 == NS) ? 0 : st + 1;
  }
  if constexpr (MODE == 0) {
#pragma unroll
    for (int mf = 0; mf < 4; ++mf)
#pragma unroll
      for (int cc = 0; cc < 3; ++cc) acc[mf][cc] = tot[mf][cc];
  }
  STAMP(3 + MODE, 12);
  wait_lgkm0();
  bar();
  {
    LDS_AS char* rp = smem + (nh * 2 + kq) * 6144 + l * 16;
    if (kq) {
#pragma unroll
      for (int mf = 0; mf < 2; ++mf)
#pragma unroll
        for (int cc = 0; cc < 3; ++cc) {
          *(LDS_AS f32x4*)(rp + (mf * 3 + cc) * 1024) = acc[mf][cc];
          acc[mf][cc] = acc[2 + mf][cc];
        }
    } else {
#pragma unroll
      for (int mf = 0; mf < 2; ++mf)
#pragma unroll
        for (int cc = 0; cc < 3; ++cc) *(LDS_AS f32x4*)(rp + (mf * 3 + cc) * 1024) = acc[2 + mf][cc];
    }
  }
  wait_lgkm0();
  bar();
  f32x4 r[2][3];
  {
    const LDS_AS char* rp = smem + (nh * 2 + (1 - kq)) * 6144 + l * 16;
#pragma unroll
    for (int mf = 0; mf < 2; ++mf)
#pragma unroll
      for (int cc = 0; cc < 3; ++cc)
        r[mf][cc] = acc[mf][cc] + lds_read16f(rp + (mf * 3 + cc) * 1024);
  }
  STAMP(3 + MODE, 13);
#pragma unroll
  for (int mf = 0; mf < 2; ++mf) {
    const int mrow = m0 + 32 * kq + 16 * mf + (l & 15);
    float v[3][4];
    u16* op;
    if constexpr (MODE == 0) {
      op = out + (size_t)((bh >> 1) * 1024 + mrow) * 768 + (bh & 1) * 384 + n0 + 48 * nh;
#pragma unroll
      for (int cc = 0; cc < 3; ++cc)
#pragma unroll
        for (int j = 0; j < 4; ++j) v[cc][j] = r[mf][cc][j];
    } else {
      op = out + (size_t)mrow * 768 + n0 + 48 * nh;
#pragma unroll
      for (int cc = 0; cc < 3; ++cc) {
        float xv[4] = {lo_f(xr[mf][cc][0]), hi_f(xr[mf][cc][0]), lo_f(xr[mf][cc][1]), hi_f(xr[mf][cc][1])};
#pragma unroll
        for (int j = 0; j < 4; ++j) {
          float gate = rbf(gq[0][cc][0][j] + gq[0][cc][1][j]);
          v[cc][j] = rbf(xv[j] + rbf(gate * rbf(r[mf][cc][j])));
        }
      }
    }
    {
      u32x4 o = pair_pack(pack2(v[0][0], v[0][1]), pack2(v[0][2], v[0][3]), pack2(v[1][0], v[1][1]),
                          pack2(v[1][2], v[1][3]));
      *(u32x4*)(op + (g & 1) * 16 + (g & 2) * 4) = o;
      u32x2 o2;
      o2[0] = pack2(v[2][0], v[2][1]);
      o2[1] = pack2(v[2][2], v[2][3]);
      *(u32x2*)(op + 32 + 4 * g) = o2;
    }
  }
  STAMP(3 + MODE, 15);
}

#define SNL 4
extern "C" void kernel_launch(void* const* d_in, const int* in_sizes, int n_in, void* d_out, int out_size,
                              void* d_ws, size_t ws_size, hipStream_t stream) {
  const u16* x = (const u16*)d_in[0];
  const u16* c = (const u16*)d_in[1];
  const u16* wqkv = (const u16*)d_in[2];
  const u16* wo = (const u16*)d_in[3];
  const u16* wmod = (const u16*)d_in[4];
  const u16* wfc1 = (const u16*)d_in[5];
  const u16* wfc2 = (const u16*)d_in[6];
  u16* out = (u16*)d_out;

  char* ws = (char*)d_ws;
  size_t off = 0;
  auto alloc = [&](size_t bytes) {
    char* p = ws + off;
    off += (bytes + 255) & ~(size_t)255;
    return p;
  };
  float* modp = (float*)alloc(2 * 2 * 4608 * 4);
  u16* h = (u16*)alloc((size_t)Mrows * 768 * 2);
  u16* qk = (u16*)alloc((size_t)Mrows * 1536 * 2);
  u16* Vt = (u16*)alloc((size_t)Mrows * 768 * 2);
  u16* P = (u16*)alloc((size_t)4 * 1024 * 1024 * 2);
  f32x2* ml = (f32x2*)alloc((size_t)4 * 1024 * 8 * 8);
  u16* O = (u16*)alloc((size_t)Mrows * 768 * 2);
  u16* x1 = (u16*)alloc((size_t)Mrows * 768 * 2);
  f32x2* st2 = (f32x2*)alloc((size_t)Mrows * 16 * 8);
  u16* h2 = (u16*)alloc((size_t)Mrows * 768 * 2);
  u16* u = (u16*)alloc((size_t)Mrows * 3072 * 2);

  k0_mod<<<144 + 96, 256, 0, stream>>>(c, wmod, modp, wqkv, wo);
  k_lnmod<<<256 + 192, 256, 0, stream>>>(x, modp, 0, h, wfc1, wfc2);
  k_gemm_big<144, 4, 0><<<256, 768, 0, stream>>>(h, wqkv, qk, Vt);
  k_scores<<<256, 768, 0, stream>>>(qk, P, ml);
  k_gemm_small<0, SNL><<<256, 64 * (4 + SNL), 0, stream>>>(P, Vt, modp, nullptr, ml, nullptr, O);
  k_gemm_small<1, SNL><<<256, 64 * (4 + SNL), 0, stream>>>(O, wo, modp, x, nullptr, st2, x1);
  k_lnmod<<<256, 256, 0, stream>>>(x1, modp, 3 * 768, h2, nullptr, nullptr);
  k_gemm_big<192, 4, 1><<<256, 768, 0, stream>>>(h2, wfc1, u, nullptr);
  k_gemm_small<2, SNL><<<256, 64 * (4 + SNL), 0, stream>>>(u, wfc2, modp, x1, nullptr, nullptr, out);
}
```

```cpp
#include <hip/hip_runtime.h>
#include <hip/hip_bf16.h>
#include <stdint.h>
#include <type_traits>

typedef __attribute__((ext_vector_type(8))) __bf16 bf16x8;
typedef __attribute__((ext_vector_type(4))) __bf16 bf16x4;
typedef __attribute__((ext_vector_type(2))) __bf16 bf16x2;
typedef __attribute__((ext_vector_type(4))) float f32x4;
typedef __attribute__((ext_vector_type(2))) float f32x2;
typedef __attribute__((ext_vector_type(4))) unsigned u32x4;
typedef __attribute__((ext_vector_type(2))) unsigned u32x2;
typedef unsigned short u16;
#define LDS_AS __attribute__((address_space(3)))

#define DEVI __device__ __forceinline__
#define STAMP(K, I)

static constexpr int Bsz = 2, Nseq = 1024, Dm = 768, Ff = 3072, Dh = 384;
static constexpr int Mrows = Bsz * Nseq;

template <int I, int N, class F>
DEVI void static_for(F&& f) {
  if constexpr (I < N) {
    f(std::integral_constant<int, I>{});
    static_for<I + 1, N>(f);
  }
}

DEVI float lo_f(unsigned u) { return __uint_as_float(u << 16); }
DEVI float hi_f(unsigned u) { return __uint_as_float(u & 0xffff0000u); }
DEVI float bf2f(u16 h) { return __uint_as_float(((unsigned)h) << 16); }
DEVI unsigned pack2(float a, float b) {
  bf16x2 v;
  v[0] = (__bf16)a;
  v[1] = (__bf16)b;
  return __builtin_bit_cast(unsigned, v);
}
DEVI float rbf(float a) { return lo_f(pack2(a, 0.f) ); }

template <int N>
DEVI void wait_vm() { asm volatile("s_waitcnt vmcnt(%0)" ::"i"(N) : "memory"); }
DEVI void wait_lgkm0() { asm volatile("s_waitcnt lgkmcnt(0)" ::: "memory"); }
DEVI void bar() { __builtin_amdgcn_s_barrier(); }

DEVI void glds16(const void* sbase, unsigned voff, unsigned lds_dst) {
  unsigned keep;
  asm volatile(
      "s_mov_b32 %0, m0\n\t"
      "s_mov_b32 m0, %3\n\t"
      "s_nop 0\n\t"
      "global_load_lds_dwordx4 %1, %2\n\t"
      "s_mov_b32 m0, %0"
      : "=&s"(keep)
      : "v"(voff), "s"(sbase), "s"(lds_dst)
      : "memory");
}

DEVI void gld16(u32x4& d, const void* p) { asm volatile("global_load_dwordx4 %0, %1, off" : "=v"(d) : "v"(p) : "memory"); }
DEVI void gld8(u32x2& d, const void* p) { asm volatile("global_load_dwordx2 %0, %1, off" : "=v"(d) : "v"(p) : "memory"); }
template <class T>
DEVI void pin(T& v) { asm volatile("" : "+v"(v)); }
DEVI f32x4 asf4(u32x4 v) { return __builtin_bit_cast(f32x4, v); }

DEVI bf16x8 lds_read16(const LDS_AS char* p) { return *(const LDS_AS bf16x8*)p; }
DEVI u32x4 lds_read16u(const LDS_AS char* p) { return *(const LDS_AS u32x4*)p; }
DEVI f32x4 lds_read16f(const LDS_AS char* p) { return *(const LDS_AS f32x4*)p; }
DEVI bf16x4 lds_tr(const LDS_AS char* p) {
  return __builtin_amdgcn_ds_read_tr16_b64_v4bf16((LDS_AS bf16x4*)p);
}
DEVI bf16x8 cat4(bf16x4 a, bf16x4 b) {
  return __builtin_shufflevector(a, b, 0, 1, 2, 3, 4, 5, 6, 7);
}
DEVI f32x4 mfma16(bf16x8 a, bf16x8 b, f32x4 c) {
  return __builtin_amdgcn_mfma_f32_16x16x32_bf16(a, b, c, 0, 0, 0);
}
DEVI int uni(int v) { return __builtin_amdgcn_readfirstlane(v); }

DEVI u32x4 pair_pack(unsigned ax, unsigned ay, unsigned bx, unsigned by) {
  auto r0 = __builtin_amdgcn_permlane16_swap(ax, bx, false, false);
  auto r1 = __builtin_amdgcn_permlane16_swap(ay, by, false, false);
  u32x4 o;
  o[0] = r0[0];
  o[1] = r1[0];
  o[2] = r0[1];
  o[3] = r1[1];
  return o;
}

__global__ __launch_bounds__(256) void k0_mod(const u16* __restrict__ c, const u16* __restrict__ wmod,
                                              float* __restrict__ modp, const u16* __restrict__ w0,
                                              const u16* __restrict__ w1) {
  __shared__ float sc[2][384];
  __shared__ float red[32][2][64];
  const int tid = threadIdx.x;
  if (blockIdx.x >= 144) {
    const int t = (blockIdx.x - 144) * 256 + tid;
    u32x4 a = {0u, 0u, 0u, 0u};
    u32x4 v[12];
#pragma unroll
    for (int i = 0; i < 12; ++i) {
      int ci = t + i * 24576;
      const u16* p = (ci < 221184) ? (w0 + (size_t)ci * 8) : (w1 + (size_t)(ci - 221184) * 8);
      v[i] = *(const u32x4*)p;
    }
#pragma unroll
    for (int i = 0; i < 12; ++i) a ^= v[i];
    asm volatile("" ::"v"(a));
    return;
  }
  const int cb = blockIdx.x % 72, kp = blockIdx.x / 72;
  const int tc = tid & 7, tr = tid >> 3;
  u32x4 w[12];
  const u16* wp = wmod + (size_t)(kp * 384 + tr) * 4608 + cb * 64 + tc * 8;
#pragma unroll
  for (int i = 0; i < 12; ++i) w[i] = *(const u32x4*)(wp + (size_t)i * 32 * 4608);
  for (int i = tid; i < 768; i += 256) {
    int b = i / 384, k = i % 384;
    float v = bf2f(c[b * 768 + kp * 384 + k]);
    float s = v / (1.f + __expf(-v));
    sc[b][k] = rbf(s);
  }
  __syncthreads();
  float acc[2][8];
#pragma unroll
  for (int b = 0; b < 2; ++b)
#pragma unroll
    for (int j = 0; j < 8; ++j) acc[b][j] = 0.f;
#pragma unroll
  for (int i = 0; i < 12; ++i) {
    float s0 = sc[0][tr + 32 * i], s1 = sc[1][tr + 32 * i];
#pragma unroll
    for (int j = 0; j < 4; ++j) {
      float a = lo_f(w[i][j]), bb = hi_f(w[i][j]);
      acc[0][2 * j] += s0 * a;
      acc[0][2 * j + 1] += s0 * bb;
      acc[1][2 * j] += s1 * a;
      acc[1][2 * j + 1] += s1 * bb;
    }
  }
#pragma unroll
  for (int b = 0; b < 2; ++b)
#pragma unroll
    for (int j = 0; j < 8; ++j) red[tr][b][tc * 8 + j] = acc[b][j];
  __syncthreads();
  if (tid < 128) {
    int b = tid >> 6, col = tid & 63;
    float s = 0.f;
#pragma unroll
    for (int r = 0; r < 32; ++r) s += red[r][b][col];
    modp[(kp * 2 + b) * 4608 + cb * 64 + col] = s;
  }
}

__global__ __launch_bounds__(256) void k_lnmod(const u16* __restrict__ x, const float* __restrict__ modp, int offS,
                                               u16* __restrict__ h, const u16* __restrict__ w0,
                                               const u16* __restrict__ w1) {
  const int tid = threadIdx.x;
  if (blockIdx.x >= 256) {
    const int t = (blockIdx.x - 256) * 256 + tid;
    u32x4 a = {0u, 0u, 0u, 0u};
    u32x4 v[12];
#pragma unroll
    for (int i = 0; i < 12; ++i) {
      int ci = t + i * 49152;
      const u16* p = (ci < 294912) ? (w0 + (size_t)ci * 8) : (w1 + (size_t)(ci - 294912) * 8);
      v[i] = *(const u32x4*)p;
    }
#pragma unroll
    for (int i = 0; i < 12; ++i) a ^= v[i];
    asm volatile("" ::"v"(a));
    return;
  }
  const int w = tid >> 6, l = tid & 63;
  const int row0 = blockIdx.x * 8 + w * 2;
  const int b = row0 >> 10;
  u32x2 v[2][3];
#pragma unroll
  for (int r = 0; r < 2; ++r) {
    const u16* xp = x + (size_t)(row0 + r) * 768 + l * 4;
#pragma unroll
    for (int i = 0; i < 3; ++i) v[r][i] = *(const u32x2*)(xp + i * 256);
  }
  f32x4 gg[3], tt[3];
#pragma unroll
  for (int i = 0; i < 3; ++i) {
    const float* m0p = modp + (0 * 2 + b) * 4608 + offS + l * 4 + i * 256;
    const float* m1p = modp + (1 * 2 + b) * 4608 + offS + l * 4 + i * 256;
    f32x4 s0 = *(const f32x4*)m0p, s1 = *(const f32x4*)m1p;
    f32x4 c0 = *(const f32x4*)(m0p + 768), c1 = *(const f32x4*)(m1p + 768);
#pragma unroll
    for (int j = 0; j < 4; ++j) {
      tt[i][j] = rbf(s0[j] + s1[j]);
      gg[i][j] = rbf(1.f + rbf(c0[j] + c1[j]));
    }
  }
#pragma unroll
  for (int r = 0; r < 2; ++r) {
    float f[12];
#pragma unroll
    for (int i = 0; i < 3; ++i) {
      f[4 * i] = lo_f(v[r][i][0]);
      f[4 * i + 1] = hi_f(v[r][i][0]);
      f[4 * i + 2] = lo_f(v[r][i][1]);
      f[4 * i + 3] = hi_f(v[r][i][1]);
    }
    float s = 0.f;
#pragma unroll
    for (int i = 0; i < 12; ++i) s += f[i];
#pragma unroll
    for (int o = 32; o > 0; o >>= 1) s += __shfl_xor(s, o);
    float mean = s * (1.f / 768.f);
    float q = 0.f;
#pragma unroll
    for (int i = 0; i < 12; ++i) {
      float d = f[i] - mean;
      q += d * d;
    }
#pragma unroll
    for (int o = 32; o > 0; o >>= 1) q += __shfl_xor(q, o);
    const float mb = rbf(mean);
    const float den = rbf(sqrtf(rbf(rbf(q * (1.f / 768.f)) + 1e-5f)));
    u16* op = h + (size_t)(row0 + r) * 768 + l * 4;
#pragma unroll
    for (int i = 0; i < 3; ++i) {
      float y[4];
#pragma unroll
      for (int j = 0; j < 4; ++j) y[j] = rbf(rbf(rbf(f[4 * i + j] - mb) / den) * gg[i][j]) + tt[i][j];
      u32x2 o;
      o[0] = pack2(y[0], y[1]);
      o[1] = pack2(y[2], y[3]);
      *(u32x2*)(op + i * 256) = o;
    }
  }
}

template <int BN, int NS, int EPI>
__global__ __launch_bounds__(768, 3) void k_gemm_big(const u16* __restrict__ A, const u16* __restrict__ W,
                                                     void* __restrict__ outv, u16* __restrict__ out2) {
  constexpr int NC = BN / 16;
  constexpr int LDW = (EPI == 0) ? 2304 : (EPI == 1 ? 3072 : 768);
  constexpr int LDA = (EPI == 2) ? 3072 : 768;
  constexpr int ASZ = 128 * 128;
  constexpr int BSZ = 8 * NC * 256;
  constexpr int STG = ASZ + BSZ;
  constexpr int SUBW = 2 * NC;
  constexpr int NBI = (SUBW + 3) / 4;
  constexpr int NLD = 4 + NBI;
  constexpr int NK = 768 / 64;
  __shared__ __attribute__((aligned(16))) char smem_raw[NS * STG];
  LDS_AS char* smem = (LDS_AS char*)smem_raw;
  const unsigned lds0 = (unsigned)(uintptr_t)smem;
  constexpr int KID = (EPI == 0) ? 1 : (EPI == 1 ? 6 : 5);
  STAMP(KID, 0);
  const int tid = threadIdx.x;
  const int l = tid & 63;
  const int w = uni(tid >> 6);
  const int bid = blockIdx.x;
  const int xcd = bid & 7, idx = bid >> 3;
  int tm, tn, ks = 0;
  if constexpr (EPI == 2) {
    tm = (xcd >> 1) * 4 + (idx & 3);
    tn = (idx >> 2) & 3;
    ks = (xcd & 1) * 2 + (idx >> 4);
  } else {
    tm = (xcd >> 1) * 4 + (idx & 3);
    tn = (xcd & 1) * 8 + (idx >> 2);
  }
  const int m0 = tm * 128, n0 = tn * BN;

  if (w >= 8) {
    const int lw = w - 8;
    const unsigned voffA = (unsigned)((l >> 3) * LDA * 2 + (((l & 7) ^ (l >> 3)) << 4));
    unsigned voffB[NBI];
#pragma unroll
    for (int i = 0; i < NBI; ++i) {
      int sub = SUBW * lw + 4 * i + (l >> 4);
      if (sub >= SUBW * (lw + 1)) sub = SUBW * (lw + 1) - 1;
      int kb = sub / NC, cc = sub % NC;
      int within = l & 15;
      int r = (within >> 1) ^ ((kb & 1) << 2);
      voffB[i] = (unsigned)(((kb * 8 + r) * LDW + cc * 16 + (within & 1) * 8) * 2);
    }
    const char* abase = (const char*)(A + (size_t)(m0 + 32 * lw) * LDA + ks * 768);
    const char* bbase = (const char*)(W + (size_t)ks * 768 * LDW + n0);
    auto issue = [&](int kt, int st) {
      const unsigned sa = lds0 + st * STG;
#pragma unroll
      for (int i = 0; i < 4; ++i)
        glds16(abase + (size_t)i * 8 * LDA * 2 + kt * 128, voffA, sa + (32 * lw + 8 * i) * 128);
      const char* sb = bbase + (size_t)kt * 64 * LDW * 2;
#pragma unroll
      for (int i = 0; i < NBI; ++i) {
        unsigned dst = sa + ASZ + (SUBW * lw + 4 * i) * 256;
        if (4 * i + 4 <= SUBW) {
          glds16(sb, voffB[i], dst);
        } else {
          if (l < (SUBW - 4 * i) * 16) glds16(sb, voffB[i], dst);
        }
      }
    };
#pragma unroll
    for (int s = 0; s < NS - 1; ++s) issue(s, s);
    int stn = NS - 1;
#pragma unroll 1
    for (int kt = 0; kt < NK; ++kt) {
      if (kt < NK - (NS - 2)) wait_vm<(NS - 2) * NLD>();
      else wait_vm<0>();
      bar();
      if (kt + NS - 1 < NK) issue(kt + NS - 1, stn);
      stn = (stn + 1 == NS) ? 0 : stn + 1;
    }
    bar();
    bar();
    return;
  }

  if constexpr (EPI == 1) {
    constexpr int NH = NC / 2;
    const int wm = w & 3, wn = w >> 2;
    const int g = l >> 4;
    const unsigned aoff = (unsigned)((32 * wm + (l & 15)) * 128 + ((g ^ (l & 7)) << 4));
    const int q = (l & 15) >> 2, p = l & 3;
    const unsigned boff_t0 = (unsigned)(ASZ + (g * NC + wn * NH) * 256 + ((4 * (g & 1) + q) * 32) + p * 8);
    const unsigned boff_t1 = (unsigned)(ASZ + (g * NC + wn * NH) * 256 + ((4 * (1 - (g & 1)) + q) * 32) + p * 8);
    f32x4 acc[2][NH];
#pragma unroll
    for (int i = 0; i < 2; ++i)
#pragma unroll
      for (int j = 0; j < NH; ++j) acc[i][j] = (f32x4){0.f, 0.f, 0.f, 0.f};
    STAMP(KID, 1);
    int st = 0;
#pragma unroll 1
    for (int kt = 0; kt < NK; ++kt) {
      wait_lgkm0();
      bar();
      const LDS_AS char* sp = smem + st * STG;
#pragma unroll
      for (int ks2 = 0; ks2 < 2; ++ks2) {
        bf16x8 xb[2];
#pragma unroll
        for (int mf = 0; mf < 2; ++mf) xb[mf] = lds_read16(sp + (aoff ^ (ks2 * 64)) + mf * 2048);
#pragma unroll
        for (int cc = 0; cc < NH; ++cc) {
          bf16x4 w0 = lds_tr(sp + boff_t0 + ks2 * 4 * NC * 256 + cc * 256);
          bf16x4 w1 = lds_tr(sp + boff_t1 + ks2 * 4 * NC * 256 + cc * 256);
          bf16x8 wf = cat4(w0, w1);
          acc[0][cc] = mfma16(wf, xb[0], acc[0][cc]);
          acc[1][cc] = mfma16(wf, xb[1], acc[1][cc]);
        }
      }
      st = (st + 1 == NS) ? 0 : st + 1;
    }
    STAMP(KID, 14);
    bar();
    bar();
    u16* out = (u16*)outv;
#pragma unroll
    for (int mf = 0; mf < 2; ++mf) {
      u16* op = out + (size_t)(m0 + 32 * wm + 16 * mf + (l & 15)) * 3072 + n0 + 16 * NH * wn;
      unsigned pk[NH][2];
#pragma unroll
      for (int cc = 0; cc < NH; ++cc) {
        f32x4 v = acc[mf][cc];
        const float c1 = -2.302208198144325f;
        const float c2 = -0.1029432397500235f;
        unsigned y01 = pack2(v[0], v[1]), y23 = pack2(v[2], v[3]);
        float y[4] = {lo_f(y01), hi_f(y01), lo_f(y23), hi_f(y23)};
#pragma unroll
        for (int j = 0; j < 4; ++j) {
          float e = __builtin_amdgcn_exp2f(y[j] * (c1 + c2 * y[j] * y[j]));
          v[j] = y[j] * __builtin_amdgcn_rcpf(1.f + e);
        }
        pk[cc][0] = pack2(v[0], v[1]);
        pk[cc][1] = pack2(v[2], v[3]);
      }
#pragma unroll
      for (int a = 0; a < NH / 2; ++a) {
        u32x4 o = pair_pack(pk[2 * a][0], pk[2 * a][1], pk[2 * a + 1][0], pk[2 * a + 1][1]);
        *(u32x4*)(op + 32 * a + (g & 1) * 16 + (g & 2) * 4) = o;
      }
    }
    STAMP(KID, 15);
    return;
  }

  const int wm = w & 3, kh = w >> 2;
  const int g = l >> 4;
  const unsigned aoff = (unsigned)(((32 * wm + (l & 15)) * 128 + ((g ^ (l & 7)) << 4)) ^ (kh * 64));
  const int q = (l & 15) >> 2, p = l & 3;
  const unsigned boff_t0 = (unsigned)(ASZ + (4 * kh + g) * NC * 256 + ((4 * (g & 1) + q) * 32) + p * 8);
  const unsigned boff_t1 = (unsigned)(ASZ + (4 * kh + g) * NC * 256 + ((4 * (1 - (g & 1)) + q) * 32) + p * 8);

  f32x4 acc[2][NC];
#pragma unroll
  for (int i = 0; i < 2; ++i)
#pragma unroll
    for (int j = 0; j < NC; ++j) acc[i][j] = (f32x4){0.f, 0.f, 0.f, 0.f};
  STAMP(KID, 1);

  int st = 0;
#pragma unroll 1
  for (int kt = 0; kt < NK; ++kt) {
    wait_lgkm0();
    bar();
    const LDS_AS char* sp = smem + st * STG;
    bf16x8 xb[2];
#pragma unroll
    for (int mf = 0; mf < 2; ++mf) xb[mf] = lds_read16(sp + aoff + mf * 2048);
#pragma unroll
    for (int cc = 0; cc < NC; ++cc) {
      bf16x4 w0 = lds_tr(sp + boff_t0 + cc * 256);
      bf16x4 w1 = lds_tr(sp + boff_t1 + cc * 256);
      bf16x8 wf = cat4(w0, w1);
      acc[0][cc] = mfma16(wf, xb[0], acc[0][cc]);
      acc[1][cc] = mfma16(wf, xb[1], acc[1][cc]);
    }
    st = (st + 1 == NS) ? 0 : st + 1;
  }
  STAMP(KID, 14);

  wait_lgkm0();
  bar();
  {
    LDS_AS char* rp = smem + (wm * 2 + kh) * NC * 1024 + l * 16;
    if (kh) {
#pragma unroll
      for (int cc = 0; cc < NC; ++cc) {
        *(LDS_AS f32x4*)(rp + cc * 1024) = acc[0][cc];
        acc[0][cc] = acc[1][cc];
      }
    } else {
#pragma unroll
      for (int cc = 0; cc < NC; ++cc) *(LDS_AS f32x4*)(rp + cc * 1024) = acc[1][cc];
    }
  }
  wait_lgkm0();
  bar();
  const int row = m0 + 32 * wm + 16 * kh + (l & 15);
  const LDS_AS char* rp = smem + (wm * 2 + (1 - kh)) * NC * 1024 + l * 16;
  if constexpr (EPI == 2) {
    float* op = (float*)outv + ((size_t)ks * 2048 + row) * 768 + n0 + 4 * g;
#pragma unroll
    for (int cc = 0; cc < NC; ++cc) *(f32x4*)(op + 16 * cc) = acc[0][cc] + lds_read16f(rp + cc * 1024);
  } else {
    u16* out = (u16*)outv;
    unsigned pk[NC][2];
#pragma unroll
    for (int cc = 0; cc < NC; ++cc) {
      f32x4 v = acc[0][cc] + lds_read16f(rp + cc * 1024);
      if constexpr (EPI == 1) {
        const float c1 = -2.302208198144325f;
        const float c2 = -0.1029432397500235f;
        unsigned y01 = pack2(v[0], v[1]), y23 = pack2(v[2], v[3]);
        float y[4] = {lo_f(y01), hi_f(y01), lo_f(y23), hi_f(y23)};
#pragma unroll
        for (int j = 0; j < 4; ++j) {
          float e = __builtin_amdgcn_exp2f(y[j] * (c1 + c2 * y[j] * y[j]));
          v[j] = y[j] * __builtin_amdgcn_rcpf(1.f + e);
        }
      }
      pk[cc][0] = pack2(v[0], v[1]);
      pk[cc][1] = pack2(v[2], v[3]);
    }
    auto dst_ptr = [&](int n) -> u16* {
      if constexpr (EPI == 1) {
        return out + (size_t)row * 3072 + n;
      } else {
        if (n < 1536) return out + (size_t)row * 1536 + n;
        int nv = n - 1536;
        int hd = nv / 96;
        int dd = nv - hd * 96;
        return out2 + ((size_t)(((row >> 10) * 8 + hd) * 1024 + (row & 1023))) * 96 + dd;
      }
    };
#pragma unroll
    for (int a = 0; a < NC / 2; ++a) {
      u32x4 o = pair_pack(pk[2 * a][0], pk[2 * a][1], pk[2 * a + 1][0], pk[2 * a + 1][1]);
      *(u32x4*)dst_ptr(n0 + 32 * a + (g & 1) * 16 + (g & 2) * 4) = o;
    }
    if constexpr (NC & 1) {
      u32x2 o;
      o[0] = pk[NC - 1][0];
      o[1] = pk[NC - 1][1];
      *(u32x2*)dst_ptr(n0 + 16 * (NC - 1) + 4 * g) = o;
    }
  }
  STAMP(KID, 15);
}

__global__ __launch_bounds__(768, 3) void k_scores(const u16* __restrict__ qk, u16* __restrict__ P,
                                                   f32x2* __restrict__ ml) {
  constexpr int NS = 4;
  constexpr int ASZ = 128 * 128;
  constexpr int STG = 2 * ASZ;
  constexpr int NK = 384 / 64;
  constexpr int NLD = 8;
  constexpr int LDQ = 1536;
  __shared__ __attribute__((aligned(16))) char smem_raw[NS * STG];
  LDS_AS char* smem = (LDS_AS char*)smem_raw;
  const unsigned lds0 = (unsigned)(uintptr_t)smem;
  STAMP(2, 0);
  const int tid = threadIdx.x, l = tid & 63;
  const int w = uni(tid >> 6);
  const int bid = blockIdx.x;
  const int xcd = bid & 7, idx = bid >> 3;
  const int bh = xcd >> 1;
  const int tq = (xcd & 1) * 4 + (idx & 3);
  const int tk = idx >> 2;
  const int b = bh >> 1, h = bh & 1;

  if (w >= 8) {
    const int lw = w - 8;
    const unsigned voff = (unsigned)((l >> 3) * LDQ * 2 + (((l & 7) ^ (l >> 3)) << 4));
    const char* qbase = (const char*)(qk + (size_t)(b * 1024 + tq * 128 + 32 * lw) * LDQ + h * 384);
    const char* kbase = (const char*)(qk + (size_t)(b * 1024 + tk * 128 + 32 * lw) * LDQ + 768 + h * 384);
    auto issue = [&](int kt, int st) {
      const unsigned sa = lds0 + st * STG;
#pragma unroll
      for (int i = 0; i < 4; ++i)
        glds16(qbase + (size_t)i * 8 * LDQ * 2 + kt * 128, voff, sa + (32 * lw + 8 * i) * 128);
#pragma unroll
      for (int i = 0; i < 4; ++i)
        glds16(kbase + (size_t)i * 8 * LDQ * 2 + kt * 128, voff, sa + ASZ + (32 * lw + 8 * i) * 128);
    };
#pragma unroll
    for (int s = 0; s < NS - 1; ++s) issue(s, s);
    int stn = NS - 1;
#pragma unroll 1
    for (int kt = 0; kt < NK; ++kt) {
      if (kt < NK - (NS - 2)) wait_vm<(NS - 2) * NLD>();
      else wait_vm<0>();
      bar();
      if (kt + NS - 1 < NK) issue(kt + NS - 1, stn);
      stn = (stn + 1 == NS) ? 0 : stn + 1;
    }
    bar();
    bar();
    return;
  }

  const int wm = w & 3, kh = w >> 2;
  const int g = l >> 4;
  const unsigned qoff = (unsigned)(((32 * wm + (l & 15)) * 128 + ((g ^ (l & 7)) << 4)) ^ (kh * 64));
  const unsigned koff = (unsigned)(ASZ + (((l & 15) * 128 + ((g ^ (l & 7)) << 4)) ^ (kh * 64)));
  f32x4 acc[2][8];
#pragma unroll
  for (int i = 0; i < 2; ++i)
#pragma unroll
    for (int j = 0; j < 8; ++j) acc[i][j] = (f32x4){0.f, 0.f, 0.f, 0.f};

  int st = 0;
#pragma unroll 1
  for (int kt = 0; kt < NK; ++kt) {
    wait_lgkm0();
    bar();
    const LDS_AS char* sp = smem + st * STG;
    bf16x8 qf[2];
#pragma unroll
    for (int mf = 0; mf < 2; ++mf) qf[mf] = lds_read16(sp + qoff + mf * 2048);
#pragma unroll
    for (int kf = 0; kf < 8; ++kf) {
      bf16x8 kfr = lds_read16(sp + koff + kf * 2048);
      acc[0][kf] = mfma16(kfr, qf[0], acc[0][kf]);
      acc[1][kf] = mfma16(kfr, qf[1], acc[1][kf]);
    }
    st = (st + 1 == NS) ? 0 : st + 1;
  }
  STAMP(2, 14);
  wait_lgkm0();
  bar();
  {
    LDS_AS char* rp = smem + (wm * 2 + kh) * 8 * 1024 + l * 16;
    if (kh) {
#pragma unroll
      for (int kf = 0; kf < 8; ++kf) {
        *(LDS_AS f32x4*)(rp + kf * 1024) = acc[0][kf];
        acc[0][kf] = acc[1][kf];
      }
    } else {
#pragma unroll
      for (int kf = 0; kf < 8; ++kf) *(LDS_AS f32x4*)(rp + kf * 1024) = acc[1][kf];
    }
  }
  wait_lgkm0();
  bar();
  f32x4 s[8];
  {
    const LDS_AS char* rp = smem + (wm * 2 + (1 - kh)) * 8 * 1024 + l * 16;
#pragma unroll
    for (int kf = 0; kf < 8; ++kf) {
      f32x4 v = acc[0][kf] + lds_read16f(rp + kf * 1024);
      unsigned a01 = pack2(v[0], v[1]), a23 = pack2(v[2], v[3]);
      a01 = pack2(lo_f(a01) * 0.051031036307982884f, hi_f(a01) * 0.051031036307982884f);
      a23 = pack2(lo_f(a23) * 0.051031036307982884f, hi_f(a23) * 0.051031036307982884f);
      s[kf] = (f32x4){lo_f(a01), hi_f(a01), lo_f(a23), hi_f(a23)};
    }
  }
  const float l2e = 1.4426950408889634f;
  float mx = -INFINITY;
#pragma unroll
  for (int kf = 0; kf < 8; ++kf)
#pragma unroll
    for (int j = 0; j < 4; ++j) mx = fmaxf(mx, s[kf][j]);
  mx = fmaxf(mx, __shfl_xor(mx, 16));
  mx = fmaxf(mx, __shfl_xor(mx, 32));
  const float mbl = mx * l2e;
  float sum = 0.f;
  unsigned pk[8][2];
#pragma unroll
  for (int kf = 0; kf < 8; ++kf) {
    float e[4];
#pragma unroll
    for (int j = 0; j < 4; ++j) {
      e[j] = __builtin_amdgcn_exp2f(s[kf][j] * l2e - mbl);
      sum += e[j];
    }
    pk[kf][0] = pack2(e[0], e[1]);
    pk[kf][1] = pack2(e[2], e[3]);
  }
  sum += __shfl_xor(sum, 16);
  sum += __shfl_xor(sum, 32);
  const int qrow = tq * 128 + 32 * wm + 16 * kh + (l & 15);
  u16* op = P + ((size_t)bh * 1024 + qrow) * 1024 + tk * 128;
#pragma unroll
  for (int a = 0; a < 4; ++a) {
    u32x4 o = pair_pack(pk[2 * a][0], pk[2 * a][1], pk[2 * a + 1][0], pk[2 * a + 1][1]);
    *(u32x4*)(op + 32 * a + (g & 1) * 16 + (g & 2) * 4) = o;
  }
  if (g == 0) {
    f32x2 s2;
    s2[0] = mbl;
    s2[1] = sum;
    ml[((size_t)bh * 1024 + qrow) * 8 + tk] = s2;
  }
  STAMP(2, 15);
}

template <int MODE, int NL>
__global__ __launch_bounds__(64 * (4 + NL), NL / 4 + 1) void k_gemm_small(const u16* __restrict__ Ain, const u16* __restrict__ Bin,
                                                       const float* __restrict__ modp,
                                                       const u16* __restrict__ resid,
                                                       const f32x2* __restrict__ ml, f32x2* __restrict__ stats,
                                                       u16* __restrict__ out) {
  constexpr int KTOT = (MODE == 0) ? 1024 : (MODE == 1 ? 768 : 3072);
  constexpr int KH = KTOT / 2;
  constexpr int NK = KH / 64;
  constexpr int LDA = KTOT;
  constexpr int LDB = (MODE == 0) ? 96 : 768;
  constexpr int NS = 4;
  constexpr int STG = 40960;
  constexpr int NLD = 40 / NL;
  constexpr int AI = 16 / NL, BI = 24 / NL;
  __shared__ __attribute__((aligned(16))) char smem_raw[NS * STG];
  LDS_AS char* smem = (LDS_AS char*)smem_raw;
  const unsigned lds0 = (unsigned)(uintptr_t)smem;
  STAMP(3 + MODE, 0);
  const int tid = threadIdx.x, l = tid & 63;
  const int w = uni(tid >> 6);
  const int bid = blockIdx.x;
  const int xcd = bid & 7, idx = bid >> 3;
  const int g = l >> 4;

  int m0, n0, bh = 0;
  const u16* Ap;
  const u16* Bp;
  if constexpr (MODE == 0) {
    bh = xcd >> 1;
    const int tq = (xcd & 1) * 8 + (idx & 7);
    const int td = idx >> 3;
    m0 = tq * 64;
    n0 = td * 96;
    Ap = Ain + ((size_t)bh * 1024 + m0) * 1024;
    Bp = Bin + ((size_t)(bh * 4 + td) * 1024) * 96;
  } else {
    const int tm = (xcd >> 1) * 8 + (idx & 7);
    const int tn = (xcd & 1) * 4 + (idx >> 3);
    m0 = tm * 64;
    n0 = tn * 96;
    Ap = Ain + (size_t)m0 * LDA;
    Bp = Bin + n0;
  }

  if (w >= 4) {
    const int lw = w - 4;
    const int kq = lw & 1, part = lw >> 1;
    const unsigned voffA = (unsigned)((l >> 3) * LDA * 2 + (((l & 7) ^ (l >> 3)) << 4));
    unsigned voffB[BI];
#pragma unroll
    for (int i = 0; i < BI; ++i) {
      int sub = 4 * BI * part + 4 * i + (l >> 4);
      int kb = sub / 6, cc = sub % 6;
      int within = l & 15;
      int r = (within >> 1) ^ ((kb & 1) << 2);
      voffB[i] = (unsigned)(((kb * 8 + r) * LDB + cc * 16 + (within & 1) * 8) * 2);
    }
    const char* abase = (const char*)(Ap + (size_t)(8 * AI * part) * LDA + kq * KH);
    const char* bbase = (const char*)(Bp + (size_t)(kq * KH) * LDB);
    auto issue = [&](int kt, int st) {
      const unsigned sa = lds0 + st * STG;
#pragma unroll
      for (int i = 0; i < AI; ++i)
        glds16(abase + (size_t)i * 8 * LDA * 2 + kt * 128, voffA, sa + kq * 8192 + (8 * AI * part + 8 * i) * 128);
      const char* sb = bbase + (size_t)kt * 64 * LDB * 2;
#pragma unroll
      for (int i = 0; i < BI; ++i) glds16(sb, voffB[i], sa + 16384 + kq * 12288 + (4 * BI * part + 4 * i) * 256);
    };
#pragma unroll
    for (int s = 0; s < NS - 1; ++s) issue(s, s);
    int stn = NS - 1;
#pragma unroll 1
    for (int kt = 0; kt < NK; ++kt) {
      if (kt < NK - (NS - 2)) wait_vm<(NS - 2) * NLD>();
      else wait_vm<0>();
      bar();
      if (kt + NS - 1 < NK) issue(kt + NS - 1, stn);
      stn = (stn + 1 == NS) ? 0 : stn + 1;
    }
    bar();
    bar();
    return;
  }

  const int kq = w & 1, nh = w >> 1;
  float cf[4][4];
  if constexpr (MODE == 0) {
#pragma unroll
    for (int mf = 0; mf < 4; ++mf) {
      const f32x4* sp = (const f32x4*)(ml + ((size_t)bh * 1024 + m0 + 16 * mf + (l & 15)) * 8);
      f32x4 sv[4];
#pragma unroll
      for (int j = 0; j < 4; ++j) sv[j] = sp[j];
      float mx = fmaxf(sv[0][0], sv[0][2]);
#pragma unroll
      for (int j = 1; j < 4; ++j) mx = fmaxf(mx, fmaxf(sv[j][0], sv[j][2]));
      float e[8];
      float lsum = 0.f;
#pragma unroll
      for (int j = 0; j < 4; ++j) {
        e[2 * j] = __builtin_amdgcn_exp2f(sv[j][0] - mx);
        e[2 * j + 1] = __builtin_amdgcn_exp2f(sv[j][2] - mx);
        lsum += e[2 * j] * sv[j][1] + e[2 * j + 1] * sv[j][3];
      }
      float inv = 1.f / lsum;
      if (kq) {
#pragma unroll
        for (int j = 0; j < 4; ++j) cf[mf][j] = e[4 + j] * inv;
      } else {
#pragma unroll
        for (int j = 0; j < 4; ++j) cf[mf][j] = e[j] * inv;
      }
    }
  }
  f32x4 gq[2][3][2];
  u32x2 xr[2][3];
  if constexpr (MODE != 0) {
    const int goff = (MODE == 1) ? 2 * 768 : 5 * 768;
    const int b = m0 >> 10;
    const float* mp0 = modp + (0 * 2 + b) * 4608 + goff + n0 + 48 * nh + 4 * g;
    const float* mp1 = modp + (1 * 2 + b) * 4608 + goff + n0 + 48 * nh + 4 * g;
#pragma unroll
    for (int cc = 0; cc < 3; ++cc) {
      gq[0][cc][0] = *(const f32x4*)(mp0 + 16 * cc);
      gq[0][cc][1] = *(const f32x4*)(mp1 + 16 * cc);
    }
#pragma unroll
    for (int mf = 0; mf < 2; ++mf) {
      const u16* rp = resid + (size_t)(m0 + 32 * kq + 16 * mf + (l & 15)) * 768 + n0 + 48 * nh + 4 * g;
#pragma unroll
      for (int cc = 0; cc < 3; ++cc) xr[mf][cc] = *(const u32x2*)(rp + 16 * cc);
    }
  }

  const unsigned aoff0 = (unsigned)(kq * 8192 + (l & 15) * 128 + ((g ^ (l & 7)) << 4));
  const int q = (l & 15) >> 2, p = l & 3;
  const unsigned boff_t0 = (unsigned)(16384 + kq * 12288 + (g * 6 + 3 * nh) * 256 + ((4 * (g & 1) + q) * 32) + p * 8);
  const unsigned boff_t1 =
      (unsigned)(16384 + kq * 12288 + (g * 6 + 3 * nh) * 256 + ((4 * (1 - (g & 1)) + q) * 32) + p * 8);

  f32x4 acc[4][3];
  f32x4 tot[4][3];
#pragma unroll
  for (int i = 0; i < 4; ++i)
#pragma unroll
    for (int j = 0; j < 3; ++j) {
      acc[i][j] = (f32x4){0.f, 0.f, 0.f, 0.f};
      tot[i][j] = (f32x4){0.f, 0.f, 0.f, 0.f};
    }
  STAMP(3 + MODE, 1);

  int st = 0;
#pragma unroll 1
  for (int kt = 0; kt < NK; ++kt) {
    wait_lgkm0();
    bar();
    const LDS_AS char* sp = smem + st * STG;
#pragma unroll
    for (int ks = 0; ks < 2; ++ks) {
      bf16x8 af[4];
#pragma unroll
      for (int mf = 0; mf < 4; ++mf) af[mf] = lds_read16(sp + ((aoff0 ^ (ks * 64)) + mf * 2048));
#pragma unroll
      for (int cc = 0; cc < 3; ++cc) {
        bf16x4 w0 = lds_tr(sp + boff_t0 + ks * 6144 + cc * 256);
        bf16x4 w1 = lds_tr(sp + boff_t1 + ks * 6144 + cc * 256);
        bf16x8 wf = cat4(w0, w1);
#pragma unroll
        for (int mf = 0; mf < 4; ++mf) acc[mf][cc] = mfma16(wf, af[mf], acc[mf][cc]);
      }
    }
    if constexpr (MODE == 0) {
      if (kt & 1) {
#pragma unroll
        for (int mf = 0; mf < 4; ++mf) {
          const float c = cf[mf][0];
          cf[mf][0] = cf[mf][1];
          cf[mf][1] = cf[mf][2];
          cf[mf][2] = cf[mf][3];
#pragma unroll
          for (int cc = 0; cc < 3; ++cc) {
            tot[mf][cc] += acc[mf][cc] * c;
            acc[mf][cc] = (f32x4){0.f, 0.f, 0.f, 0.f};
          }
        }
      }
    }
    st = (st + 1 == NS) ? 0 : st + 1;
  }
  if constexpr (MODE == 0) {
#pragma unroll
    for (int mf = 0; mf < 4; ++mf)
#pragma unroll
      for (int cc = 0; cc < 3; ++cc) acc[mf][cc] = tot[mf][cc];
  }
  STAMP(3 + MODE, 12);
  wait_lgkm0();
  bar();
  {
    LDS_AS char* rp = smem + (nh * 2 + kq) * 6144 + l * 16;
    if (kq) {
#pragma unroll
      for (int mf = 0; mf < 2; ++mf)
#pragma unroll
        for (int cc = 0; cc < 3; ++cc) {
          *(LDS_AS f32x4*)(rp + (mf * 3 + cc) * 1024) = acc[mf][cc];
          acc[mf][cc] = acc[2 + mf][cc];
        }
    } else {
#pragma unroll
      for (int mf = 0; mf < 2; ++mf)
#pragma unroll
        for (int cc = 0; cc < 3; ++cc) *(LDS_AS f32x4*)(rp + (mf * 3 + cc) * 1024) = acc[2 + mf][cc];
    }
  }
  wait_lgkm0();
  bar();
  f32x4 r[2][3];
  {
    const LDS_AS char* rp = smem + (nh * 2 + (1 - kq)) * 6144 + l * 16;
#pragma unroll
    for (int mf = 0; mf < 2; ++mf)
#pragma unroll
      for (int cc = 0; cc < 3; ++cc)
        r[mf][cc] = acc[mf][cc] + lds_read16f(rp + (mf * 3 + cc) * 1024);
  }
  STAMP(3 + MODE, 13);
#pragma unroll
  for (int mf = 0; mf < 2; ++mf) {
    const int mrow = m0 + 32 * kq + 16 * mf + (l & 15);
    float v[3][4];
    u16* op;
    if constexpr (MODE == 0) {
      op = out + (size_t)((bh >> 1) * 1024 + mrow) * 768 + (bh & 1) * 384 + n0 + 48 * nh;
#pragma unroll
      for (int cc = 0; cc < 3; ++cc)
#pragma unroll
        for (int j = 0; j < 4; ++j) v[cc][j] = r[mf][cc][j];
    } else {
      op = out + (size_t)mrow * 768 + n0 + 48 * nh;
#pragma unroll
      for (int cc = 0; cc < 3; ++cc) {
        float xv[4] = {lo_f(xr[mf][cc][0]), hi_f(xr[mf][cc][0]), lo_f(xr[mf][cc][1]), hi_f(xr[mf][cc][1])};
#pragma unroll
        for (int j = 0; j < 4; ++j) {
          float gate = rbf(gq[0][cc][0][j] + gq[0][cc][1][j]);
          v[cc][j] = rbf(xv[j] + rbf(gate * rbf(r[mf][cc][j])));
        }
      }
    }
    {
      u32x4 o = pair_pack(pack2(v[0][0], v[0][1]), pack2(v[0][2], v[0][3]), pack2(v[1][0], v[1][1]),
                          pack2(v[1][2], v[1][3]));
      *(u32x4*)(op + (g & 1) * 16 + (g & 2) * 4) = o;
      u32x2 o2;
      o2[0] = pack2(v[2][0], v[2][1]);
      o2[1] = pack2(v[2][2], v[2][3]);
      *(u32x2*)(op + 32 + 4 * g) = o2;
    }
  }
  STAMP(3 + MODE, 15);
}

#define SNL 4
extern "C" void kernel_launch(void* const* d_in, const int* in_sizes, int n_in, void* d_out, int out_size,
                              void* d_ws, size_t ws_size, hipStream_t stream) {
  const u16* x = (const u16*)d_in[0];
  const u16* c = (const u16*)d_in[1];
  const u16* wqkv = (const u16*)d_in[2];
  const u16* wo = (const u16*)d_in[3];
  const u16* wmod = (const u16*)d_in[4];
  const u16* wfc1 = (const u16*)d_in[5];
  const u16* wfc2 = (const u16*)d_in[6];
  u16* out = (u16*)d_out;

  char* ws = (char*)d_ws;
  size_t off = 0;
  auto alloc = [&](size_t bytes) {
    char* p = ws + off;
    off += (bytes + 255) & ~(size_t)255;
    return p;
  };
  float* modp = (float*)alloc(2 * 2 * 4608 * 4);
  u16* h = (u16*)alloc((size_t)Mrows * 768 * 2);
  u16* qk = (u16*)alloc((size_t)Mrows * 1536 * 2);
  u16* Vt = (u16*)alloc((size_t)Mrows * 768 * 2);
  u16* P = (u16*)alloc((size_t)4 * 1024 * 1024 * 2);
  f32x2* ml = (f32x2*)alloc((size_t)4 * 1024 * 8 * 8);
  u16* O = (u16*)alloc((size_t)Mrows * 768 * 2);
  u16* x1 = (u16*)alloc((size_t)Mrows * 768 * 2);
  f32x2* st2 = (f32x2*)alloc((size_t)Mrows * 16 * 8);
  u16* h2 = (u16*)alloc((size_t)Mrows * 768 * 2);
  u16* u = (u16*)alloc((size_t)Mrows * 3072 * 2);

  k0_mod<<<144 + 96, 256, 0, stream>>>(c, wmod, modp, wqkv, wo);
  k_lnmod<<<256 + 192, 256, 0, stream>>>(x, modp, 0, h, wfc1, wfc2);
  k_gemm_big<144, 4, 0><<<256, 768, 0, stream>>>(h, wqkv, qk, Vt);
  k_scores<<<256, 768, 0, stream>>>(qk, P, ml);
  k_gemm_small<0, SNL><<<256, 64 * (4 + SNL), 0, stream>>>(P, Vt, modp, nullptr, ml, nullptr, O);
  k_gemm_small<1, SNL><<<256, 64 * (4 + SNL), 0, stream>>>(O, wo, modp, x, nullptr, st2, x1);
  k_lnmod<<<256, 256, 0, stream>>>(x1, modp, 3 * 768, h2, nullptr, nullptr);
  k_gemm_big<192, 4, 1><<<256, 768, 0, stream>>>(h2, wfc1, u, nullptr);
  k_gemm_small<2, SNL><<<256, 64 * (4 + SNL), 0, stream>>>(u, wfc2, modp, x1, nullptr, nullptr, out);
}
```

```cpp
#include <hip/hip_runtime.h>
#include <hip/hip_bf16.h>
#include <stdint.h>
#include <type_traits>

typedef __attribute__((ext_vector_type(8))) __bf16 bf16x8;
typedef __attribute__((ext_vector_type(4))) __bf16 bf16x4;
typedef __attribute__((ext_vector_type(2))) __bf16 bf16x2;
typedef __attribute__((ext_vector_type(4))) float f32x4;
typedef __attribute__((ext_vector_type(2))) float f32x2;
typedef __attribute__((ext_vector_type(4))) unsigned u32x4;
typedef __attribute__((ext_vector_type(2))) unsigned u32x2;
typedef unsigned short u16;
#define LDS_AS __attribute__((address_space(3)))

#define DEVI __device__ __forceinline__

static constexpr int Bsz = 2, Nseq = 1024, Dm = 768, Ff = 3072, Dh = 384;
static constexpr int Mrows = Bsz * Nseq;

template <int I, int N, class F>
DEVI void static_for(F&& f) {
  if constexpr (I < N) {
    f(std::integral_constant<int, I>{});
    static_for<I + 1, N>(f);
  }
}

DEVI float lo_f(unsigned u) { return __uint_as_float(u << 16); }
DEVI float hi_f(unsigned u) { return __uint_as_float(u & 0xffff0000u); }
DEVI float bf2f(u16 h) { return __uint_as_float(((unsigned)h) << 16); }
DEVI unsigned pack2(float a, float b) {
  bf16x2 v;
  v[0] = (__bf16)a;
  v[1] = (__bf16)b;
  return __builtin_bit_cast(unsigned, v);
}
DEVI float rbf(float a) { return lo_f(pack2(a, 0.f) ); }

template <int N>
DEVI void wait_vm() { asm volatile("s_waitcnt vmcnt(%0)" ::"i"(N) : "memory"); }
DEVI void wait_lgkm0() { asm volatile("s_waitcnt lgkmcnt(0)" ::: "memory"); }
DEVI void bar() { __builtin_amdgcn_s_barrier(); }

DEVI void glds16(const void* sbase, unsigned voff, unsigned lds_dst) {
  unsigned keep;
  asm volatile(
      "s_mov_b32 %0, m0\n\t"
      "s_mov_b32 m0, %3\n\t"
      "s_nop 0\n\t"
      "global_load_lds_dwordx4 %1, %2\n\t"
      "s_mov_b32 m0, %0"
      : "=&s"(keep)
      : "v"(voff), "s"(sbase), "s"(lds_dst)
      : "memory");
}

DEVI void gld16(u32x4& d, const void* p) { asm volatile("global_load_dwordx4 %0, %1, off" : "=v"(d) : "v"(p) : "memory"); }
DEVI void gld8(u32x2& d, const void* p) { asm volatile("global_load_dwordx2 %0, %1, off" : "=v"(d) : "v"(p) : "memory"); }
template <class T>
DEVI void pin(T& v) { asm volatile("" : "+v"(v)); }
DEVI f32x4 asf4(u32x4 v) { return __builtin_bit_cast(f32x4, v); }

DEVI bf16x8 lds_read16(const LDS_AS char* p) { return *(const LDS_AS bf16x8*)p; }
DEVI u32x4 lds_read16u(const LDS_AS char* p) { return *(const LDS_AS u32x4*)p; }
DEVI f32x4 lds_read16f(const LDS_AS char* p) { return *(const LDS_AS f32x4*)p; }
DEVI bf16x4 lds_tr(const LDS_AS char* p) {
  return __builtin_amdgcn_ds_read_tr16_b64_v4bf16((LDS_AS bf16x4*)p);
}
DEVI bf16x8 cat4(bf16x4 a, bf16x4 b) {
  return __builtin_shufflevector(a, b, 0, 1, 2, 3, 4, 5, 6, 7);
}
DEVI f32x4 mfma16(bf16x8 a, bf16x8 b, f32x4 c) {
  return __builtin_amdgcn_mfma_f32_16x16x32_bf16(a, b, c, 0, 0, 0);
}
DEVI int uni(int v) { return __builtin_amdgcn_readfirstlane(v); }

DEVI u32x4 pair_pack(unsigned ax, unsigned ay, unsigned bx, unsigned by) {
  auto r0 = __builtin_amdgcn_permlane16_swap(ax, bx, false, false);
  auto r1 = __builtin_amdgcn_permlane16_swap(ay, by, false, false);
  u32x4 o;
  o[0] = r0[0];
  o[1] = r1[0];
  o[2] = r0[1];
  o[3] = r1[1];
  return o;
}

__global__ __launch_bounds__(256) void k0_mod(const u16* __restrict__ c, const u16* __restrict__ wmod,
                                              float* __restrict__ modp, const u16* __restrict__ w0,
                                              const u16* __restrict__ w1) {
  __shared__ float sc[2][384];
  __shared__ float red[32][2][64];
  const int tid = threadIdx.x;
  if (blockIdx.x >= 144) {
    const int t = (blockIdx.x - 144) * 256 + tid;
    u32x4 a = {0u, 0u, 0u, 0u};
    u32x4 v[12];
#pragma unroll
    for (int i = 0; i < 12; ++i) {
      int ci = t + i * 24576;
      const u16* p = (ci < 221184) ? (w0 + (size_t)ci * 8) : (w1 + (size_t)(ci - 221184) * 8);
      v[i] = *(const u32x4*)p;
    }
#pragma unroll
    for (int i = 0; i < 12; ++i) a ^= v[i];
    asm volatile("" ::"v"(a));
    return;
  }
  const int cb = blockIdx.x % 72, kp = blockIdx.x / 72;
  const int tc = tid & 7, tr = tid >> 3;
  u32x4 w[12];
  const u16* wp = wmod + (size_t)(kp * 384 + tr) * 4608 + cb * 64 + tc * 8;
#pragma unroll
  for (int i = 0; i < 12; ++i) w[i] = *(const u32x4*)(wp + (size_t)i * 32 * 4608);
  for (int i = tid; i < 768; i += 256) {
    int b = i / 384, k = i % 384;
    float v = bf2f(c[b * 768 + kp * 384 + k]);
    float s = v / (1.f + __expf(-v));
    sc[b][k] = rbf(s);
  }
  __syncthreads();
  float acc[2][8];
#pragma unroll
  for (int b = 0; b < 2; ++b)
#pragma unroll
    for (int j = 0; j < 8; ++j) acc[b][j] = 0.f;
#pragma unroll
  for (int i = 0; i < 12; ++i) {
    float s0 = sc[0][tr + 32 * i], s1 = sc[1][tr + 32 * i];
#pragma unroll
    for (int j = 0; j < 4; ++j) {
      float a = lo_f(w[i][j]), bb = hi_f(w[i][j]);
      acc[0][2 * j] += s0 * a;
      acc[0][2 * j + 1] += s0 * bb;
      acc[1][2 * j] += s1 * a;
      acc[1][2 * j + 1] += s1 * bb;
    }
  }
#pragma unroll
  for (int b = 0; b < 2; ++b)
#pragma unroll
    for (int j = 0; j < 8; ++j) red[tr][b][tc * 8 + j] = acc[b][j];
  __syncthreads();
  if (tid < 128) {
    int b = tid >> 6, col = tid & 63;
    float s = 0.f;
#pragma unroll
    for (int r = 0; r < 32; ++r) s += red[r][b][col];
    modp[(kp * 2 + b) * 4608 + cb * 64 + col] = s;
  }
}

__global__ __launch_bounds__(256) void k_lnmod(const u16* __restrict__ x, const float* __restrict__ modp, int offS,
                                               u16* __restrict__ h, const u16* __restrict__ w0,
                                               const u16* __restrict__ w1) {
  const int tid = threadIdx.x;
  if (blockIdx.x >= 256) {
    const int t = (blockIdx.x - 256) * 256 + tid;
    u32x4 a = {0u, 0u, 0u, 0u};
    u32x4 v[12];
#pragma unroll
    for (int i = 0; i < 12; ++i) {
      int ci = t + i * 49152;
      const u16* p = (ci < 294912) ? (w0 + (size_t)ci * 8) : (w1 + (size_t)(ci - 294912) * 8);
      v[i] = *(const u32x4*)p;
    }
#pragma unroll
    for (int i = 0; i < 12; ++i) a ^= v[i];
    asm volatile("" ::"v"(a));
    return;
  }
  const int w = tid >> 6, l = tid & 63;
  const int row0 = blockIdx.x * 8 + w * 2;
  const int b = row0 >> 10;
  u32x2 v[2][3];
#pragma unroll
  for (int r = 0; r < 2; ++r) {
    const u16* xp = x + (size_t)(row0 + r) * 768 + l * 4;
#pragma unroll
    for (int i = 0; i < 3; ++i) v[r][i] = *(const u32x2*)(xp + i * 256);
  }
  f32x4 gg[3], tt[3];
#pragma unroll
  for (int i = 0; i < 3; ++i) {
    const float* m0p = modp + (0 * 2 + b) * 4608 + offS + l * 4 + i * 256;
    const float* m1p = modp + (1 * 2 + b) * 4608 + offS + l * 4 + i * 256;
    f32x4 s0 = *(const f32x4*)m0p, s1 = *(const f32x4*)m1p;
    f32x4 c0 = *(const f32x4*)(m0p + 768), c1 = *(const f32x4*)(m1p + 768);
#pragma unroll
    for (int j = 0; j < 4; ++j) {
      tt[i][j] = rbf(s0[j] + s1[j]);
      gg[i][j] = rbf(1.f + rbf(c0[j] + c1[j]));
    }
  }
#pragma unroll
  for (int r = 0; r < 2; ++r) {
    float f[12];
#pragma unroll
    for (int i = 0; i < 3; ++i) {
      f[4 * i] = lo_f(v[r][i][0]);
      f[4 * i + 1] = hi_f(v[r][i][0]);
      f[4 * i + 2] = lo_f(v[r][i][1]);
      f[4 * i + 3] = hi_f(v[r][i][1]);
    }
    float s = 0.f;
#pragma unroll
    for (int i = 0; i < 12; ++i) s += f[i];
#pragma unroll
    for (int o = 32; o > 0; o >>= 1) s += __shfl_xor(s, o);
    float mean = s * (1.f / 768.f);
    float q = 0.f;
#pragma unroll
    for (int i = 0; i < 12; ++i) {
      float d = f[i] - mean;
      q += d * d;
    }
#pragma unroll
    for (int o = 32; o > 0; o >>= 1) q += __shfl_xor(q, o);
    const float mb = rbf(mean);
    const float den = rbf(sqrtf(rbf(rbf(q * (1.f / 768.f)) + 1e-5f)));
    u16* op = h + (size_t)(row0 + r) * 768 + l * 4;
#pragma unroll
    for (int i = 0; i < 3; ++i) {
      float y[4];
#pragma unroll
      for (int j = 0; j < 4; ++j) y[j] = rbf(rbf(rbf(f[4 * i + j] - mb) / den) * gg[i][j]) + tt[i][j];
      u32x2 o;
      o[0] = pack2(y[0], y[1]);
      o[1] = pack2(y[2], y[3]);
      *(u32x2*)(op + i * 256) = o;
    }
  }
}

template <int BN, int NS, int EPI>
__global__ __launch_bounds__(768, 3) void k_gemm_big(const u16* __restrict__ A, const u16* __restrict__ W,
                                                     void* __restrict__ outv, u16* __restrict__ out2) {
  constexpr int NC = BN / 16;
  constexpr int LDW = (EPI == 0) ? 2304 : (EPI == 1 ? 3072 : 768);
  constexpr int LDA = (EPI == 2) ? 3072 : 768;
  constexpr int ASZ = 128 * 128;
  constexpr int BSZ = 8 * NC * 256;
  constexpr int STG = ASZ + BSZ;
  constexpr int SUBW = 2 * NC;
  constexpr int NBI = (SUBW + 3) / 4;
  constexpr int NLD = 4 + NBI;
  constexpr int NK = 768 / 64;
  __shared__ __attribute__((aligned(16))) char smem_raw[NS * STG];
  LDS_AS char* smem = (LDS_AS char*)smem_raw;
  const unsigned lds0 = (unsigned)(uintptr_t)smem;
  const int tid = threadIdx.x;
  const int l = tid & 63;
  const int w = uni(tid >> 6);
  const int bid = blockIdx.x;
  const int xcd = bid & 7, idx = bid >> 3;
  int tm, tn, ks = 0;
  if constexpr (EPI == 2) {
    tm = (xcd >> 1) * 4 + (idx & 3);
    tn = (idx >> 2) & 3;
    ks = (xcd & 1) * 2 + (idx >> 4);
  } else {
    tm = (xcd >> 1) * 4 + (idx & 3);
    tn = (xcd & 1) * 8 + (idx >> 2);
  }
  const int m0 = tm * 128, n0 = tn * BN;

  if (w >= 8) {
    const int lw = w - 8;
    const unsigned voffA = (unsigned)((l >> 3) * LDA * 2 + (((l & 7) ^ (l >> 3)) << 4));
    unsigned voffB[NBI];
#pragma unroll
    for (int i = 0; i < NBI; ++i) {
      int sub = SUBW * lw + 4 * i + (l >> 4);
      if (sub >= SUBW * (lw + 1)) sub = SUBW * (lw + 1) - 1;
      int kb = sub / NC, cc = sub % NC;
      int within = l & 15;
      int r = (within >> 1) ^ ((kb & 1) << 2);
      voffB[i] = (unsigned)(((kb * 8 + r) * LDW + cc * 16 + (within & 1) * 8) * 2);
    }
    const char* abase = (const char*)(A + (size_t)(m0 + 32 * lw) * LDA + ks * 768);
    const char* bbase = (const char*)(W + (size_t)ks * 768 * LDW + n0);
    auto issue = [&](int kt, int st) {
      const unsigned sa = lds0 + st * STG;
#pragma unroll
      for (int i = 0; i < 4; ++i)
        glds16(abase + (size_t)i * 8 * LDA * 2 + kt * 128, voffA, sa + (32 * lw + 8 * i) * 128);
      const char* sb = bbase + (size_t)kt * 64 * LDW * 2;
#pragma unroll
      for (int i = 0; i < NBI; ++i) {
        unsigned dst = sa + ASZ + (SUBW * lw + 4 * i) * 256;
        if (4 * i + 4 <= SUBW) {
          glds16(sb, voffB[i], dst);
        } else {
          if (l < (SUBW - 4 * i) * 16) glds16(sb, voffB[i], dst);
        }
      }
    };
#pragma unroll
    for (int s = 0; s < NS - 1; ++s) issue(s, s);
    int stn = NS - 1;
#pragma unroll 1
    for (int kt = 0; kt < NK; ++kt) {
      if (kt < NK - (NS - 2)) wait_vm<(NS - 2) * NLD>();
      else wait_vm<0>();
      bar();
      if (kt + NS - 1 < NK) issue(kt + NS - 1, stn);
      stn = (stn + 1 == NS) ? 0 : stn + 1;
    }
    bar();
    bar();
    return;
  }

  if constexpr (EPI == 1) {
    constexpr int NH = NC / 2;
    const int wm = w & 3, wn = w >> 2;
    const int g = l >> 4;
    const unsigned aoff = (unsigned)((32 * wm + (l & 15)) * 128 + ((g ^ (l & 7)) << 4));
    const int q = (l & 15) >> 2, p = l & 3;
    const unsigned boff_t0 = (unsigned)(ASZ + (g * NC + wn * NH) * 256 + ((4 * (g & 1) + q) * 32) + p * 8);
    const unsigned boff_t1 = (unsigned)(ASZ + (g * NC + wn * NH) * 256 + ((4 * (1 - (g & 1)) + q) * 32) + p * 8);
    f32x4 acc[2][NH];
#pragma unroll
    for (int i = 0; i < 2; ++i)
#pragma unroll
      for (int j = 0; j < NH; ++j) acc[i][j] = (f32x4){0.f, 0.f, 0.f, 0.f};
    int st = 0;
#pragma unroll 1
    for (int kt = 0; kt < NK; ++kt) {
      wait_lgkm0();
      bar();
      const LDS_AS char* sp = smem + st * STG;
#pragma unroll
      for (int ks2 = 0; ks2 < 2; ++ks2) {
        bf16x8 xb[2];
#pragma unroll
        for (int mf = 0; mf < 2; ++mf) xb[mf] = lds_read16(sp + (aoff ^ (ks2 * 64)) + mf * 2048);
#pragma unroll
        for (int cc = 0; cc < NH; ++cc) {
          bf16x4 w0 = lds_tr(sp + boff_t0 + ks2 * 4 * NC * 256 + cc * 256);
          bf16x4 w1 = lds_tr(sp + boff_t1 + ks2 * 4 * NC * 256 + cc * 256);
          bf16x8 wf = cat4(w0, w1);
          acc[0][cc] = mfma16(wf, xb[0], acc[0][cc]);
          acc[1][cc] = mfma16(wf, xb[1], acc[1][cc]);
        }
      }
      st = (st + 1 == NS) ? 0 : st + 1;
    }
    bar();
    bar();
    u16* out = (u16*)outv;
#pragma unroll
    for (int mf = 0; mf < 2; ++mf) {
      u16* op = out + (size_t)(m0 + 32 * wm + 16 * mf + (l & 15)) * 3072 + n0 + 16 * NH * wn;
      unsigned pk[NH][2];
#pragma unroll
      for (int cc = 0; cc < NH; ++cc) {
        f32x4 v = acc[mf][cc];
        const float c1 = -2.302208198144325f;
        const float c2 = -0.1029432397500235f;
        unsigned y01 = pack2(v[0], v[1]), y23 = pack2(v[2], v[3]);
        float y[4] = {lo_f(y01), hi_f(y01), lo_f(y23), hi_f(y23)};
#pragma unroll
        for (int j = 0; j < 4; ++j) {
          float e = __builtin_amdgcn_exp2f(y[j] * (c1 + c2 * y[j] * y[j]));
          v[j] = y[j] * __builtin_amdgcn_rcpf(1.f + e);
        }
        pk[cc][0] = pack2(v[0], v[1]);
        pk[cc][1] = pack2(v[2], v[3]);
      }
#pragma unroll
      for (int a = 0; a < NH / 2; ++a) {
        u32x4 o = pair_pack(pk[2 * a][0], pk[2 * a][1], pk[2 * a + 1][0], pk[2 * a + 1][1]);
        *(u32x4*)(op + 32 * a + (g & 1) * 16 + (g & 2) * 4) = o;
      }
    }
    return;
  }

  const int wm = w & 3, kh = w >> 2;
  const int g = l >> 4;
  const unsigned aoff = (unsigned)(((32 * wm + (l & 15)) * 128 + ((g ^ (l & 7)) << 4)) ^ (kh * 64));
  const int q = (l & 15) >> 2, p = l & 3;
  const unsigned boff_t0 = (unsigned)(ASZ + (4 * kh + g) * NC * 256 + ((4 * (g & 1) + q) * 32) + p * 8);
  const unsigned boff_t1 = (unsigned)(ASZ + (4 * kh + g) * NC * 256 + ((4 * (1 - (g & 1)) + q) * 32) + p * 8);

  f32x4 acc[2][NC];
#pragma unroll
  for (int i = 0; i < 2; ++i)
#pragma unroll
    for (int j = 0; j < NC; ++j) acc[i][j] = (f32x4){0.f, 0.f, 0.f, 0.f};

  int st = 0;
#pragma unroll 1
  for (int kt = 0; kt < NK; ++kt) {
    wait_lgkm0();
    bar();
    const LDS_AS char* sp = smem + st * STG;
    bf16x8 xb[2];
#pragma unroll
    for (int mf = 0; mf < 2; ++mf) xb[mf] = lds_read16(sp + aoff + mf * 2048);
#pragma unroll
    for (int cc = 0; cc < NC; ++cc) {
      bf16x4 w0 = lds_tr(sp + boff_t0 + cc * 256);
      bf16x4 w1 = lds_tr(sp + boff_t1 + cc * 256);
      bf16x8 wf = cat4(w0, w1);
      acc[0][cc] = mfma16(wf, xb[0], acc[0][cc]);
      acc[1][cc] = mfma16(wf, xb[1], acc[1][cc]);
    }
    st = (st + 1 == NS) ? 0 : st + 1;
  }

  wait_lgkm0();
  bar();
  {
    LDS_AS char* rp = smem + (wm * 2 + kh) * NC * 1024 + l * 16;
    if (kh) {
#pragma unroll
      for (int cc = 0; cc < NC; ++cc) {
        *(LDS_AS f32x4*)(rp + cc * 1024) = acc[0][cc];
        acc[0][cc] = acc[1][cc];
      }
    } else {
#pragma unroll
      for (int cc = 0; cc < NC; ++cc) *(LDS_AS f32x4*)(rp + cc * 1024) = acc[1][cc];
    }
  }
  wait_lgkm0();
  bar();
  const int row = m0 + 32 * wm + 16 * kh + (l & 15);
  const LDS_AS char* rp = smem + (wm * 2 + (1 - kh)) * NC * 1024 + l * 16;
  if constexpr (EPI == 2) {
    float* op = (float*)outv + ((size_t)ks * 2048 + row) * 768 + n0 + 4 * g;
#pragma unroll
    for (int cc = 0; cc < NC; ++cc) *(f32x4*)(op + 16 * cc) = acc[0][cc] + lds_read16f(rp + cc * 1024);
  } else {
    u16* out = (u16*)outv;
    unsigned pk[NC][2];
#pragma unroll
    for (int cc = 0; cc < NC; ++cc) {
      f32x4 v = acc[0][cc] + lds_read16f(rp + cc * 1024);
      if constexpr (EPI == 1) {
        const float c1 = -2.302208198144325f;
        const float c2 = -0.1029432397500235f;
        unsigned y01 = pack2(v[0], v[1]), y23 = pack2(v[2], v[3]);
        float y[4] = {lo_f(y01), hi_f(y01), lo_f(y23), hi_f(y23)};
#pragma unroll
        for (int j = 0; j < 4; ++j) {
          float e = __builtin_amdgcn_exp2f(y[j] * (c1 + c2 * y[j] * y[j]));
          v[j] = y[j] * __builtin_amdgcn_rcpf(1.f + e);
        }
      }
      pk[cc][0] = pack2(v[0], v[1]);
      pk[cc][1] = pack2(v[2], v[3]);
    }
    auto dst_ptr = [&](int n) -> u16* {
      if constexpr (EPI == 1) {
        return out + (size_t)row * 3072 + n;
      } else {
        if (n < 1536) return out + (size_t)row * 1536 + n;
        int nv = n - 1536;
        int hd = nv / 96;
        int dd = nv - hd * 96;
        return out2 + ((size_t)(((row >> 10) * 8 + hd) * 1024 + (row & 1023))) * 96 + dd;
      }
    };
#pragma unroll
    for (int a = 0; a < NC / 2; ++a) {
      u32x4 o = pair_pack(pk[2 * a][0], pk[2 * a][1], pk[2 * a + 1][0], pk[2 * a + 1][1]);
      *(u32x4*)dst_ptr(n0 + 32 * a + (g & 1) * 16 + (g & 2) * 4) = o;
    }
    if constexpr (NC & 1) {
      u32x2 o;
      o[0] = pk[NC - 1][0];
      o[1] = pk[NC - 1][1];
      *(u32x2*)dst_ptr(n0 + 16 * (NC - 1) + 4 * g) = o;
    }
  }
}

__global__ __launch_bounds__(768, 3) void k_scores(const u16* __restrict__ qk, u16* __restrict__ P,
                                                   f32x2* __restrict__ ml) {
  constexpr int NS = 4;
  constexpr int ASZ = 128 * 128;
  constexpr int STG = 2 * ASZ;
  constexpr int NK = 384 / 64;
  constexpr int NLD = 8;
  constexpr int LDQ = 1536;
  __shared__ __attribute__((aligned(16))) char smem_raw[NS * STG];
  LDS_AS char* smem = (LDS_AS char*)smem_raw;
  const unsigned lds0 = (unsigned)(uintptr_t)smem;
  const int tid = threadIdx.x, l = tid & 63;
  const int w = uni(tid >> 6);
  const int bid = blockIdx.x;
  const int xcd = bid & 7, idx = bid >> 3;
  const int bh = xcd >> 1;
  const int tq = (xcd & 1) * 4 + (idx & 3);
  const int tk = idx >> 2;
  const int b = bh >> 1, h = bh & 1;

  if (w >= 8) {
    const int lw = w - 8;
    const unsigned voff = (unsigned)((l >> 3) * LDQ * 2 + (((l & 7) ^ (l >> 3)) << 4));
    const char* qbase = (const char*)(qk + (size_t)(b * 1024 + tq * 128 + 32 * lw) * LDQ + h * 384);
    const char* kbase = (const char*)(qk + (size_t)(b * 1024 + tk * 128 + 32 * lw) * LDQ + 768 + h * 384);
    auto issue = [&](int kt, int st) {
      const unsigned sa = lds0 + st * STG;
#pragma unroll
      for (int i = 0; i < 4; ++i)
        glds16(qbase + (size_t)i * 8 * LDQ * 2 + kt * 128, voff, sa + (32 * lw + 8 * i) * 128);
#pragma unroll
      for (int i = 0; i < 4; ++i)
        glds16(kbase + (size_t)i * 8 * LDQ * 2 + kt * 128, voff, sa + ASZ + (32 * lw + 8 * i) * 128);
    };
#pragma unroll
    for (int s = 0; s < NS - 1; ++s) issue(s, s);
    int stn = NS - 1;
#pragma unroll 1
    for (int kt = 0; kt < NK; ++kt) {
      if (kt < NK - (NS - 2)) wait_vm<(NS - 2) * NLD>();
      else wait_vm<0>();
      bar();
      if (kt + NS - 1 < NK) issue(kt + NS - 1, stn);
      stn = (stn + 1 == NS) ? 0 : stn + 1;
    }
    bar();
    bar();
    return;
  }

  const int wm = w & 3, kh = w >> 2;
  const int g = l >> 4;
  const unsigned qoff = (unsigned)(((32 * wm + (l & 15)) * 128 + ((g ^ (l & 7)) << 4)) ^ (kh * 64));
  const unsigned koff = (unsigned)(ASZ + (((l & 15) * 128 + ((g ^ (l & 7)) << 4)) ^ (kh * 64)));
  f32x4 acc[2][8];
#pragma unroll
  for (int i = 0; i < 2; ++i)
#pragma unroll
    for (int j = 0; j < 8; ++j) acc[i][j] = (f32x4){0.f, 0.f, 0.f, 0.f};

  int st = 0;
#pragma unroll 1
  for (int kt = 0; kt < NK; ++kt) {
    wait_lgkm0();
    bar();
    const LDS_AS char* sp = smem + st * STG;
    bf16x8 qf[2];
#pragma unroll
    for (int mf = 0; mf < 2; ++mf) qf[mf] = lds_read16(sp + qoff + mf * 2048);
#pragma unroll
    for (int kf = 0; kf < 8; ++kf) {
      bf16x8 kfr = lds_read16(sp + koff + kf * 2048);
      acc[0][kf] = mfma16(kfr, qf[0], acc[0][kf]);
      acc[1][kf] = mfma16(kfr, qf[1], acc[1][kf]);
    }
    st = (st + 1 == NS) ? 0 : st + 1;
  }
  wait_lgkm0();
  bar();
  {
    LDS_AS char* rp = smem + (wm * 2 + kh) * 8 * 1024 + l * 16;
    if (kh) {
#pragma unroll
      for (int kf = 0; kf < 8; ++kf) {
        *(LDS_AS f32x4*)(rp + kf * 1024) = acc[0][kf];
        acc[0][kf] = acc[1][kf];
      }
    } else {
#pragma unroll
      for (int kf = 0; kf < 8; ++kf) *(LDS_AS f32x4*)(rp + kf * 1024) = acc[1][kf];
    }
  }
  wait_lgkm0();
  bar();
  f32x4 s[8];
  {
    const LDS_AS char* rp = smem + (wm * 2 + (1 - kh)) * 8 * 1024 + l * 16;
#pragma unroll
    for (int kf = 0; kf < 8; ++kf) {
      f32x4 v = acc[0][kf] + lds_read16f(rp + kf * 1024);
      unsigned a01 = pack2(v[0], v[1]), a23 = pack2(v[2], v[3]);
      a01 = pack2(lo_f(a01) * 0.051031036307982884f, hi_f(a01) * 0.051031036307982884f);
      a23 = pack2(lo_f(a23) * 0.051031036307982884f, hi_f(a23) * 0.051031036307982884f);
      s[kf] = (f32x4){lo_f(a01), hi_f(a01), lo_f(a23), hi_f(a23)};
    }
  }
  const float l2e = 1.4426950408889634f;
  float mx = -INFINITY;
#pragma unroll
  for (int kf = 0; kf < 8; ++kf)
#pragma unroll
    for (int j = 0; j < 4; ++j) mx = fmaxf(mx, s[kf][j]);
  mx = fmaxf(mx, __shfl_xor(mx, 16));
  mx = fmaxf(mx, __shfl_xor(mx, 32));
  const float mbl = mx * l2e;
  float sum = 0.f;
  unsigned pk[8][2];
#pragma unroll
  for (int kf = 0; kf < 8; ++kf) {
    float e[4];
#pragma unroll
    for (int j = 0; j < 4; ++j) {
      e[j] = __builtin_amdgcn_exp2f(s[kf][j] * l2e - mbl);
      sum += e[j];
    }
    pk[kf][0] = pack2(e[0], e[1]);
    pk[kf][1] = pack2(e[2], e[3]);
  }
  sum += __shfl_xor(sum, 16);
  sum += __shfl_xor(sum, 32);
  const int qrow = tq * 128 + 32 * wm + 16 * kh + (l & 15);
  u16* op = P + ((size_t)bh * 1024 + qrow) * 1024 + tk * 128;
#pragma unroll
  for (int a = 0; a < 4; ++a) {
    u32x4 o = pair_pack(pk[2 * a][0], pk[2 * a][1], pk[2 * a + 1][0], pk[2 * a + 1][1]);
    *(u32x4*)(op + 32 * a + (g & 1) * 16 + (g & 2) * 4) = o;
  }
  if (g == 0) {
    f32x2 s2;
    s2[0] = mbl;
    s2[1] = sum;
    ml[((size_t)bh * 1024 + qrow) * 8 + tk] = s2;
  }
}

template <int MODE, int NL>
__global__ __launch_bounds__(64 * (4 + NL), NL / 4 + 1) void k_gemm_small(const u16* __restrict__ Ain, const u16* __restrict__ Bin,
                                                       const float* __restrict__ modp,
                                                       const u16* __restrict__ resid,
                                                       const f32x2* __restrict__ ml, f32x2* __restrict__ stats,
                                                       u16* __restrict__ out) {
  constexpr int KTOT = (MODE == 0) ? 1024 : (MODE == 1 ? 768 : 3072);
  constexpr int KH = KTOT / 2;
  constexpr int NK = KH / 64;
  constexpr int LDA = KTOT;
  constexpr int LDB = (MODE == 0) ? 96 : 768;
  constexpr int NS = 4;
  constexpr int STG = 40960;
  constexpr int NLD = 40 / NL;
  constexpr int AI = 16 / NL, BI = 24 / NL;
  __shared__ __attribute__((aligned(16))) char smem_raw[NS * STG];
  LDS_AS char* smem = (LDS_AS char*)smem_raw;
  const unsigned lds0 = (unsigned)(uintptr_t)smem;
  const int tid = threadIdx.x, l = tid & 63;
  const int w = uni(tid >> 6);
  const int bid = blockIdx.x;
  const int xcd = bid & 7, idx = bid >> 3;
  const int g = l >> 4;

  int m0, n0, bh = 0;
  const u16* Ap;
  const u16* Bp;
  if constexpr (MODE == 0) {
    bh = xcd >> 1;
    const int tq = (xcd & 1) * 8 + (idx & 7);
    const int td = idx >> 3;
    m0 = tq * 64;
    n0 = td * 96;
    Ap = Ain + ((size_t)bh * 1024 + m0) * 1024;
    Bp = Bin + ((size_t)(bh * 4 + td) * 1024) * 96;
  } else {
    const int tm = (xcd >> 1) * 8 + (idx & 7);
    const int tn = (xcd & 1) * 4 + (idx >> 3);
    m0 = tm * 64;
    n0 = tn * 96;
    Ap = Ain + (size_t)m0 * LDA;
    Bp = Bin + n0;
  }

  if (w >= 4) {
    const int lw = w - 4;
    const int kq = lw & 1, part = lw >> 1;
    const unsigned voffA = (unsigned)((l >> 3) * LDA * 2 + (((l & 7) ^ (l >> 3)) << 4));
    unsigned voffB[BI];
#pragma unroll
    for (int i = 0; i < BI; ++i) {
      int sub = 4 * BI * part + 4 * i + (l >> 4);
      int kb = sub / 6, cc = sub % 6;
      int within = l & 15;
      int r = (within >> 1) ^ ((kb & 1) << 2);
      voffB[i] = (unsigned)(((kb * 8 + r) * LDB + cc * 16 + (within & 1) * 8) * 2);
    }
    const char* abase = (const char*)(Ap + (size_t)(8 * AI * part) * LDA + kq * KH);
    const char* bbase = (const char*)(Bp + (size_t)(kq * KH) * LDB);
    auto issue = [&](int kt, int st) {
      const unsigned sa = lds0 + st * STG;
#pragma unroll
      for (int i = 0; i < AI; ++i)
        glds16(abase + (size_t)i * 8 * LDA * 2 + kt * 128, voffA, sa + kq * 8192 + (8 * AI * part + 8 * i) * 128);
      const char* sb = bbase + (size_t)kt * 64 * LDB * 2;
#pragma unroll
      for (int i = 0; i < BI; ++i) glds16(sb, voffB[i], sa + 16384 + kq * 12288 + (4 * BI * part + 4 * i) * 256);
    };
#pragma unroll
    for (int s = 0; s < NS - 1; ++s) issue(s, s);
    int stn = NS - 1;
#pragma unroll 1
    for (int kt = 0; kt < NK; ++kt) {
      if (kt < NK - (NS - 2)) wait_vm<(NS - 2) * NLD>();
      else wait_vm<0>();
      bar();
      if (kt + NS - 1 < NK) issue(kt + NS - 1, stn);
      stn = (stn + 1 == NS) ? 0 : stn + 1;
    }
    bar();
    bar();
    return;
  }

  const int kq = w & 1, nh = w >> 1;
  float cf[4][4];
  if constexpr (MODE == 0) {
#pragma unroll
    for (int mf = 0; mf < 4; ++mf) {
      const f32x4* sp = (const f32x4*)(ml + ((size_t)bh * 1024 + m0 + 16 * mf + (l & 15)) * 8);
      f32x4 sv[4];
#pragma unroll
      for (int j = 0; j < 4; ++j) sv[j] = sp[j];
      float mx = fmaxf(sv[0][0], sv[0][2]);
#pragma unroll
      for (int j = 1; j < 4; ++j) mx = fmaxf(mx, fmaxf(sv[j][0], sv[j][2]));
      float e[8];
      float lsum = 0.f;
#pragma unroll
      for (int j = 0; j < 4; ++j) {
        e[2 * j] = __builtin_amdgcn_exp2f(sv[j][0] - mx);
        e[2 * j + 1] = __builtin_amdgcn_exp2f(sv[j][2] - mx);
        lsum += e[2 * j] * sv[j][1] + e[2 * j + 1] * sv[j][3];
      }
      float inv = 1.f / lsum;
      if (kq) {
#pragma unroll
        for (int j = 0; j < 4; ++j) cf[mf][j] = e[4 + j] * inv;
      } else {
#pragma unroll
        for (int j = 0; j < 4; ++j) cf[mf][j] = e[j] * inv;
      }
    }
  }
  f32x4 gq[2][3][2];
  u32x2 xr[2][3];
  if constexpr (MODE != 0) {
    const int goff = (MODE == 1) ? 2 * 768 : 5 * 768;
    const int b = m0 >> 10;
    const float* mp0 = modp + (0 * 2 + b) * 4608 + goff + n0 + 48 * nh + 4 * g;
    const float* mp1 = modp + (1 * 2 + b) * 4608 + goff + n0 + 48 * nh + 4 * g;
#pragma unroll
    for (int cc = 0; cc < 3; ++cc) {
      gq[0][cc][0] = *(const f32x4*)(mp0 + 16 * cc);
      gq[0][cc][1] = *(const f32x4*)(mp1 + 16 * cc);
    }
#pragma unroll
    for (int mf = 0; mf < 2; ++mf) {
      const u16* rp = resid + (size_t)(m0 + 32 * kq + 16 * mf + (l & 15)) * 768 + n0 + 48 * nh + 4 * g;
#pragma unroll
      for (int cc = 0; cc < 3; ++cc) xr[mf][cc] = *(const u32x2*)(rp + 16 * cc);
    }
  }

  const unsigned aoff0 = (unsigned)(kq * 8192 + (l & 15) * 128 + ((g ^ (l & 7)) << 4));
  const int q = (l & 15) >> 2, p = l & 3;
  const unsigned boff_t0 = (unsigned)(16384 + kq * 12288 + (g * 6 + 3 * nh) * 256 + ((4 * (g & 1) + q) * 32) + p * 8);
  const unsigned boff_t1 =
      (unsigned)(16384 + kq * 12288 + (g * 6 + 3 * nh) * 256 + ((4 * (1 - (g & 1)) + q) * 32) + p * 8);

  f32x4 acc[4][3];
  f32x4 tot[4][3];
#pragma unroll
  for (int i = 0; i < 4; ++i)
#pragma unroll
    for (int j = 0; j < 3; ++j) {
      acc[i][j] = (f32x4){0.f, 0.f, 0.f, 0.f};
      tot[i][j] = (f32x4){0.f, 0.f, 0.f, 0.f};
    }

  int st = 0;
#pragma unroll 1
  for (int kt = 0; kt < NK; ++kt) {
    wait_lgkm0();
    bar();
    const LDS_AS char* sp = smem + st * STG;
#pragma unroll
    for (int ks = 0; ks < 2; ++ks) {
      bf16x8 af[4];
#pragma unroll
      for (int mf = 0; mf < 4; ++mf) af[mf] = lds_read16(sp + ((aoff0 ^ (ks * 64)) + mf * 2048));
#pragma unroll
      for (int cc = 0; cc < 3; ++cc) {
        bf16x4 w0 = lds_tr(sp + boff_t0 + ks * 6144 + cc * 256);
        bf16x4 w1 = lds_tr(sp + boff_t1 + ks * 6144 + cc * 256);
        bf16x8 wf = cat4(w0, w1);
#pragma unroll
        for (int mf = 0; mf < 4; ++mf) acc[mf][cc] = mfma16(wf, af[mf], acc[mf][cc]);
      }
    }
    if constexpr (MODE == 0) {
      if (kt & 1) {
#pragma unroll
        for (int mf = 0; mf < 4; ++mf) {
          const float c = cf[mf][0];
          cf[mf][0] = cf[mf][1];
          cf[mf][1] = cf[mf][2];
          cf[mf][2] = cf[mf][3];
#pragma unroll
          for (int cc = 0; cc < 3; ++cc) {
            tot[mf][cc] += acc[mf][cc] * c;
            acc[mf][cc] = (f32x4){0.f, 0.f, 0.f, 0.f};
          }
        }
      }
    }
    st = (st + 1 == NS) ? 0 : st + 1;
  }
  if constexpr (MODE == 0) {
#pragma unroll
    for (int mf = 0; mf < 4; ++mf)
#pragma unroll
      for (int cc = 0; cc < 3; ++cc) acc[mf][cc] = tot[mf][cc];
  }
  wait_lgkm0();
  bar();
  {
    LDS_AS char* rp = smem + (nh * 2 + kq) * 6144 + l * 16;
    if (kq) {
#pragma unroll
      for (int mf = 0; mf < 2; ++mf)
#pragma unroll
        for (int cc = 0; cc < 3; ++cc) {
          *(LDS_AS f32x4*)(rp + (mf * 3 + cc) * 1024) = acc[mf][cc];
          acc[mf][cc] = acc[2 + mf][cc];
        }
    } else {
#pragma unroll
      for (int mf = 0; mf < 2; ++mf)
#pragma unroll
        for (int cc = 0; cc < 3; ++cc) *(LDS_AS f32x4*)(rp + (mf * 3 + cc) * 1024) = acc[2 + mf][cc];
    }
  }
  wait_lgkm0();
  bar();
  f32x4 r[2][3];
  {
    const LDS_AS char* rp = smem + (nh * 2 + (1 - kq)) * 6144 + l * 16;
#pragma unroll
    for (int mf = 0; mf < 2; ++mf)
#pragma unroll
      for (int cc = 0; cc < 3; ++cc)
        r[mf][cc] = acc[mf][cc] + lds_read16f(rp + (mf * 3 + cc) * 1024);
  }
#pragma unroll
  for (int mf = 0; mf < 2; ++mf) {
    const int mrow = m0 + 32 * kq + 16 * mf + (l & 15);
    float v[3][4];
    u16* op;
    if constexpr (MODE == 0) {
      op = out + (size_t)((bh >> 1) * 1024 + mrow) * 768 + (bh & 1) * 384 + n0 + 48 * nh;
#pragma unroll
      for (int cc = 0; cc < 3; ++cc)
#pragma unroll
        for (int j = 0; j < 4; ++j) v[cc][j] = r[mf][cc][j];
    } else {
      op = out + (size_t)mrow * 768 + n0 + 48 * nh;
#pragma unroll
      for (int cc = 0; cc < 3; ++cc) {
        float xv[4] = {lo_f(xr[mf][cc][0]), hi_f(xr[mf][cc][0]), lo_f(xr[mf][cc][1]), hi_f(xr[mf][cc][1])};
#pragma unroll
        for (int j = 0; j < 4; ++j) {
          float gate = rbf(gq[0][cc][0][j] + gq[0][cc][1][j]);
          v[cc][j] = rbf(xv[j] + rbf(gate * rbf(r[mf][cc][j])));
        }
      }
    }
    {
      u32x4 o = pair_pack(pack2(v[0][0], v[0][1]), pack2(v[0][2], v[0][3]), pack2(v[1][0], v[1][1]),
                          pack2(v[1][2], v[1][3]));
      *(u32x4*)(op + (g & 1) * 16 + (g & 2) * 4) = o;
      u32x2 o2;
      o2[0] = pack2(v[2][0], v[2][1]);
      o2[1] = pack2(v[2][2], v[2][3]);
      *(u32x2*)(op + 32 + 4 * g) = o2;
    }
  }
}

#define SNL 4
extern "C" void kernel_launch(void* const* d_in, const int* in_sizes, int n_in, void* d_out, int out_size,
                              void* d_ws, size_t ws_size, hipStream_t stream) {
  const u16* x = (const u16*)d_in[0];
  const u16* c = (const u16*)d_in[1];
  const u16* wqkv = (const u16*)d_in[2];
  const u16* wo = (const u16*)d_in[3];
  const u16* wmod = (const u16*)d_in[4];
  const u16* wfc1 = (const u16*)d_in[5];
  const u16* wfc2 = (const u16*)d_in[6];
  u16* out = (u16*)d_out;

  char* ws = (char*)d_ws;
  size_t off = 0;
  auto alloc = [&](size_t bytes) {
    char* p = ws + off;
    off += (bytes + 255) & ~(size_t)255;
    return p;
  };
  float* modp = (float*)alloc(2 * 2 * 4608 * 4);
  u16* h = (u16*)alloc((size_t)Mrows * 768 * 2);
  u16* qk = (u16*)alloc((size_t)Mrows * 1536 * 2);
  u16* Vt = (u16*)alloc((size_t)Mrows * 768 * 2);
  u16* P = (u16*)alloc((size_t)4 * 1024 * 1024 * 2);
  f32x2* ml = (f32x2*)alloc((size_t)4 * 1024 * 8 * 8);
  u16* O = (u16*)alloc((size_t)Mrows * 768 * 2);
  u16* x1 = (u16*)alloc((size_t)Mrows * 768 * 2);
  f32x2* st2 = (f32x2*)alloc((size_t)Mrows * 16 * 8);
  u16* h2 = (u16*)alloc((size_t)Mrows * 768 * 2);
  u16* u = (u16*)alloc((size_t)Mrows * 3072 * 2);

  k0_mod<<<144 + 96, 256, 0, stream>>>(c, wmod, modp, wqkv, wo);
  k_lnmod<<<256 + 192, 256, 0, stream>>>(x, modp, 0, h, wfc1, wfc2);
  k_gemm_big<144, 4, 0><<<256, 768, 0, stream>>>(h, wqkv, qk, Vt);
  k_scores<<<256, 768, 0, stream>>>(qk, P, ml);
  k_gemm_small<0, SNL><<<256, 64 * (4 + SNL), 0, stream>>>(P, Vt, modp, nullptr, ml, nullptr, O);
  k_gemm_small<1, SNL><<<256, 64 * (4 + SNL), 0, stream>>>(O, wo, modp, x, nullptr, st2, x1);
  k_lnmod<<<256, 256, 0, stream>>>(x1, modp, 3 * 768, h2, nullptr, nullptr);
  k_gemm_big<192, 4, 1><<<256, 768, 0, stream>>>(h2, wfc1, u, nullptr);
  k_gemm_small<2, SNL><<<256, 64 * (4 + SNL), 0, stream>>>(u, wfc2, modp, x1, nullptr, nullptr, out);
}
```

```cpp
#include <hip/hip_runtime.h>
#include <hip/hip_bf16.h>
#include <stdint.h>
#include <type_traits>

typedef __attribute__((ext_vector_type(8))) __bf16 bf16x8;
typedef __attribute__((ext_vector_type(4))) __bf16 bf16x4;
typedef __attribute__((ext_vector_type(2))) __bf16 bf16x2;
typedef __attribute__((ext_vector_type(4))) float f32x4;
typedef __attribute__((ext_vector_type(2))) float f32x2;
typedef __attribute__((ext_vector_type(4))) unsigned u32x4;
typedef __attribute__((ext_vector_type(2))) unsigned u32x2;
typedef unsigned short u16;
#define LDS_AS __attribute__((address_space(3)))

#define DEVI __device__ __forceinline__

static constexpr int Bsz = 2, Nseq = 1024, Dm = 768, Ff = 3072, Dh = 384;
static constexpr int Mrows = Bsz * Nseq;

template <int I, int N, class F>
DEVI void static_for(F&& f) {
  if constexpr (I < N) {
    f(std::integral_constant<int, I>{});
    static_for<I + 1, N>(f);
  }
}

DEVI float lo_f(unsigned u) { return __uint_as_float(u << 16); }
DEVI float hi_f(unsigned u) { return __uint_as_float(u & 0xffff0000u); }
DEVI float bf2f(u16 h) { return __uint_as_float(((unsigned)h) << 16); }
DEVI unsigned pack2(float a, float b) {
  bf16x2 v;
  v[0] = (__bf16)a;
  v[1] = (__bf16)b;
  return __builtin_bit_cast(unsigned, v);
}
DEVI float rbf(float a) { return lo_f(pack2(a, 0.f) ); }

template <int N>
DEVI void wait_vm() { asm volatile("s_waitcnt vmcnt(%0)" ::"i"(N) : "memory"); }
DEVI void wait_lgkm0() { asm volatile("s_waitcnt lgkmcnt(0)" ::: "memory"); }
DEVI void bar() { __builtin_amdgcn_s_barrier(); }

DEVI void glds16(const void* sbase, unsigned voff, unsigned lds_dst) {
  unsigned keep;
  asm volatile(
      "s_mov_b32 %0, m0\n\t"
      "s_mov_b32 m0, %3\n\t"
      "s_nop 0\n\t"
      "global_load_lds_dwordx4 %1, %2\n\t"
      "s_mov_b32 m0, %0"
      : "=&s"(keep)
      : "v"(voff), "s"(sbase), "s"(lds_dst)
      : "memory");
}

DEVI void gld16(u32x4& d, const void* p) { asm volatile("global_load_dwordx4 %0, %1, off" : "=v"(d) : "v"(p) : "memory"); }
DEVI void gld8(u32x2& d, const void* p) { asm volatile("global_load_dwordx2 %0, %1, off" : "=v"(d) : "v"(p) : "memory"); }
template <class T>
DEVI void pin(T& v) { asm volatile("" : "+v"(v)); }
DEVI f32x4 asf4(u32x4 v) { return __builtin_bit_cast(f32x4, v); }

DEVI bf16x8 lds_read16(const LDS_AS char* p) { return *(const LDS_AS bf16x8*)p; }
DEVI u32x4 lds_read16u(const LDS_AS char* p) { return *(const LDS_AS u32x4*)p; }
DEVI f32x4 lds_read16f(const LDS_AS char* p) { return *(const LDS_AS f32x4*)p; }
DEVI bf16x4 lds_tr(const LDS_AS char* p) {
  return __builtin_amdgcn_ds_read_tr16_b64_v4bf16((LDS_AS bf16x4*)p);
}
DEVI bf16x8 cat4(bf16x4 a, bf16x4 b) {
  return __builtin_shufflevector(a, b, 0, 1, 2, 3, 4, 5, 6, 7);
}
DEVI f32x4 mfma16(bf16x8 a, bf16x8 b, f32x4 c) {
  return __builtin_amdgcn_mfma_f32_16x16x32_bf16(a, b, c, 0, 0, 0);
}
DEVI int uni(int v) { return __builtin_amdgcn_readfirstlane(v); }

DEVI u32x4 pair_pack(unsigned ax, unsigned ay, unsigned bx, unsigned by) {
  auto r0 = __builtin_amdgcn_permlane16_swap(ax, bx, false, false);
  auto r1 = __builtin_amdgcn_permlane16_swap(ay, by, false, false);
  u32x4 o;
  o[0] = r0[0];
  o[1] = r1[0];
  o[2] = r0[1];
  o[3] = r1[1];
  return o;
}

__global__ __launch_bounds__(256) void k0_mod(const u16* __restrict__ c, const u16* __restrict__ wmod,
                                              float* __restrict__ modp, const u16* __restrict__ w0,
                                              const u16* __restrict__ w1) {
  __shared__ float sc[2][384];
  __shared__ float red[32][2][64];
  const int tid = threadIdx.x;
  if (blockIdx.x >= 144) {
    const int t = (blockIdx.x - 144) * 256 + tid;
    u32x4 a = {0u, 0u, 0u, 0u};
    u32x4 v[12];
#pragma unroll
    for (int i = 0; i < 12; ++i) {
      int ci = t + i * 24576;
      const u16* p = (ci < 221184) ? (w0 + (size_t)ci * 8) : (w1 + (size_t)(ci - 221184) * 8);
      v[i] = *(const u32x4*)p;
    }
#pragma unroll
    for (int i = 0; i < 12; ++i) a ^= v[i];
    asm volatile("" ::"v"(a));
    return;
  }
  const int cb = blockIdx.x % 72, kp = blockIdx.x / 72;
  const int tc = tid & 7, tr = tid >> 3;
  u32x4 w[12];
  const u16* wp = wmod + (size_t)(kp * 384 + tr) * 4608 + cb * 64 + tc * 8;
#pragma unroll
  for (int i = 0; i < 12; ++i) w[i] = *(const u32x4*)(wp + (size_t)i * 32 * 4608);
  for (int i = tid; i < 768; i += 256) {
    int b = i / 384, k = i % 384;
    float v = bf2f(c[b * 768 + kp * 384 + k]);
    float s = v / (1.f + __expf(-v));
    sc[b][k] = rbf(s);
  }
  __syncthreads();
  float acc[2][8];
#pragma unroll
  for (int b = 0; b < 2; ++b)
#pragma unroll
    for (int j = 0; j < 8; ++j) acc[b][j] = 0.f;
#pragma unroll
  for (int i = 0; i < 12; ++i) {
    float s0 = sc[0][tr + 32 * i], s1 = sc[1][tr + 32 * i];
#pragma unroll
    for (int j = 0; j < 4; ++j) {
      float a = lo_f(w[i][j]), bb = hi_f(w[i][j]);
      acc[0][2 * j] += s0 * a;
      acc[0][2 * j + 1] += s0 * bb;
      acc[1][2 * j] += s1 * a;
      acc[1][2 * j + 1] += s1 * bb;
    }
  }
#pragma unroll
  for (int b = 0; b < 2; ++b)
#pragma unroll
    for (int j = 0; j < 8; ++j) red[tr][b][tc * 8 + j] = acc[b][j];
  __syncthreads();
  if (tid < 128) {
    int b = tid >> 6, col = tid & 63;
    float s = 0.f;
#pragma unroll
    for (int r = 0; r < 32; ++r) s += red[r][b][col];
    modp[(kp * 2 + b) * 4608 + cb * 64 + col] = s;
  }
}

__global__ __launch_bounds__(256) void k_lnmod(const u16* __restrict__ x, const float* __restrict__ modp, int offS,
                                               u16* __restrict__ h, const u16* __restrict__ w0,
                                               const u16* __restrict__ w1) {
  const int tid = threadIdx.x;
  if (blockIdx.x >= 256) {
    const int t = (blockIdx.x - 256) * 256 + tid;
    u32x4 a = {0u, 0u, 0u, 0u};
    u32x4 v[12];
#pragma unroll
    for (int i = 0; i < 12; ++i) {
      int ci = t + i * 49152;
      const u16* p = (ci < 294912) ? (w0 + (size_t)ci * 8) : (w1 + (size_t)(ci - 294912) * 8);
      v[i] = *(const u32x4*)p;
    }
#pragma unroll
    for (int i = 0; i < 12; ++i) a ^= v[i];
    asm volatile("" ::"v"(a));
    return;
  }
  const int w = tid >> 6, l = tid & 63;
  const int row0 = blockIdx.x * 8 + w * 2;
  const int b = row0 >> 10;
  u32x2 v[2][3];
#pragma unroll
  for (int r = 0; r < 2; ++r) {
    const u16* xp = x + (size_t)(row0 + r) * 768 + l * 4;
#pragma unroll
    for (int i = 0; i < 3; ++i) v[r][i] = *(const u32x2*)(xp + i * 256);
  }
  f32x4 gg[3], tt[3];
#pragma unroll
  for (int i = 0; i < 3; ++i) {
    const float* m0p = modp + (0 * 2 + b) * 4608 + offS + l * 4 + i * 256;
    const float* m1p = modp + (1 * 2 + b) * 4608 + offS + l * 4 + i * 256;
    f32x4 s0 = *(const f32x4*)m0p, s1 = *(const f32x4*)m1p;
    f32x4 c0 = *(const f32x4*)(m0p + 768), c1 = *(const f32x4*)(m1p + 768);
#pragma unroll
    for (int j = 0; j < 4; ++j) {
      tt[i][j] = rbf(s0[j] + s1[j]);
      gg[i][j] = rbf(1.f + rbf(c0[j] + c1[j]));
    }
  }
#pragma unroll
  for (int r = 0; r < 2; ++r) {
    float f[12];
#pragma unroll
    for (int i = 0; i < 3; ++i) {
      f[4 * i] = lo_f(v[r][i][0]);
      f[4 * i + 1] = hi_f(v[r][i][0]);
      f[4 * i + 2] = lo_f(v[r][i][1]);
      f[4 * i + 3] = hi_f(v[r][i][1]);
    }
    float s = 0.f;
#pragma unroll
    for (int i = 0; i < 12; ++i) s += f[i];
#pragma unroll
    for (int o = 32; o > 0; o >>= 1) s += __shfl_xor(s, o);
    float mean = s * (1.f / 768.f);
    float q = 0.f;
#pragma unroll
    for (int i = 0; i < 12; ++i) {
      float d = f[i] - mean;
      q += d * d;
    }
#pragma unroll
    for (int o = 32; o > 0; o >>= 1) q += __shfl_xor(q, o);
    const float mb = rbf(mean);
    const float den = rbf(sqrtf(rbf(rbf(q * (1.f / 768.f)) + 1e-5f)));
    u16* op = h + (size_t)(row0 + r) * 768 + l * 4;
#pragma unroll
    for (int i = 0; i < 3; ++i) {
      float y[4];
#pragma unroll
      for (int j = 0; j < 4; ++j) y[j] = rbf(rbf(rbf(f[4 * i + j] - mb) / den) * gg[i][j]) + tt[i][j];
      u32x2 o;
      o[0] = pack2(y[0], y[1]);
      o[1] = pack2(y[2], y[3]);
      *(u32x2*)(op + i * 256) = o;
    }
  }
}

template <int BN, int NS, int EPI>
__global__ __launch_bounds__(768, 3) void k_gemm_big(const u16* __restrict__ A, const u16* __restrict__ W,
                                                     void* __restrict__ outv, u16* __restrict__ out2) {
  constexpr int NC = BN / 16;
  constexpr int LDW = (EPI == 0) ? 2304 : (EPI == 1 ? 3072 : 768);
  constexpr int LDA = (EPI == 2) ? 3072 : 768;
  constexpr int ASZ = 128 * 128;
  constexpr int BSZ = 8 * NC * 256;
  constexpr int STG = ASZ + BSZ;
  constexpr int SUBW = 2 * NC;
  constexpr int NBI = (SUBW + 3) / 4;
  constexpr int NLD = 4 + NBI;
  constexpr int NK = 768 / 64;
  __shared__ __attribute__((aligned(16))) char smem_raw[NS * STG];
  LDS_AS char* smem = (LDS_AS char*)smem_raw;
  const unsigned lds0 = (unsigned)(uintptr_t)smem;
  const int tid = threadIdx.x;
  const int l = tid & 63;
  const int w = uni(tid >> 6);
  const int bid = blockIdx.x;
  const int xcd = bid & 7, idx = bid >> 3;
  int tm, tn, ks = 0;
  if constexpr (EPI == 2) {
    tm = (xcd >> 1) * 4 + (idx & 3);
    tn = (idx >> 2) & 3;
    ks = (xcd & 1) * 2 + (idx >> 4);
  } else {
    tm = (xcd >> 1) * 4 + (idx & 3);
    tn = (xcd & 1) * 8 + (idx >> 2);
  }
  const int m0 = tm * 128, n0 = tn * BN;

  if (w >= 8) {
    const int lw = w - 8;
    const unsigned voffA = (unsigned)((l >> 3) * LDA * 2 + (((l & 7) ^ (l >> 3)) << 4));
    unsigned voffB[NBI];
#pragma unroll
    for (int i = 0; i < NBI; ++i) {
      int sub = SUBW * lw + 4 * i + (l >> 4);
      if (sub >= SUBW * (lw + 1)) sub = SUBW * (lw + 1) - 1;
      int kb = sub / NC, cc = sub % NC;
      int within = l & 15;
      int r = (within >> 1) ^ ((kb & 1) << 2);
      voffB[i] = (unsigned)(((kb * 8 + r) * LDW + cc * 16 + (within & 1) * 8) * 2);
    }
    const char* abase = (const char*)(A + (size_t)(m0 + 32 * lw) * LDA + ks * 768);
    const char* bbase = (const char*)(W + (size_t)ks * 768 * LDW + n0);
    auto issue = [&](int kt, int st) {
      const unsigned sa = lds0 + st * STG;
#pragma unroll
      for (int i = 0; i < 4; ++i)
        glds16(abase + (size_t)i * 8 * LDA * 2 + kt * 128, voffA, sa + (32 * lw + 8 * i) * 128);
      const char* sb = bbase + (size_t)kt * 64 * LDW * 2;
#pragma unroll
      for (int i = 0; i < NBI; ++i) {
        unsigned dst = sa + ASZ + (SUBW * lw + 4 * i) * 256;
        if (4 * i + 4 <= SUBW) {
          glds16(sb, voffB[i], dst);
        } else {
          if (l < (SUBW - 4 * i) * 16) glds16(sb, voffB[i], dst);
        }
      }
    };
#pragma unroll
    for (int s = 0; s < NS - 1; ++s) issue(s, s);
    int stn = NS - 1;
#pragma unroll 1
    for (int kt = 0; kt < NK; ++kt) {
      if (kt < NK - (NS - 2)) wait_vm<(NS - 2) * NLD>();
      else wait_vm<0>();
      bar();
      if (kt + NS - 1 < NK) issue(kt + NS - 1, stn);
      stn = (stn + 1 == NS) ? 0 : stn + 1;
    }
    bar();
    bar();
    return;
  }

  if constexpr (EPI == 1) {
    constexpr int NH = NC / 2;
    const int wm = w & 3, wn = w >> 2;
    const int g = l >> 4;
    const unsigned aoff = (unsigned)((32 * wm + (l & 15)) * 128 + ((g ^ (l & 7)) << 4));
    const int q = (l & 15) >> 2, p = l & 3;
    const unsigned boff_t0 = (unsigned)(ASZ + (g * NC + wn * NH) * 256 + ((4 * (g & 1) + q) * 32) + p * 8);
    const unsigned boff_t1 = (unsigned)(ASZ + (g * NC + wn * NH) * 256 + ((4 * (1 - (g & 1)) + q) * 32) + p * 8);
    f32x4 acc[2][NH];
#pragma unroll
    for (int i = 0; i < 2; ++i)
#pragma unroll
      for (int j = 0; j < NH; ++j) acc[i][j] = (f32x4){0.f, 0.f, 0.f, 0.f};
    int st = 0;
#pragma unroll 1
    for (int kt = 0; kt < NK; ++kt) {
      wait_lgkm0();
      bar();
      const LDS_AS char* sp = smem + st * STG;
#pragma unroll
      for (int ks2 = 0; ks2 < 2; ++ks2) {
        bf16x8 xb[2];
#pragma unroll
        for (int mf = 0; mf < 2; ++mf) xb[mf] = lds_read16(sp + (aoff ^ (ks2 * 64)) + mf * 2048);
#pragma unroll
        for (int cc = 0; cc < NH; ++cc) {
          bf16x4 w0 = lds_tr(sp + boff_t0 + ks2 * 4 * NC * 256 + cc * 256);
          bf16x4 w1 = lds_tr(sp + boff_t1 + ks2 * 4 * NC * 256 + cc * 256);
          bf16x8 wf = cat4(w0, w1);
          acc[0][cc] = mfma16(wf, xb[0], acc[0][cc]);
          acc[1][cc] = mfma16(wf, xb[1], acc[1][cc]);
        }
      }
      st = (st + 1 == NS) ? 0 : st + 1;
    }
    bar();
    bar();
    u16* out = (u16*)outv;
#pragma unroll
    for (int mf = 0; mf < 2; ++mf) {
      u16* op = out + (size_t)(m0 + 32 * wm + 16 * mf + (l & 15)) * 3072 + n0 + 16 * NH * wn;
      unsigned pk[NH][2];
#pragma unroll
      for (int cc = 0; cc < NH; ++cc) {
        f32x4 v = acc[mf][cc];
        const float c1 = -2.302208198144325f;
        const float c2 = -0.1029432397500235f;
        unsigned y01 = pack2(v[0], v[1]), y23 = pack2(v[2], v[3]);
        float y[4] = {lo_f(y01), hi_f(y01), lo_f(y23), hi_f(y23)};
#pragma unroll
        for (int j = 0; j < 4; ++j) {
          float e = __builtin_amdgcn_exp2f(y[j] * (c1 + c2 * y[j] * y[j]));
          v[j] = y[j] * __builtin_amdgcn_rcpf(1.f + e);
        }
        pk[cc][0] = pack2(v[0], v[1]);
        pk[cc][1] = pack2(v[2], v[3]);
      }
#pragma unroll
      for (int a = 0; a < NH / 2; ++a) {
        u32x4 o = pair_pack(pk[2 * a][0], pk[2 * a][1], pk[2 * a + 1][0], pk[2 * a + 1][1]);
        *(u32x4*)(op + 32 * a + (g & 1) * 16 + (g & 2) * 4) = o;
      }
    }
    return;
  }

  const int wm = w & 3, kh = w >> 2;
  const int g = l >> 4;
  const unsigned aoff = (unsigned)(((32 * wm + (l & 15)) * 128 + ((g ^ (l & 7)) << 4)) ^ (kh * 64));
  const int q = (l & 15) >> 2, p = l & 3;
  const unsigned boff_t0 = (unsigned)(ASZ + (4 * kh + g) * NC * 256 + ((4 * (g & 1) + q) * 32) + p * 8);
  const unsigned boff_t1 = (unsigned)(ASZ + (4 * kh + g) * NC * 256 + ((4 * (1 - (g & 1)) + q) * 32) + p * 8);

  f32x4 acc[2][NC];
#pragma unroll
  for (int i = 0; i < 2; ++i)
#pragma unroll
    for (int j = 0; j < NC; ++j) acc[i][j] = (f32x4){0.f, 0.f, 0.f, 0.f};

  int st = 0;
#pragma unroll 1
  for (int kt = 0; kt < NK; ++kt) {
    wait_lgkm0();
    bar();
    const LDS_AS char* sp = smem + st * STG;
    bf16x8 xb[2];
#pragma unroll
    for (int mf = 0; mf < 2; ++mf) xb[mf] = lds_read16(sp + aoff + mf * 2048);
#pragma unroll
    for (int cc = 0; cc < NC; ++cc) {
      bf16x4 w0 = lds_tr(sp + boff_t0 + cc * 256);
      bf16x4 w1 = lds_tr(sp + boff_t1 + cc * 256);
      bf16x8 wf = cat4(w0, w1);
      acc[0][cc] = mfma16(wf, xb[0], acc[0][cc]);
      acc[1][cc] = mfma16(wf, xb[1], acc[1][cc]);
    }
    st = (st + 1 == NS) ? 0 : st + 1;
  }

  wait_lgkm0();
  bar();
  {
    LDS_AS char* rp = smem + (wm * 2 + kh) * NC * 1024 + l * 16;
    if (kh) {
#pragma unroll
      for (int cc = 0; cc < NC; ++cc) {
        *(LDS_AS f32x4*)(rp + cc * 1024) = acc[0][cc];
        acc[0][cc] = acc[1][cc];
      }
    } else {
#pragma unroll
      for (int cc = 0; cc < NC; ++cc) *(LDS_AS f32x4*)(rp + cc * 1024) = acc[1][cc];
    }
  }
  wait_lgkm0();
  bar();
  const int row = m0 + 32 * wm + 16 * kh + (l & 15);
  const LDS_AS char* rp = smem + (wm * 2 + (1 - kh)) * NC * 1024 + l * 16;
  if constexpr (EPI == 2) {
    float* op = (float*)outv + ((size_t)ks * 2048 + row) * 768 + n0 + 4 * g;
#pragma unroll
    for (int cc = 0; cc < NC; ++cc) *(f32x4*)(op + 16 * cc) = acc[0][cc] + lds_read16f(rp + cc * 1024);
  } else {
    u16* out = (u16*)outv;
    unsigned pk[NC][2];
#pragma unroll
    for (int cc = 0; cc < NC; ++cc) {
      f32x4 v = acc[0][cc] + lds_read16f(rp + cc * 1024);
      if constexpr (EPI == 1) {
        const float c1 = -2.302208198144325f;
        const float c2 = -0.1029432397500235f;
        unsigned y01 = pack2(v[0], v[1]), y23 = pack2(v[2], v[3]);
        float y[4] = {lo_f(y01), hi_f(y01), lo_f(y23), hi_f(y23)};
#pragma unroll
        for (int j = 0; j < 4; ++j) {
          float e = __builtin_amdgcn_exp2f(y[j] * (c1 + c2 * y[j] * y[j]));
          v[j] = y[j] * __builtin_amdgcn_rcpf(1.f + e);
        }
      }
      pk[cc][0] = pack2(v[0], v[1]);
      pk[cc][1] = pack2(v[2], v[3]);
    }
    auto dst_ptr = [&](int n) -> u16* {
      if constexpr (EPI == 1) {
        return out + (size_t)row * 3072 + n;
      } else {
        if (n < 1536) return out + (size_t)row * 1536 + n;
        int nv = n - 1536;
        int hd = nv / 96;
        int dd = nv - hd * 96;
        return out2 + ((size_t)(((row >> 10) * 8 + hd) * 1024 + (row & 1023))) * 96 + dd;
      }
    };
#pragma unroll
    for (int a = 0; a < NC / 2; ++a) {
      u32x4 o = pair_pack(pk[2 * a][0], pk[2 * a][1], pk[2 * a + 1][0], pk[2 * a + 1][1]);
      *(u32x4*)dst_ptr(n0 + 32 * a + (g & 1) * 16 + (g & 2) * 4) = o;
    }
    if constexpr (NC & 1) {
      u32x2 o;
      o[0] = pk[NC - 1][0];
      o[1] = pk[NC - 1][1];
      *(u32x2*)dst_ptr(n0 + 16 * (NC - 1) + 4 * g) = o;
    }
  }
}

__global__ __launch_bounds__(768, 3) void k_scores(const u16* __restrict__ qk, u16* __restrict__ P,
                                                   f32x2* __restrict__ ml) {
  constexpr int NS = 4;
  constexpr int ASZ = 128 * 128;
  constexpr int STG = 2 * ASZ;
  constexpr int NK = 384 / 64;
  constexpr int NLD = 8;
  constexpr int LDQ = 1536;
  __shared__ __attribute__((aligned(16))) char smem_raw[NS * STG];
  LDS_AS char* smem = (LDS_AS char*)smem_raw;
  const unsigned lds0 = (unsigned)(uintptr_t)smem;
  const int tid = threadIdx.x, l = tid & 63;
  const int w = uni(tid >> 6);
  const int bid = blockIdx.x;
  const int xcd = bid & 7, idx = bid >> 3;
  const int bh = xcd >> 1;
  const int tq = (xcd & 1) * 4 + (idx & 3);
  const int tk = idx >> 2;
  const int b = bh >> 1, h = bh & 1;

  if (w >= 8) {
    const int lw = w - 8;
    const unsigned voff = (unsigned)((l >> 3) * LDQ * 2 + (((l & 7) ^ (l >> 3)) << 4));
    const char* qbase = (const char*)(qk + (size_t)(b * 1024 + tq * 128 + 32 * lw) * LDQ + h * 384);
    const char* kbase = (const char*)(qk + (size_t)(b * 1024 + tk * 128 + 32 * lw) * LDQ + 768 + h * 384);
    auto issue = [&](int kt, int st) {
      const unsigned sa = lds0 + st * STG;
#pragma unroll
      for (int i = 0; i < 4; ++i)
        glds16(qbase + (size_t)i * 8 * LDQ * 2 + kt * 128, voff, sa + (32 * lw + 8 * i) * 128);
#pragma unroll
      for (int i = 0; i < 4; ++i)
        glds16(kbase + (size_t)i * 8 * LDQ * 2 + kt * 128, voff, sa + ASZ + (32 * lw + 8 * i) * 128);
    };
#pragma unroll
    for (int s = 0; s < NS - 1; ++s) issue(s, s);
    int stn = NS - 1;
#pragma unroll 1
    for (int kt = 0; kt < NK; ++kt) {
      if (kt < NK - (NS - 2)) wait_vm<(NS - 2) * NLD>();
      else wait_vm<0>();
      bar();
      if (kt + NS - 1 < NK) issue(kt + NS - 1, stn);
      stn = (stn + 1 == NS) ? 0 : stn + 1;
    }
    bar();
    bar();
    return;
  }

  const int wm = w & 3, kh = w >> 2;
  const int g = l >> 4;
  const unsigned qoff = (unsigned)(((32 * wm + (l & 15)) * 128 + ((g ^ (l & 7)) << 4)) ^ (kh * 64));
  const unsigned koff = (unsigned)(ASZ + (((l & 15) * 128 + ((g ^ (l & 7)) << 4)) ^ (kh * 64)));
  f32x4 acc[2][8];
#pragma unroll
  for (int i = 0; i < 2; ++i)
#pragma unroll
    for (int j = 0; j < 8; ++j) acc[i][j] = (f32x4){0.f, 0.f, 0.f, 0.f};

  int st = 0;
#pragma unroll 1
  for (int kt = 0; kt < NK; ++kt) {
    wait_lgkm0();
    bar();
    const LDS_AS char* sp = smem + st * STG;
    bf16x8 qf[2];
#pragma unroll
    for (int mf = 0; mf < 2; ++mf) qf[mf] = lds_read16(sp + qoff + mf * 2048);
#pragma unroll
    for (int kf = 0; kf < 8; ++kf) {
      bf16x8 kfr = lds_read16(sp + koff + kf * 2048);
      acc[0][kf] = mfma16(kfr, qf[0], acc[0][kf]);
      acc[1][kf] = mfma16(kfr, qf[1], acc[1][kf]);
    }
    st = (st + 1 == NS) ? 0 : st + 1;
  }
  wait_lgkm0();
  bar();
  {
    LDS_AS char* rp = smem + (wm * 2 + kh) * 8 * 1024 + l * 16;
    if (kh) {
#pragma unroll
      for (int kf = 0; kf < 8; ++kf) {
        *(LDS_AS f32x4*)(rp + kf * 1024) = acc[0][kf];
        acc[0][kf] = acc[1][kf];
      }
    } else {
#pragma unroll
      for (int kf = 0; kf < 8; ++kf) *(LDS_AS f32x4*)(rp + kf * 1024) = acc[1][kf];
    }
  }
  wait_lgkm0();
  bar();
  f32x4 s[8];
  {
    const LDS_AS char* rp = smem + (wm * 2 + (1 - kh)) * 8 * 1024 + l * 16;
#pragma unroll
    for (int kf = 0; kf < 8; ++kf) {
      f32x4 v = acc[0][kf] + lds_read16f(rp + kf * 1024);
      unsigned a01 = pack2(v[0], v[1]), a23 = pack2(v[2], v[3]);
      a01 = pack2(lo_f(a01) * 0.051031036307982884f, hi_f(a01) * 0.051031036307982884f);
      a23 = pack2(lo_f(a23) * 0.051031036307982884f, hi_f(a23) * 0.051031036307982884f);
      s[kf] = (f32x4){lo_f(a01), hi_f(a01), lo_f(a23), hi_f(a23)};
    }
  }
  const float l2e = 1.4426950408889634f;
  float mx = -INFINITY;
#pragma unroll
  for (int kf = 0; kf < 8; ++kf)
#pragma unroll
    for (int j = 0; j < 4; ++j) mx = fmaxf(mx, s[kf][j]);
  mx = fmaxf(mx, __shfl_xor(mx, 16));
  mx = fmaxf(mx, __shfl_xor(mx, 32));
  const float mbl = mx * l2e;
  float sum = 0.f;
  unsigned pk[8][2];
#pragma unroll
  for (int kf = 0; kf < 8; ++kf) {
    float e[4];
#pragma unroll
    for (int j = 0; j < 4; ++j) {
      e[j] = __builtin_amdgcn_exp2f(s[kf][j] * l2e - mbl);
      sum += e[j];
    }
    pk[kf][0] = pack2(e[0], e[1]);
    pk[kf][1] = pack2(e[2], e[3]);
  }
  sum += __shfl_xor(sum, 16);
  sum += __shfl_xor(sum, 32);
  const int qrow = tq * 128 + 32 * wm + 16 * kh + (l & 15);
  u16* op = P + ((size_t)bh * 1024 + qrow) * 1024 + tk * 128;
#pragma unroll
  for (int a = 0; a < 4; ++a) {
    u32x4 o = pair_pack(pk[2 * a][0], pk[2 * a][1], pk[2 * a + 1][0], pk[2 * a + 1][1]);
    *(u32x4*)(op + 32 * a + (g & 1) * 16 + (g & 2) * 4) = o;
  }
  if (g == 0) {
    f32x2 s2;
    s2[0] = mbl;
    s2[1] = sum;
    ml[((size_t)bh * 1024 + qrow) * 8 + tk] = s2;
  }
}

template <int MODE, int NL>
__global__ __launch_bounds__(64 * (4 + NL), NL / 4 + 1) void k_gemm_small(const u16* __restrict__ Ain, const u16* __restrict__ Bin,
                                                       const float* __restrict__ modp,
                                                       const u16* __restrict__ resid,
                                                       const f32x2* __restrict__ ml, f32x2* __restrict__ stats,
                                                       u16* __restrict__ out) {
  constexpr int KTOT = (MODE == 0) ? 1024 : (MODE == 1 ? 768 : 3072);
  constexpr int KH = KTOT / 2;
  constexpr int NK = KH / 64;
  constexpr int LDA = KTOT;
  constexpr int LDB = (MODE == 0) ? 96 : 768;
  constexpr int NS = 4;
  constexpr int STG = 40960;
  constexpr int NLD = 40 / NL;
  constexpr int AI = 16 / NL, BI = 24 / NL;
  __shared__ __attribute__((aligned(16))) char smem_raw[NS * STG];
  LDS_AS char* smem = (LDS_AS char*)smem_raw;
  const unsigned lds0 = (unsigned)(uintptr_t)smem;
  const int tid = threadIdx.x, l = tid & 63;
  const int w = uni(tid >> 6);
  const int bid = blockIdx.x;
  const int xcd = bid & 7, idx = bid >> 3;
  const int g = l >> 4;

  int m0, n0, bh = 0;
  const u16* Ap;
  const u16* Bp;
  if constexpr (MODE == 0) {
    bh = xcd >> 1;
    const int tq = (xcd & 1) * 8 + (idx & 7);
    const int td = idx >> 3;
    m0 = tq * 64;
    n0 = td * 96;
    Ap = Ain + ((size_t)bh * 1024 + m0) * 1024;
    Bp = Bin + ((size_t)(bh * 4 + td) * 1024) * 96;
  } else {
    const int tm = (xcd >> 1) * 8 + (idx & 7);
    const int tn = (xcd & 1) * 4 + (idx >> 3);
    m0 = tm * 64;
    n0 = tn * 96;
    Ap = Ain + (size_t)m0 * LDA;
    Bp = Bin + n0;
  }

  if (w >= 4) {
    const int lw = w - 4;
    const int kq = lw & 1, part = lw >> 1;
    const unsigned voffA = (unsigned)((l >> 3) * LDA * 2 + (((l & 7) ^ (l >> 3)) << 4));
    unsigned voffB[BI];
#pragma unroll
    for (int i = 0; i < BI; ++i) {
      int sub = 4 * BI * part + 4 * i + (l >> 4);
      int kb = sub / 6, cc = sub % 6;
      int within = l & 15;
      int r = (within >> 1) ^ ((kb & 1) << 2);
      voffB[i] = (unsigned)(((kb * 8 + r) * LDB + cc * 16 + (within & 1) * 8) * 2);
    }
    const char* abase = (const char*)(Ap + (size_t)(8 * AI * part) * LDA + kq * KH);
    const char* bbase = (const char*)(Bp + (size_t)(kq * KH) * LDB);
    auto issue = [&](int kt, int st) {
      const unsigned sa = lds0 + st * STG;
#pragma unroll
      for (int i = 0; i < AI; ++i)
        glds16(abase + (size_t)i * 8 * LDA * 2 + kt * 128, voffA, sa + kq * 8192 + (8 * AI * part + 8 * i) * 128);
      const char* sb = bbase + (size_t)kt * 64 * LDB * 2;
#pragma unroll
      for (int i = 0; i < BI; ++i) glds16(sb, voffB[i], sa + 16384 + kq * 12288 + (4 * BI * part + 4 * i) * 256);
    };
#pragma unroll
    for (int s = 0; s < NS - 1; ++s) issue(s, s);
    int stn = NS - 1;
#pragma unroll 1
    for (int kt = 0; kt < NK; ++kt) {
      if (kt < NK - (NS - 2)) wait_vm<(NS - 2) * NLD>();
      else wait_vm<0>();
      bar();
      if (kt + NS - 1 < NK) issue(kt + NS - 1, stn);
      stn = (stn + 1 == NS) ? 0 : stn + 1;
    }
    bar();
    bar();
    return;
  }

  const int kq = w & 1, nh = w >> 1;
  float cf[4][4];
  f32x4 sv[4][4];
  if constexpr (MODE == 0) {
#pragma unroll
    for (int mf = 0; mf < 4; ++mf) {
      const f32x4* sp = (const f32x4*)(ml + ((size_t)bh * 1024 + m0 + 16 * mf + (l & 15)) * 8);
#pragma unroll
      for (int j = 0; j < 4; ++j) sv[mf][j] = sp[j];
    }
  }
  auto compute_cf = [&]() {
#pragma unroll
    for (int mf = 0; mf < 4; ++mf) {
      float mx = fmaxf(sv[mf][0][0], sv[mf][0][2]);
#pragma unroll
      for (int j = 1; j < 4; ++j) mx = fmaxf(mx, fmaxf(sv[mf][j][0], sv[mf][j][2]));
      float e[8];
      float lsum = 0.f;
#pragma unroll
      for (int j = 0; j < 4; ++j) {
        e[2 * j] = __builtin_amdgcn_exp2f(sv[mf][j][0] - mx);
        e[2 * j + 1] = __builtin_amdgcn_exp2f(sv[mf][j][2] - mx);
        lsum += e[2 * j] * sv[mf][j][1] + e[2 * j + 1] * sv[mf][j][3];
      }
      float inv = 1.f / lsum;
      if (kq) {
#pragma unroll
        for (int j = 0; j < 4; ++j) cf[mf][j] = e[4 + j] * inv;
      } else {
#pragma unroll
        for (int j = 0; j < 4; ++j) cf[mf][j] = e[j] * inv;
      }
    }
  };
  f32x4 gq[2][3][2];
  u32x2 xr[2][3];
  if constexpr (MODE != 0) {
    const int goff = (MODE == 1) ? 2 * 768 : 5 * 768;
    const int b = m0 >> 10;
    const float* mp0 = modp + (0 * 2 + b) * 4608 + goff + n0 + 48 * nh + 4 * g;
    const float* mp1 = modp + (1 * 2 + b) * 4608 + goff + n0 + 48 * nh + 4 * g;
#pragma unroll
    for (int cc = 0; cc < 3; ++cc) {
      gq[0][cc][0] = *(const f32x4*)(mp0 + 16 * cc);
      gq[0][cc][1] = *(const f32x4*)(mp1 + 16 * cc);
    }
#pragma unroll
    for (int mf = 0; mf < 2; ++mf) {
      const u16* rp = resid + (size_t)(m0 + 32 * kq + 16 * mf + (l & 15)) * 768 + n0 + 48 * nh + 4 * g;
#pragma unroll
      for (int cc = 0; cc < 3; ++cc) xr[mf][cc] = *(const u32x2*)(rp + 16 * cc);
    }
  }

  const unsigned aoff0 = (unsigned)(kq * 8192 + (l & 15) * 128 + ((g ^ (l & 7)) << 4));
  const int q = (l & 15) >> 2, p = l & 3;
  const unsigned boff_t0 = (unsigned)(16384 + kq * 12288 + (g * 6 + 3 * nh) * 256 + ((4 * (g & 1) + q) * 32) + p * 8);
  const unsigned boff_t1 =
      (unsigned)(16384 + kq * 12288 + (g * 6 + 3 * nh) * 256 + ((4 * (1 - (g & 1)) + q) * 32) + p * 8);

  f32x4 acc[4][3];
  f32x4 tot[4][3];
#pragma unroll
  for (int i = 0; i < 4; ++i)
#pragma unroll
    for (int j = 0; j < 3; ++j) {
      acc[i][j] = (f32x4){0.f, 0.f, 0.f, 0.f};
      tot[i][j] = (f32x4){0.f, 0.f, 0.f, 0.f};
    }

  int st = 0;
#pragma unroll 1
  for (int kt = 0; kt < NK; ++kt) {
    wait_lgkm0();
    bar();
    const LDS_AS char* sp = smem + st * STG;
#pragma unroll
    for (int ks = 0; ks < 2; ++ks) {
      bf16x8 af[4];
#pragma unroll
      for (int mf = 0; mf < 4; ++mf) af[mf] = lds_read16(sp + ((aoff0 ^ (ks * 64)) + mf * 2048));
#pragma unroll
      for (int cc = 0; cc < 3; ++cc) {
        bf16x4 w0 = lds_tr(sp + boff_t0 + ks * 6144 + cc * 256);
        bf16x4 w1 = lds_tr(sp + boff_t1 + ks * 6144 + cc * 256);
        bf16x8 wf = cat4(w0, w1);
#pragma unroll
        for (int mf = 0; mf < 4; ++mf) acc[mf][cc] = mfma16(wf, af[mf], acc[mf][cc]);
      }
    }
    if constexpr (MODE == 0) {
      if (kt & 1) {
        if (kt == 1) compute_cf();
#pragma unroll
        for (int mf = 0; mf < 4; ++mf) {
          const float c = cf[mf][0];
          cf[mf][0] = cf[mf][1];
          cf[mf][1] = cf[mf][2];
          cf[mf][2] = cf[mf][3];
#pragma unroll
          for (int cc = 0; cc < 3; ++cc) {
            tot[mf][cc] += acc[mf][cc] * c;
            acc[mf][cc] = (f32x4){0.f, 0.f, 0.f, 0.f};
          }
        }
      }
    }
    st = (st + 1 == NS) ? 0 : st + 1;
  }
  if constexpr (MODE == 0) {
#pragma unroll
    for (int mf = 0; mf < 4; ++mf)
#pragma unroll
      for (int cc = 0; cc < 3; ++cc) acc[mf][cc] = tot[mf][cc];
  }
  wait_lgkm0();
  bar();
  {
    LDS_AS char* rp = smem + (nh * 2 + kq) * 6144 + l * 16;
    if (kq) {
#pragma unroll
      for (int mf = 0; mf < 2; ++mf)
#pragma unroll
        for (int cc = 0; cc < 3; ++cc) {
          *(LDS_AS f32x4*)(rp + (mf * 3 + cc) * 1024) = acc[mf][cc];
          acc[mf][cc] = acc[2 + mf][cc];
        }
    } else {
#pragma unroll
      for (int mf = 0; mf < 2; ++mf)
#pragma unroll
        for (int cc = 0; cc < 3; ++cc) *(LDS_AS f32x4*)(rp + (mf * 3 + cc) * 1024) = acc[2 + mf][cc];
    }
  }
  wait_lgkm0();
  bar();
  f32x4 r[2][3];
  {
    const LDS_AS char* rp = smem + (nh * 2 + (1 - kq)) * 6144 + l * 16;
#pragma unroll
    for (int mf = 0; mf < 2; ++mf)
#pragma unroll
      for (int cc = 0; cc < 3; ++cc)
        r[mf][cc] = acc[mf][cc] + lds_read16f(rp + (mf * 3 + cc) * 1024);
  }
#pragma unroll
  for (int mf = 0; mf < 2; ++mf) {
    const int mrow = m0 + 32 * kq + 16 * mf + (l & 15);
    float v[3][4];
    u16* op;
    if constexpr (MODE == 0) {
      op = out + (size_t)((bh >> 1) * 1024 + mrow) * 768 + (bh & 1) * 384 + n0 + 48 * nh;
#pragma unroll
      for (int cc = 0; cc < 3; ++cc)
#pragma unroll
        for (int j = 0; j < 4; ++j) v[cc][j] = r[mf][cc][j];
    } else {
      op = out + (size_t)mrow * 768 + n0 + 48 * nh;
#pragma unroll
      for (int cc = 0; cc < 3; ++cc) {
        float xv[4] = {lo_f(xr[mf][cc][0]), hi_f(xr[mf][cc][0]), lo_f(xr[mf][cc][1]), hi_f(xr[mf][cc][1])};
#pragma unroll
        for (int j = 0; j < 4; ++j) {
          float gate = rbf(gq[0][cc][0][j] + gq[0][cc][1][j]);
          v[cc][j] = rbf(xv[j] + rbf(gate * rbf(r[mf][cc][j])));
        }
      }
    }
    {
      u32x4 o = pair_pack(pack2(v[0][0], v[0][1]), pack2(v[0][2], v[0][3]), pack2(v[1][0], v[1][1]),
                          pack2(v[1][2], v[1][3]));
      *(u32x4*)(op + (g & 1) * 16 + (g & 2) * 4) = o;
      u32x2 o2;
      o2[0] = pack2(v[2][0], v[2][1]);
      o2[1] = pack2(v[2][2], v[2][3]);
      *(u32x2*)(op + 32 + 4 * g) = o2;
    }
  }
}

#define SNL 4
extern "C" void kernel_launch(void* const* d_in, const int* in_sizes, int n_in, void* d_out, int out_size,
                              void* d_ws, size_t ws_size, hipStream_t stream) {
  const u16* x = (const u16*)d_in[0];
  const u16* c = (const u16*)d_in[1];
  const u16* wqkv = (const u16*)d_in[2];
  const u16* wo = (const u16*)d_in[3];
  const u16* wmod = (const u16*)d_in[4];
  const u16* wfc1 = (const u16*)d_in[5];
  const u16* wfc2 = (const u16*)d_in[6];
  u16* out = (u16*)d_out;

  char* ws = (char*)d_ws;
  size_t off = 0;
  auto alloc = [&](size_t bytes) {
    char* p = ws + off;
    off += (bytes + 255) & ~(size_t)255;
    return p;
  };
  float* modp = (float*)alloc(2 * 2 * 4608 * 4);
  u16* h = (u16*)alloc((size_t)Mrows * 768 * 2);
  u16* qk = (u16*)alloc((size_t)Mrows * 1536 * 2);
  u16* Vt = (u16*)alloc((size_t)Mrows * 768 * 2);
  u16* P = (u16*)alloc((size_t)4 * 1024 * 1024 * 2);
  f32x2* ml = (f32x2*)alloc((size_t)4 * 1024 * 8 * 8);
  u16* O = (u16*)alloc((size_t)Mrows * 768 * 2);
  u16* x1 = (u16*)alloc((size_t)Mrows * 768 * 2);
  f32x2* st2 = (f32x2*)alloc((size_t)Mrows * 16 * 8);
  u16* h2 = (u16*)alloc((size_t)Mrows * 768 * 2);
  u16* u = (u16*)alloc((size_t)Mrows * 3072 * 2);

  k0_mod<<<144 + 96, 256, 0, stream>>>(c, wmod, modp, wqkv, wo);
  k_lnmod<<<256 + 192, 256, 0, stream>>>(x, modp, 0, h, wfc1, wfc2);
  k_gemm_big<144, 4, 0><<<256, 768, 0, stream>>>(h, wqkv, qk, Vt);
  k_scores<<<256, 768, 0, stream>>>(qk, P, ml);
  k_gemm_small<0, SNL><<<256, 64 * (4 + SNL), 0, stream>>>(P, Vt, modp, nullptr, ml, nullptr, O);
  k_gemm_small<1, SNL><<<256, 64 * (4 + SNL), 0, stream>>>(O, wo, modp, x, nullptr, st2, x1);
  k_lnmod<<<256, 256, 0, stream>>>(x1, modp, 3 * 768, h2, nullptr, nullptr);
  k_gemm_big<192, 4, 1><<<256, 768, 0, stream>>>(h2, wfc1, u, nullptr);
  k_gemm_small<2, SNL><<<256, 64 * (4 + SNL), 0, stream>>>(u, wfc2, modp, x1, nullptr, nullptr, out);
}
```
